```python
import torch

def _softmax(x, axis=-1):
    return torch.softmax(x, dim=axis)

def _gelu(x):
    return torch.nn.functional.gelu(x, approximate='tanh')

def _silu(x):
    return torch.nn.functional.silu(x)

def _sigmoid(x):
    return torch.sigmoid(x)

def _layernorm(x, axis=-1, eps=1e-05):
    m = x.mean(dim=axis, keepdim=True)
    v = x.var(dim=axis, keepdim=True, unbiased=False)
    return (x - m) / torch.sqrt(v + eps)

def _scatter_add(tgt, idx, src, axis=0):
    return tgt.clone().index_add_(axis, idx, src)

def _segment_sum(src, seg, n):
    out = torch.zeros((n,) + src.shape[1:], dtype=src.dtype)
    return out.index_add_(0, seg, src)

def _pad(x, pw):
    flat = tuple((v for pair in reversed(pw) for v in pair))
    return torch.nn.functional.pad(x, flat)
kernel_name = 'parallel_sum__sliding_local_dilated__mixer_gmlp__prompt'

def setup_inputs():

    _w = lambda a: (lambda t: t.to(torch.bfloat16) if t.is_floating_point() else t)(torch.as_tensor(a))

    def _A_setup():
        B, S, D = (1, 2048, 512)
        return {'x': _w((lambda *s: torch.randn(*s, dtype=torch.bfloat16) * 0.05)(B, S, D)), 'wqkv': _w((lambda *s: torch.randn(*s, dtype=torch.bfloat16) * 0.1)(D, 3 * D)), 'wo': _w((lambda *s: torch.randn(*s, dtype=torch.bfloat16) * 0.05)(D, D))}
    _A_in = _A_setup()

    def _B_setup():
        B, S, D, F = (1, 2048, 512, 1792)
        Fh = F // 2
        return {'x': _w((lambda *s: torch.randn(*s, dtype=torch.bfloat16) * 0.05)(B, S, D)), 'w_in': _w((lambda *s: torch.randn(*s, dtype=torch.bfloat16) * 0.4)(D, 2 * Fh)), 'w_sp': _w(0.01 * (lambda *s: torch.randn(*s, dtype=torch.bfloat16) * 0.4)(S, S)), 'b_sp': _w(1.0 + 0.01 * (lambda *s: torch.randn(*s, dtype=torch.bfloat16) * 0.05)(S)), 'w_out': _w((lambda *s: torch.randn(*s, dtype=torch.bfloat16) * 0.05)(Fh, D))}
    _B_in = _B_setup()
    return {'A_x': _A_in['x'], 'A_wqkv': _A_in['wqkv'], 'A_wo': _A_in['wo'], 'B_x': _B_in['x'], 'B_w_in': _B_in['w_in'], 'B_w_sp': _B_in['w_sp'], 'B_b_sp': _B_in['b_sp'], 'B_w_out': _B_in['w_out']}

def reference(A_x, A_wqkv, A_wo, B_x, B_w_in, B_w_sp, B_b_sp, B_w_out):

    def _A_ref(x, wqkv, wo):
        B, S, D = x.shape
        H = 32
        Dh = D // H
        W, dil = (8, 2)
        qkv = (lambda x, *d: x.permute(*d))((x @ wqkv).reshape(B, S, 3, H, Dh), 2, 0, 3, 1, 4)
        q, k, v = (qkv[0], qkv[1], qkv[2])
        scale = Dh ** (-0.5)
        pos = torch.arange(S)
        off = torch.arange(W) * dil
        idx = pos[:, None] - off[None, :]
        mask = idx >= 0
        idx_c = torch.where(mask, idx, 0).reshape(-1)
        kg = k[:, :, idx_c, :].reshape(B, H, S, W, Dh)
        vg = v[:, :, idx_c, :].reshape(B, H, S, W, Dh)
        logits = torch.einsum('bhsd,bhswd->bhsw', q, kg) * scale
        logits = torch.where(mask[None, None, :, :], logits, -1000000000.0)
        attn = _softmax(logits, axis=-1)
        o = torch.einsum('bhsw,bhswd->bhsd', attn, vg)
        return (lambda x, *d: x.permute(*d))(o, 0, 2, 1, 3).reshape(B, S, D) @ wo

    def _B_ref(x, w_in, w_sp, b_sp, w_out):
        B, S, D = x.shape
        F = w_in.shape[-1]
        Fh = F // 2
        z = _gelu(torch.einsum('bsd,df->bsf', x, w_in))
        z1 = z[..., :Fh].clone()
        z2 = _layernorm(z[..., Fh:], axis=-1)
        g = torch.einsum('ts,bsf->btf', w_sp, z2) + b_sp[None, :, None]
        return torch.einsum('bsf,fd->bsd', z1 * g, w_out)
    x = A_x
    xn = _layernorm(x, axis=-1)
    return x + _A_ref(xn, A_wqkv, A_wo) + _B_ref(xn, B_w_in, B_w_sp, B_b_sp, B_w_out)
```

```cpp
#include <hip/hip_runtime.h>
#include <hip/hip_bf16.h>
typedef unsigned short u16;
typedef short bf16x8 __attribute__((ext_vector_type(8)));
typedef float f32x4 __attribute__((ext_vector_type(4)));
__device__ __forceinline__ float bf2f(u16 b){ return __builtin_bit_cast(float,(unsigned)b<<16); }
__device__ __forceinline__ u16 f2bf(float f){ __bf16 h=(__bf16)f; return __builtin_bit_cast(u16,h); }
constexpr int S=2048,D=512,F=1792,FH=896;
__global__ void k_ln(const u16* in,int ld,int c0,int n,u16* out){
  int r=blockIdx.x; __shared__ float red[8];
  float s=0; for(int i=threadIdx.x;i<n;i+=256) s+=bf2f(in[(size_t)r*ld+c0+i]);
  for(int o=32;o;o>>=1) s+=__shfl_xor(s,o);
  if((threadIdx.x&63)==0) red[threadIdx.x>>6]=s; __syncthreads();
  float m=(red[0]+red[1]+red[2]+red[3])/n; __syncthreads();
  float q=0; for(int i=threadIdx.x;i<n;i+=256){float d=bf2f(in[(size_t)r*ld+c0+i])-m; q+=d*d;}
  for(int o=32;o;o>>=1) q+=__shfl_xor(q,o);
  if((threadIdx.x&63)==0) red[4+(threadIdx.x>>6)]=q; __syncthreads();
  float rs=rsqrtf((red[4]+red[5]+red[6]+red[7])/n+1e-5f);
  for(int i=threadIdx.x;i<n;i+=256) out[(size_t)r*n+i]=f2bf((bf2f(in[(size_t)r*ld+c0+i])-m)*rs);
}
template<int MODE>
__global__ __launch_bounds__(256) void k_gemm(const u16* A,int lda,const u16* B,int ldb,int K,u16* C,int ldc,
    const u16* aux1,const u16* aux2,const float* accin,float* accout){
  __shared__ u16 sa[64][40]; __shared__ u16 sb[64][40];
  int bm=blockIdx.y*64,bn=blockIdx.x*64,t=threadIdx.x,w=t>>6,l=t&63,wr=w>>1,wc=w&1;
  f32x4 acc[2][2]={};
  for(int k0=0;k0<K;k0+=32){
    { int r=t>>2,c=(t&3)*8; *(uint4*)&sa[r][c]=*(const uint4*)&A[(size_t)(bm+r)*lda+k0+c]; }
    { int k=t>>3,c=(t&7)*8; uint4 v=*(const uint4*)&B[(size_t)(k0+k)*ldb+bn+c]; const u16* p=(const u16*)&v; for(int j=0;j<8;j++) sb[c+j][k]=p[j]; }
    __syncthreads();
    bf16x8 a[2],b[2];
    for(int i=0;i<2;i++){ a[i]=*(bf16x8*)&sa[wr*32+i*16+(l&15)][(l>>4)*8]; b[i]=*(bf16x8*)&sb[wc*32+i*16+(l&15)][(l>>4)*8]; }
    for(int i=0;i<2;i++)for(int j=0;j<2;j++) acc[i][j]=__builtin_amdgcn_mfma_f32_16x16x32_bf16(a[i],b[j],acc[i][j],0,0,0);
    __syncthreads();
  }
  for(int i=0;i<2;i++)for(int j=0;j<2;j++)for(int r=0;r<4;r++){
    int row=bm+wr*32+i*16+(l>>4)*4+r,col=bn+wc*32+j*16+(l&15); float v=acc[i][j][r];
    if(MODE==0) C[(size_t)row*ldc+col]=f2bf(v);
    if(MODE==1){ float u=0.7978845608f*(v+0.044715f*v*v*v); float g=v/(1.f+__expf(-2.f*u)); C[(size_t)row*ldc+col]=f2bf(g);}
    if(MODE==2){ float g=v+bf2f(aux2[row]); C[(size_t)row*ldc+col]=f2bf(g*bf2f(aux1[(size_t)row*F+col])); }
    if(MODE==3){ accout[(size_t)row*ldc+col]=v; }
    if(MODE==4){ C[(size_t)row*ldc+col]=f2bf(v+accin[(size_t)row*ldc+col]+bf2f(aux1[(size_t)row*ldc+col])); }
  }
}
__global__ void k_attn(const u16* qkv,u16* o){
  int i=blockIdx.x*256+threadIdx.x; int s=i>>5,h=i&31;
  float q[16]; for(int d=0;d<16;d++) q[d]=bf2f(qkv[(size_t)s*1536+h*16+d]);
  float lg[8]; float m=-1e30f;
  for(int w=0;w<8;w++){ int j=s-2*w; float a=0; if(j>=0){ for(int d=0;d<16;d++) a+=q[d]*bf2f(qkv[(size_t)j*1536+512+h*16+d]); a*=0.25f;} else a=-1e9f; lg[w]=a; m=fmaxf(m,a);}
  float sum=0; for(int w=0;w<8;w++){ lg[w]=__expf(lg[w]-m); sum+=lg[w]; }
  float acc[16]={};
  for(int w=0;w<8;w++){ int j=s-2*w; if(j<0) j=0; for(int d=0;d<16;d++) acc[d]+=lg[w]*bf2f(qkv[(size_t)j*1536+1024+h*16+d]); }
  for(int d=0;d<16;d++) o[(size_t)s*512+h*16+d]=f2bf(acc[d]/sum);
}
extern "C" void kernel_launch(void* const* d_in, const int* in_sizes, int n_in,
                              void* d_out, int out_size, void* d_ws, size_t ws_size,
                              hipStream_t stream) {
  const u16* x=(const u16*)d_in[0]; const u16* wqkv=(const u16*)d_in[1]; const u16* wo=(const u16*)d_in[2];
  const u16* win=(const u16*)d_in[4]; const u16* wsp=(const u16*)d_in[5]; const u16* bsp=(const u16*)d_in[6]; const u16* wout=(const u16*)d_in[7];
  char* ws=(char*)d_ws;
  u16* xn=(u16*)ws; u16* qkv=(u16*)(ws+(4<<20)); u16* z=(u16*)(ws+(12<<20)); u16* z2n=(u16*)(ws+(20<<20)); u16* o=(u16*)(ws+(24<<20)); u16* h=(u16*)(ws+(28<<20)); float* ao=(float*)(ws+(32<<20));
  k_ln<<<S,256,0,stream>>>(x,D,0,D,xn);
  k_gemm<0><<<dim3(1536/64,S/64),256,0,stream>>>(xn,D,wqkv,1536,D,qkv,1536,nullptr,nullptr,nullptr,nullptr);
  k_gemm<1><<<dim3(F/64,S/64),256,0,stream>>>(xn,D,win,F,D,z,F,nullptr,nullptr,nullptr,nullptr);
  k_ln<<<S,256,0,stream>>>(z,F,FH,FH,z2n);
  k_attn<<<S*32/256,256,0,stream>>>(qkv,o);
  k_gemm<2><<<dim3(FH/64,S/64),256,0,stream>>>(wsp,S,z2n,FH,S,h,FH,z,bsp,nullptr,nullptr);
  k_gemm<3><<<dim3(D/64,S/64),256,0,stream>>>(o,D,wo,D,D,nullptr,D,nullptr,nullptr,nullptr,ao);
  k_gemm<4><<<dim3(D/64,S/64),256,0,stream>>>(h,FH,wout,D,FH,(u16*)d_out,D,x,nullptr,ao,nullptr);
}
```

```cpp
#include <hip/hip_runtime.h>
#include <stdint.h>

namespace mk {
constexpr unsigned WS_Z2N = 626688, WS_Z1 = 4296704, WS_H = 7966720, WS_O = 11636736, WS_END = 13733888, LDS_BYTES = 163840;
#define ST(i)
#define ST_BEGIN
#define ST_END

__global__ __launch_bounds__(256, 1) void fused_kernel(const uint16_t* px, const uint16_t* pwqkv, const uint16_t* pwo, const uint16_t* pwin, const uint16_t* pwsp,
                                                       const uint16_t* pbsp, const uint16_t* pwout, uint16_t* pout, unsigned char* pws) {
  unsigned tid = threadIdx.x;
  auto ka = __builtin_amdgcn_kernarg_segment_ptr();
  asm volatile(ST_BEGIN
    "s_load_dwordx2 s[4:5], s[0:1], 0\n"
    "s_load_dwordx2 s[8:9], s[0:1], 64\n"
    "s_load_dwordx2 s[12:13], s[0:1], 8\n"
    "s_load_dwordx2 s[16:17], s[0:1], 24\n"
    "s_load_dwordx2 s[20:21], s[0:1], 32\n"
    "s_load_dwordx2 s[24:25], s[0:1], 16\n"
    "s_load_dwordx2 s[28:29], s[0:1], 48\n"
    "s_load_dwordx2 s[34:35], s[0:1], 40\n"
    "s_load_dwordx2 s[36:37], s[0:1], 56\n"
    "s_getpc_b64 s[40:41]\n"
    "v_readfirstlane_b32 s45, v0\n"
    "v_lshlrev_b32_e64 v1, 4, v0\n"
    "s_and_b32 s40, s40, 0xffffff00\n"
    "s_mov_b32 s42, 59136\n"
    "s_mov_b32 s43, 0x20000\n"
    "s_lshr_b32 s45, s45, 6\n"
    "s_lshr_b32 s67, s2, 3\n"
    "s_lshl_b32 s47, s45, 10\n"
    "s_lshl_b32 s67, s67, 12\n"
    "s_add_u32 m0, s47, 159744\n"
    "v_and_b32_e64 v0, 63, v0\n"
    "buffer_load_dwordx4 v1, s[40:43], s67 offen lds\n"
    "v_lshlrev_b32 v1, 4, v0\n"
    "v_lshrrev_b32 v16, 3, v0\n"
    "v_lshrrev_b32 v17, 4, v0\n"
    "v_and_b32 v18, 7, v0\n"
    "v_xor_b32_e64 v19, v18, v17\n"
    "v_bitop3_b32 v20, v16, v18, 6 bitop3:0x6c\n"
    "v_lshlrev_b32 v19, 4, v19\n"
    "v_lshlrev_b32 v20, 4, v20\n"
    "s_bfe_u32 s68, s2, 0x20001\n"
    "s_and_b32 s69, s2, 1\n"
    "s_lshr_b32 s59, s2, 6\n"
    "s_lshl2_add_u32 s59, s68, s59\n"
    "s_bfe_u32 s60, s2, 0x20004\n"
    "s_lshl2_add_u32 s60, s69, s60\n"
    "s_lshl_b32 s48, s45, 11\n"
    "s_lshl_b32 s49, s45, 12\n"
    "s_lshl_b32 s50, s45, 13\n"
    "v_lshl_add_u32 v252, v16, 11, v19\n"
    "s_movk_i32 s70, 0xe00\n"
    "s_movk_i32 s71, 0xc00\n"
    "v_mad_u32_u24 v250, v16, s70, v20\n"
    "v_mad_u32_u24 v248, v16, s71, v20\n"
    "v_xor_b32 v253, 64, v252\n"
    "v_xor_b32 v251, 64, v250\n"
    "v_xor_b32 v249, 64, v248\n"
    "v_add_u32 v253, 0x4000, v253\n"
    "v_add_u32 v251, 0x7000, v251\n"
    "v_add_u32 v249, 0x6000, v249\n"
    "s_lshl_b32 s51, s59, 17\n"
    "s_lshl_b32 s70, s45, 15\n"
    "s_add_u32 s51, s51, s70\n"
    "s_nop 0\n"
    "s_mov_b32 s6, 2097152\n"
    "s_mov_b32 s7, 0x20000\n"
    "s_mov_b32 s10, 13733888\n"
    "s_mov_b32 s11, 0x20000\n"
    "s_mov_b32 s14, 1572864\n"
    "s_mov_b32 s15, 0x20000\n"
    "s_mov_b32 s18, 1835008\n"
    "s_mov_b32 s19, 0x20000\n"
    "s_lshr_b32 s75, s59, 1\n"
    "s_sub_u32 s3, 8, s75\n"
    "s_lshl_b32 s76, s75, 7\n"
    "s_add_u32 s51, s51, s76\n"
    "s_lshl2_add_u32 s75, s75, s45\n"
    "s_mul_i32 s73, s75, 57344\n"
    "s_lshl_b32 s74, s60, 8\n"
    "s_add_u32 s54, s73, s74\n"
    "s_mul_i32 s73, s75, 49152\n"
    "s_lshl_b32 s74, s60, 7\n"
    "s_add_u32 s73, s73, s74\n"
    "s_add_u32 s74, s54, 1792\n"
    "s_bitcmp0_b32 s2, 3\n"
    "s_cselect_b32 s53, s73, s74\n"
    "s_cselect_b32 s75, 896, 0\n"
    "s_cselect_b32 s76, 2048, 0\n"
    "s_cselect_b64 vcc, -1, 0\n"
    "s_add_u32 s75, s75, s53\n"
    "s_add_u32 s76, s76, s48\n"
    "s_add_u32 s75, s75, 128\n"
    "v_cndmask_b32 v21, v250, v248, vcc\n"
    "v_cndmask_b32 v22, v251, v249, vcc\n"
    "s_mov_b32 s99, 0x20000\n"
    "s_waitcnt lgkmcnt(0)\n"
    "s_bitcmp0_b32 s2, 3\n"
    "s_cselect_b32 s96, s12, s16\n"
    "s_cselect_b32 s97, s13, s17\n"
    "s_cselect_b32 s98, s14, s18\n"
    "buffer_load_dword v13, off, s[8:11], 0 sc1\n"
    "s_and_b32 s67, s2, 0x39\n"
    "s_cmp_eq_u32 s67, 0x39\n"
    "s_cbranch_scc1 LS_3_%=\n"
    "v_lshl_add_u32 v2, v16, 13, v19\n"
    "v_lshl_add_u32 v8, v16, 10, v20\n"
    "v_and_b32 v23, 15, v0\n"
    "v_xor_b32 v3, 64, v2\n"
    "s_add_u32 m0, s49, 6144\n"
    "v_xor_b32_e64 v9, 64, v8\n"
    "buffer_load_dwordx4 v252, s[4:7], s51 offen offset:0 lds\n"
    "v_add_u32 v3, 0x10000, v3\n"
    "v_add_u32 v9, 0x2000, v9\n"
    "v_lshrrev_b32 v24, 1, v23\n"
    "v_xor_b32 v24, v24, v17\n"
    "v_lshlrev_b32_e64 v24, 4, v24\n"
    "v_lshl_add_u32 v11, v23, 7, v24\n"
    "s_add_u32 m0, s49, 7168\n"
    "v_add_u32_e64 v4, s49, v11\n"
    "buffer_load_dwordx4 v253, s[4:7], s51 offen offset:0 lds\n"
    "v_xor_b32_e64 v5, 64, v4\n"
    "v_add_u32 v6, 0x10000, v4\n"
    "v_add_u32 v7, 0x10000, v5\n"
    "v_bfe_u32 v24, v0, 2, 2\n"
    "v_lshl_add_u32 v25, v17, 3, v24\n"
    "v_and_b32 v26, 3, v0\n"
    "v_lshlrev_b32 v26, 3, v26\n"
    "s_add_u32 m0, s49, 7168\n"
    "v_lshl_add_u32 v25, v25, 7, v26\n"
    "buffer_load_dwordx4 v252, s[4:7], s51 offen offset:1024 lds\n"
    "v_lshrrev_b32 v26, 1, v24\n"
    "v_and_b32 v27, 1, v17\n"
    "v_lshl_or_b32 v26, v27, 1, v26\n"
    "v_lshl_add_u32 v10, v26, 5, v25\n"
    "v_lshlrev_b32 v12, 3, v0\n"
    "v_lshlrev_b32 v14, 2, v23\n"
    "s_add_u32 m0, s49, 8192\n"
    "v_lshlrev_b32_e64 v247, 4, v17\n"
    "buffer_load_dwordx4 v253, s[4:7], s51 offen offset:1024 lds\n"
    "s_lshr_b32 s61, s2, 5\n"
    "s_lshl3_add_u32 s61, s68, s61\n"
    "s_bfe_u32 s62, s2, 0x20003\n"
    "s_lshl2_add_u32 s62, s69, s62\n"
    "s_mov_b32 s22, 8388608\n"
    "s_mov_b32 s23, 0x20000\n"
    "s_add_u32 m0, s76, 22528\n"
    "s_add_u32 s70, s45, 0\n"
    "buffer_load_dwordx4 v21, s[96:99], s53 offen lds\n"
    "s_and_b32 s71, s70, 1\n"
    "s_and_b32 s70, s70, 2\n"
    "s_lshl4_add_u32 s70, s70, s71\n"
    "s_lshl_b32 s71, s61, 6\n"
    "s_add_u32 s70, s70, s71\n"
    "s_lshl_b32 s70, s70, 12\n"
    "s_add_u32 s55, s70, s47\n"
    "s_add_u32 m0, s76, 23552\n"
    "s_add_u32 s70, s45, 1\n"
    "buffer_load_dwordx4 v22, s[96:99], s53 offen lds\n"
    "s_and_b32 s71, s70, 1\n"
    "s_and_b32 s70, s70, 2\n"
    "s_lshl4_add_u32 s70, s70, s71\n"
    "s_lshl_b32 s71, s61, 6\n"
    "s_add_u32 s70, s70, s71\n"
    "s_lshl_b32 s70, s70, 12\n"
    "s_add_u32 m0, s76, 30720\n"
    "s_add_u32 s56, s70, s47\n"
    "buffer_load_dwordx4 v21, s[96:99], s75 offen lds\n"
    "s_add_u32 s70, s45, 2\n"
    "s_and_b32 s71, s70, 1\n"
    "s_and_b32 s70, s70, 2\n"
    "s_lshl4_add_u32 s70, s70, s71\n"
    "s_lshl_b32 s71, s61, 6\n"
    "s_add_u32 s70, s70, s71\n"
    "s_add_u32 m0, s76, 31744\n"
    "s_lshl_b32 s70, s70, 12\n"
    "buffer_load_dwordx4 v22, s[96:99], s75 offen lds\n"
    "s_add_u32 s57, s70, s47\n"
    "s_add_u32 s70, s45, 3\n"
    "s_and_b32 s71, s70, 1\n"
    "s_and_b32 s70, s70, 2\n"
    "s_lshl4_add_u32 s70, s70, s71\n"
    "s_lshl_b32 s71, s61, 6\n"
    "s_add_u32 s70, s70, s71\n"
    "s_lshl_b32 s70, s70, 12\n"
    "s_add_u32 s58, s70, s47\n"
    ST(12)
    "s_bitcmp1_b32 s2, 3\n"
    "s_cbranch_scc1 LZ_2_%=\n"
    "s_lshl_b32 s67, s59, 7\n"
    "s_sub_u32 s73, s67, 16\n"
    "s_max_i32 s73, s73, 0\n"
    "s_and_b32 s70, s45, 1\n"
    "s_add_u32 s52, s73, s70\n"
    "s_lshl_b32 s52, s52, 10\n"
    "s_and_b32 s71, s51, 0x380\n"
    "s_add_u32 s52, s52, s71\n"
    "s_lshl_b32 s70, s70, 6\n"
    "v_xor_b32 v254, s70, v252\n"
    "s_add_u32 s54, s54, 128\n"
    "s_cmp_lt_u32 s60, 7\n"
    "s_cselect_b32 s54, s54, 2147483648\n"
    "s_lshl2_add_u32 s72, s60, s45\n"
    "s_lshl2_add_u32 s72, s72, s67\n"
    "s_lshl_b32 s72, s72, 10\n"
    "v_xor_b32_e64 v71, 64, v11\n"
    "buffer_load_dwordx4 v[84:87], v1, s[4:7], s72 offen offset:0\n"
    "v_add_u32 v72, 0x10000, v11\n"
    "buffer_load_dwordx4 v[88:91], v1, s[4:7], s72 offen offset:1024\n"
    "v_add_u32 v73, 0x10000, v71\n"
    "buffer_load_dwordx4 v[92:95], v1, s[4:7], s72 offen offset:2048\n"
    "s_lshl_b32 s70, s45, 5\n"
    "buffer_load_dwordx4 v[96:99], v1, s[4:7], s72 offen offset:3072\n"
    "v_xor_b32 v74, s70, v10\n"
    "s_add_u32 m0, s48, 38912\n"
    "v_xor_b32_e64 v75, 64, v74\n"
    "buffer_load_dwordx4 v248, s[12:15], s53 offen offset:2048 lds\n"
    "v_add_u32 v76, 0x10000, v74\n"
    "s_add_u32 m0, s48, 39936\n"
    "v_add_u32 v77, 0x10000, v75\n"
    "buffer_load_dwordx4 v249, s[12:15], s53 offen offset:2048 lds\n"
    "s_add_u32 s53, s53, 196608\n"
    "v_add_u32 v78, 0x20000, v74\n"
    "s_cmp_gt_u32 s47, 1024\n"
    "s_cbranch_scc1 Lnh_5_%=\n"
    "s_add_u32 m0, s47, 22528\n"
    "s_nop 0\n"
    "buffer_load_dwordx4 v254, s[4:7], s52 offen offset:0 lds\n"
    "Lnh_5_%=:\n"
    "v_add_u32 v79, 0x20000, v75\n"
    "s_add_u32 m0, s48, 49152\n"
    "s_lshr_b32 s74, s72, 6\n"
    "buffer_load_dwordx4 v250, s[16:19], s54 offen offset:0 lds\n"
    "s_add_u32 s74, s74, 331776\n"
    "s_add_u32 m0, s48, 50176\n"
    "s_nop 0\n"
    "buffer_load_dwordx4 v251, s[16:19], s54 offen offset:0 lds\n"
    "s_add_u32 s54, s54, 229376\n"
    "s_cmp_eq_u32 s3, 1\n"
    "s_cselect_b32 s63, 1024, 0\n"
    "s_sub_u32 s51, s51, s63\n"
    "s_sub_u32 s52, s52, s63\n"
    "s_mul_i32 s66, s63, 1536\n"
    "s_sub_u32 s53, s53, s66\n"
    "s_mul_i32 s66, s63, 1792\n"
    "s_sub_u32 s54, s54, s66\n"
    "s_add_u32 m0, s49, 57216\n"
    "s_nop 0\n"
    "buffer_load_dwordx4 v252, s[4:7], s51 offen offset:128 lds\n"
    "s_add_u32 m0, s49, 58240\n"
    "s_nop 0\n"
    "buffer_load_dwordx4 v253, s[4:7], s51 offen offset:128 lds\n"
    "s_add_u32 m0, s49, 58240\n"
    "s_nop 0\n"
    "buffer_load_dwordx4 v252, s[4:7], s51 offen offset:1152 lds\n"
    "s_add_u32 m0, s49, 59264\n"
    "s_nop 0\n"
    "buffer_load_dwordx4 v253, s[4:7], s51 offen offset:1152 lds\n"
    "s_cmp_gt_u32 s47, 1024\n"
    "s_cbranch_scc1 Lnh_6_%=\n"
    "s_add_u32 m0, s47, 73600\n"
    "s_nop 0\n"
    "buffer_load_dwordx4 v254, s[4:7], s52 offen offset:128 lds\n"
    "Lnh_6_%=:\n"
    "s_waitcnt vmcnt(20)\n"
    "v_add_u32 v13, 1, v13\n"
    "s_nop 0\n"
    "v_readfirstlane_b32 s46, v13\n"
    "s_waitcnt vmcnt(8)\n"
    "v_lshlrev_b32_e64 v64, 16, v84\n"
    "v_and_b32 v65, 0xffff0000, v84\n"
    "v_add_f32 v236, v64, v65\n"
    "v_mul_f32 v240, v64, v64\n"
    "v_fmac_f32 v240, v65, v65\n"
    "v_lshlrev_b32 v64, 16, v85\n"
    "v_and_b32 v65, 0xffff0000, v85\n"
    "v_add_f32_e64 v236, v236, v64\n"
    "s_add_u32 m0, s48, 100352\n"
    "v_fmac_f32 v240, v64, v64\n"
    "buffer_load_dwordx4 v250, s[16:19], s54 offen offset:0 lds\n"
    "v_add_f32 v236, v236, v65\n"
    "v_fmac_f32 v240, v65, v65\n"
    "v_lshlrev_b32 v64, 16, v86\n"
    "v_and_b32 v65, 0xffff0000, v86\n"
    "v_add_f32 v236, v236, v64\n"
    "v_fmac_f32 v240, v64, v64\n"
    "v_add_f32 v236, v236, v65\n"
    "v_fmac_f32 v240, v65, v65\n"
    "v_lshlrev_b32_e64 v64, 16, v87\n"
    "v_and_b32 v65, 0xffff0000, v87\n"
    "v_add_f32 v236, v236, v64\n"
    "v_fmac_f32 v240, v64, v64\n"
    "v_add_f32 v236, v236, v65\n"
    "v_fmac_f32 v240, v65, v65\n"
    "v_lshlrev_b32_e64 v64, 16, v88\n"
    "s_add_u32 m0, s48, 101376\n"
    "v_and_b32 v65, 0xffff0000, v88\n"
    "buffer_load_dwordx4 v251, s[16:19], s54 offen offset:0 lds\n"
    "s_add_u32 s54, s54, 229376\n"
    "v_add_f32 v237, v64, v65\n"
    "v_mul_f32 v241, v64, v64\n"
    "v_fmac_f32 v241, v65, v65\n"
    "v_lshlrev_b32 v64, 16, v89\n"
    "v_and_b32 v65, 0xffff0000, v89\n"
    "v_add_f32 v237, v237, v64\n"
    "v_fmac_f32 v241, v64, v64\n"
    "v_add_f32 v237, v237, v65\n"
    "v_fmac_f32 v241, v65, v65\n"
    "v_lshlrev_b32_e64 v64, 16, v90\n"
    "v_and_b32 v65, 0xffff0000, v90\n"
    "v_add_f32 v237, v237, v64\n"
    "v_fmac_f32 v241, v64, v64\n"
    "v_add_f32 v237, v237, v65\n"
    "v_fmac_f32 v241, v65, v65\n"
    "s_add_u32 m0, s48, 75776\n"
    "v_lshlrev_b32_e64 v64, 16, v91\n"
    "buffer_load_dwordx4 v248, s[12:15], s53 offen offset:0 lds\n"
    "v_and_b32 v65, 0xffff0000, v91\n"
    "v_add_f32 v237, v237, v64\n"
    "v_fmac_f32 v241, v64, v64\n"
    "v_add_f32 v237, v237, v65\n"
    "v_fmac_f32 v241, v65, v65\n"
    "v_lshlrev_b32_e64 v64, 16, v92\n"
    "v_and_b32 v65, 0xffff0000, v92\n"
    "v_add_f32 v238, v64, v65\n"
    "v_mul_f32 v242, v64, v64\n"
    "v_fmac_f32 v242, v65, v65\n"
    "v_lshlrev_b32 v64, 16, v93\n"
    "v_and_b32 v65, 0xffff0000, v93\n"
    "v_add_f32 v238, v238, v64\n"
    "v_fmac_f32 v242, v64, v64\n"
    "s_add_u32 m0, s48, 76800\n"
    "v_add_f32_e64 v238, v238, v65\n"
    "buffer_load_dwordx4 v249, s[12:15], s53 offen offset:0 lds\n"
    "v_fmac_f32 v242, v65, v65\n"
    "v_lshlrev_b32 v64, 16, v94\n"
    "v_and_b32 v65, 0xffff0000, v94\n"
    "v_add_f32 v238, v238, v64\n"
    "v_fmac_f32 v242, v64, v64\n"
    "v_add_f32 v238, v238, v65\n"
    "v_fmac_f32 v242, v65, v65\n"
    "v_lshlrev_b32_e64 v64, 16, v95\n"
    "v_and_b32 v65, 0xffff0000, v95\n"
    "v_add_f32 v238, v238, v64\n"
    "v_fmac_f32 v242, v64, v64\n"
    "v_add_f32 v238, v238, v65\n"
    "v_fmac_f32 v242, v65, v65\n"
    "v_lshlrev_b32_e64 v64, 16, v96\n"
    "v_and_b32 v65, 0xffff0000, v96\n"
    "s_add_u32 m0, s48, 82944\n"
    "v_add_f32_e64 v239, v64, v65\n"
    "buffer_load_dwordx4 v248, s[12:15], s53 offen offset:1024 lds\n"
    "v_mul_f32 v243, v64, v64\n"
    "v_fmac_f32 v243, v65, v65\n"
    "v_lshlrev_b32_e64 v64, 16, v97\n"
    "v_and_b32 v65, 0xffff0000, v97\n"
    "v_add_f32 v239, v239, v64\n"
    "v_fmac_f32 v243, v64, v64\n"
    "v_add_f32 v239, v239, v65\n"
    "v_fmac_f32 v243, v65, v65\n"
    "v_lshlrev_b32_e64 v64, 16, v98\n"
    "v_and_b32 v65, 0xffff0000, v98\n"
    "v_add_f32 v239, v239, v64\n"
    "v_fmac_f32 v243, v64, v64\n"
    "v_add_f32 v239, v239, v65\n"
    "v_fmac_f32 v243, v65, v65\n"
    "v_lshlrev_b32_e64 v64, 16, v99\n"
    "s_add_u32 m0, s48, 83968\n"
    "v_and_b32 v65, 0xffff0000, v99\n"
    "buffer_load_dwordx4 v249, s[12:15], s53 offen offset:1024 lds\n"
    "v_add_f32 v239, v239, v64\n"
    "v_fmac_f32 v243, v64, v64\n"
    "v_add_f32 v239, v239, v65\n"
    "v_fmac_f32 v243, v65, v65\n"
    "v_add_f32_dpp v66, v236, v236 row_ror:8 row_mask:0xf bank_mask:0x3\n"
    "v_add_f32_dpp v67, v237, v237 row_ror:8 row_mask:0xf bank_mask:0x3\n"
    "v_add_f32_dpp v68, v238, v238 row_ror:8 row_mask:0xf bank_mask:0x3\n"
    "v_add_f32_dpp v69, v239, v239 row_ror:8 row_mask:0xf bank_mask:0x3\n"
    "v_add_f32_dpp v66, v240, v240 row_ror:8 row_mask:0xf bank_mask:0xc\n"
    "v_add_f32_dpp v67, v241, v241 row_ror:8 row_mask:0xf bank_mask:0xc\n"
    "v_add_f32_dpp v68, v242, v242 row_ror:8 row_mask:0xf bank_mask:0xc\n"
    "v_add_f32_dpp v69, v243, v243 row_ror:8 row_mask:0xf bank_mask:0xc\n"
    "v_add_f32_dpp v64, v66, v66 row_half_mirror row_mask:0xf bank_mask:0x5\n"
    "v_add_f32_dpp v65, v67, v67 row_half_mirror row_mask:0xf bank_mask:0x5\n"
    "s_add_u32 m0, s48, 90112\n"
    "v_bfe_u32 v70, v0, 2, 1\n"
    "buffer_load_dwordx4 v248, s[12:15], s53 offen offset:2048 lds\n"
    "v_add_f32_dpp v64, v68, v68 row_half_mirror row_mask:0xf bank_mask:0xa\n"
    "v_add_f32_dpp v65, v69, v69 row_half_mirror row_mask:0xf bank_mask:0xa\n"
    "v_lshrrev_b32_e64 v236, 5, v0\n"
    "v_add_f32_dpp v64, v64, v64 quad_perm:[1,0,3,2] row_mask:0xf bank_mask:0xf\n"
    "v_add_f32_dpp v65, v65, v65 quad_perm:[1,0,3,2] row_mask:0xf bank_mask:0xf\n"
    "v_lshl_add_u32 v70, v70, 1, v236\n"
    "v_add_f32_dpp v64, v64, v64 quad_perm:[2,3,0,1] row_mask:0xf bank_mask:0xf\n"
    "v_add_f32_dpp v65, v65, v65 quad_perm:[2,3,0,1] row_mask:0xf bank_mask:0xf\n"
    "v_lshlrev_b32 v70, 4, v70\n"
    "v_mov_b32 v237, s46\n"
    "v_permlane32_swap_b32 v64, v65\n"
    "v_add_f32 v64, v64, v65\n"
    "v_mov_b32 v65, v64\n"
    "v_mov_b32 v239, s46\n"
    "s_nop 0\n"
    "s_add_u32 m0, s48, 91136\n"
    "v_permlane16_swap_b32 v64, v65\n"
    "buffer_load_dwordx4 v249, s[12:15], s53 offen offset:2048 lds\n"
    "s_add_u32 s53, s53, 196608\n"
    "s_cmp_eq_u32 s3, 2\n"
    "s_cselect_b32 s63, 1024, 0\n"
    "s_sub_u32 s51, s51, s63\n"
    "s_sub_u32 s52, s52, s63\n"
    "s_mul_i32 s66, s63, 1536\n"
    "s_sub_u32 s53, s53, s66\n"
    "s_mul_i32 s66, s63, 1792\n"
    "s_sub_u32 s54, s54, s66\n"
    "v_add_f32 v64, v64, v65\n"
    "v_mul_f32 v236, 0x3b000000, v64\n"
    "s_nop 1\n"
    "v_mov_b32_dpp v65, v236 row_ror:8 row_mask:0xf bank_mask:0xf\n"
    "v_fma_f32 v65, -v236, v236, v65\n"
    "v_max_f32 v65, 0, v65\n"
    "v_add_f32 v65, 0x3727c5ac, v65\n"
    "v_rsq_f32 v238, v65\n"
    "s_mov_b32 exec_lo, 0x11\n"
    "s_mov_b32 exec_hi, 0x11\n"
    "buffer_store_dwordx4 v[236:239], v70, s[8:11], s74 offen sc1\n"
    "s_mov_b64 exec, -1\n"
    ST(1)
    "s_waitcnt vmcnt(13)\n"
    ST(61)
    "s_barrier\n"
    "ds_read_b64_tr_b16 v[32:33], v74 offset:24576\n"
    "ds_read_b64_tr_b16 v[34:35], v75 offset:25088\n"
    "ds_read_b64_tr_b16 v[36:37], v74 offset:32768\n"
    "ds_read_b64_tr_b16 v[38:39], v75 offset:33280\n"
    "ds_read_b64_tr_b16 v[40:41], v74 offset:40960\n"
    "ds_read_b64_tr_b16 v[42:43], v75 offset:41472\n"
    "ds_read_b64_tr_b16 v[44:45], v74 offset:49152\n"
    "ds_read_b64_tr_b16 v[46:47], v75 offset:49664\n"
    "ds_read_b128 a[184:187], v11 offset:6144\n"
    "ds_read_b128 a[188:191], v11 offset:8192\n"
    "ds_read_b128 a[192:195], v11 offset:10240\n"
    "ds_read_b128 a[196:199], v11 offset:12288\n"
    "ds_read_b128 a[200:203], v11 offset:14336\n"
    "ds_read_b128 a[204:207], v11 offset:16384\n"
    "ds_read_b128 a[208:211], v11 offset:18432\n"
    "ds_read_b128 a[212:215], v11 offset:20480\n"
    "ds_read_b128 a[216:219], v11 offset:22528\n"
    "s_waitcnt lgkmcnt(0)\n"
    "v_mfma_f32_16x16x32_bf16 v[100:103], v[32:35], a[184:187], 0\n"
    "ds_read_b64_tr_b16 v[48:49], v74 offset:28672\n"
    "s_add_u32 m0, s49, 108288\n"
    "v_mfma_f32_16x16x32_bf16 v[204:207], v[44:47], a[184:187], 0\n"
    "buffer_load_dwordx4 v252, s[4:7], s51 offen offset:256 lds\n"
    "ds_read_b64_tr_b16 v[50:51], v75 offset:29184\n"
    "v_mfma_f32_16x16x32_bf16 v[132:135], v[36:39], a[184:187], 0\n"
    "ds_read_b64_tr_b16 v[52:53], v74 offset:36864\n"
    "s_lshl_b32 s70, s45, 6\n"
    "v_mfma_f32_16x16x32_bf16 v[168:171], a[184:187], v[40:43], 0\n"
    "v_add_u32_e64 v24, s70, v0\n"
    "v_mfma_f32_16x16x32_bf16 v[104:107], v[32:35], a[188:191], 0\n"
    "ds_read_b64_tr_b16 v[54:55], v75 offset:37376\n"
    "s_add_u32 m0, s49, 109312\n"
    "v_mfma_f32_16x16x32_bf16 v[208:211], v[44:47], a[188:191], 0\n"
    "buffer_load_dwordx4 v253, s[4:7], s51 offen offset:256 lds\n"
    "ds_read_b64_tr_b16 v[56:57], v74 offset:45056\n"
    "v_mfma_f32_16x16x32_bf16 v[136:139], v[36:39], a[188:191], 0\n"
    "v_lshlrev_b32_e64 v83, 2, v24\n"
    "v_mfma_f32_16x16x32_bf16 v[172:175], a[188:191], v[40:43], 0\n"
    "ds_read_b64_tr_b16 v[58:59], v75 offset:45568\n"
    "s_add_u32 m0, s49, 109312\n"
    "v_mfma_f32_16x16x32_bf16 v[108:111], v[32:35], a[192:195], 0\n"
    "buffer_load_dwordx4 v252, s[4:7], s51 offen offset:1280 lds\n"
    "ds_read_b64_tr_b16 v[60:61], v74 offset:53248\n"
    "v_mfma_f32_16x16x32_bf16 v[212:215], v[44:47], a[192:195], 0\n"
    "ds_read_b64_tr_b16 v[62:63], v75 offset:53760\n"
    "v_min_u32 v24, 0x8f, v24\n"
    "v_mfma_f32_16x16x32_bf16 v[140:143], v[36:39], a[192:195], 0\n"
    "v_lshlrev_b32_e64 v81, 2, v24\n"
    "s_add_u32 m0, s49, 110336\n"
    "v_mfma_f32_16x16x32_bf16 v[176:179], a[192:195], v[40:43], 0\n"
    "buffer_load_dwordx4 v253, s[4:7], s51 offen offset:1280 lds\n"
    "ds_read_b128 a[220:223], v71 offset:6144\n"
    "v_mfma_f32_16x16x32_bf16 v[112:115], v[32:35], a[196:199], 0\n"
    "ds_read_b128 a[224:227], v71 offset:8192\n"
    "v_and_b32 v25, 0x60, v24\n"
    "v_mfma_f32_16x16x32_bf16 v[216:219], v[44:47], a[196:199], 0\n"
    "v_and_b32_e64 v26, 15, v24\n"
    "v_mfma_f32_16x16x32_bf16 v[144:147], v[36:39], a[196:199], 0\n"
    "ds_read_b128 a[228:231], v71 offset:10240\n"
    "v_lshl_add_u32 v25, v26, 1, v25\n"
    "v_mfma_f32_16x16x32_bf16 v[180:183], a[196:199], v[40:43], 0\n"
    "ds_read_b128 a[232:235], v71 offset:12288\n"
    "s_cmp_gt_u32 s47, 1024\n"
    "s_cbranch_scc1 Lnh_7_%=\n"
    "s_add_u32 m0, s47, 124672\n"
    "s_nop 0\n"
    "buffer_load_dwordx4 v254, s[4:7], s52 offen offset:256 lds\n"
    "Lnh_7_%=:\n"
    "v_mfma_f32_16x16x32_bf16 v[116:119], v[32:35], a[200:203], 0\n"
    "ds_read_b128 a[236:239], v71 offset:14336\n"
    "v_bfe_u32 v26, v24, 4, 1\n"
    "v_mfma_f32_16x16x32_bf16 v[220:223], v[44:47], a[200:203], 0\n"
    "v_add_u32_e64 v25, v25, v26\n"
    "s_add_u32 m0, s48, 151552\n"
    "v_mfma_f32_16x16x32_bf16 v[148:151], v[36:39], a[200:203], 0\n"
    "buffer_load_dwordx4 v250, s[16:19], s54 offen offset:0 lds\n"
    "ds_read_b128 a[240:243], v71 offset:16384\n"
    "v_mfma_f32_16x16x32_bf16 v[184:187], a[200:203], v[40:43], 0\n"
    "ds_read_b128 a[244:247], v71 offset:18432\n"
    "v_add_u32_e64 v25, s67, v25\n"
    "v_mfma_f32_16x16x32_bf16 v[120:123], v[32:35], a[204:207], 0\n"
    "v_and_b32_e64 v26, 7, v24\n"
    "v_mfma_f32_16x16x32_bf16 v[224:227], v[44:47], a[204:207], 0\n"
    "ds_read_b128 a[248:251], v71 offset:20480\n"
    "s_add_u32 m0, s48, 152576\n"
    "v_mfma_f32_16x16x32_bf16 v[152:155], v[36:39], a[204:207], 0\n"
    "buffer_load_dwordx4 v251, s[16:19], s54 offen offset:0 lds\n"
    "s_add_u32 s54, s54, 229376\n"
    "ds_read_b128 a[252:255], v71 offset:22528\n"
    "v_mfma_f32_16x16x32_bf16 v[188:191], a[204:207], v[40:43], 0\n"
    "v_bfe_u32 v27, v24, 3, 1\n"
    "v_mfma_f32_16x16x32_bf16 v[124:127], v[32:35], a[208:211], 0\n"
    "v_lshl_add_u32 v26, v26, 1, v27\n"
    "s_add_u32 m0, s48, 126976\n"
    "v_mfma_f32_16x16x32_bf16 v[228:231], v[44:47], a[208:211], 0\n"
    "buffer_load_dwordx4 v248, s[12:15], s53 offen offset:0 lds\n"
    "v_add_u32_e64 v26, s73, v26\n"
    "v_mfma_f32_16x16x32_bf16 v[156:159], v[36:39], a[208:211], 0\n"
    "v_cmp_gt_u32 vcc, 0x80, v24\n"
    "s_lshl1_add_u32 s70, s60, 25\n"
    "v_mfma_f32_16x16x32_bf16 v[192:195], a[208:211], v[40:43], 0\n"
    "s_add_u32 m0, s48, 128000\n"
    "v_mfma_f32_16x16x32_bf16 v[128:131], v[32:35], a[212:215], 0\n"
    "buffer_load_dwordx4 v249, s[12:15], s53 offen offset:0 lds\n"
    "v_mfma_f32_16x16x32_bf16 v[232:235], v[44:47], a[212:215], 0\n"
    "s_cmp_lt_u32 s60, 7\n"
    "s_cselect_b32 s70, s70, 24\n"
    "v_mfma_f32_16x16x32_bf16 v[160:163], v[36:39], a[212:215], 0\n"
    "s_lshl3_add_u32 s71, s45, s60\n"
    "v_mfma_f32_16x16x32_bf16 v[196:199], a[212:215], v[40:43], 0\n"
    "s_add_u32 m0, s48, 134144\n"
    "v_mfma_f32_16x16x32_bf16 v[164:167], v[36:39], a[216:219], 0\n"
    "buffer_load_dwordx4 v248, s[12:15], s53 offen offset:1024 lds\n"
    "v_mfma_f32_16x16x32_bf16 v[200:203], a[216:219], v[40:43], 0\n"
    "s_cmp_eq_u32 s45, 3\n"
    "s_cselect_b32 s70, s70, s71\n"
    "s_waitcnt vmcnt(10) lgkmcnt(0)\n"
    ST(16)
    "s_barrier\n"
    "v_mfma_f32_16x16x32_bf16 v[100:103], v[48:51], a[220:223], v[100:103]\n"
    "ds_read_b64_tr_b16 v[32:33], v76 offset:10240\n"
    "s_add_u32 m0, s48, 135168\n"
    "v_mfma_f32_16x16x32_bf16 v[204:207], v[60:63], a[220:223], v[204:207]\n"
    "buffer_load_dwordx4 v249, s[12:15], s53 offen offset:1024 lds\n"
    "ds_read_b64_tr_b16 v[34:35], v77 offset:10752\n"
    "v_mfma_f32_16x16x32_bf16 v[132:135], v[52:55], a[220:223], v[132:135]\n"
    "ds_read_b64_tr_b16 v[36:37], v76 offset:18432\n"
    "s_lshl_b32 s70, s70, 9\n"
    "v_mfma_f32_16x16x32_bf16 v[168:171], a[220:223], v[56:59], v[168:171]\n"
    "s_add_u32 s70, s70, 364544\n"
    "v_add_u32 v82, s70, v12\n"
    "v_mfma_f32_16x16x32_bf16 v[104:107], v[48:51], a[224:227], v[104:107]\n"
    "ds_read_b64_tr_b16 v[38:39], v77 offset:18944\n"
    "s_add_u32 m0, s48, 141312\n"
    "v_mfma_f32_16x16x32_bf16 v[208:211], v[60:63], a[224:227], v[208:211]\n"
    "buffer_load_dwordx4 v248, s[12:15], s53 offen offset:2048 lds\n"
    "ds_read_b64_tr_b16 v[40:41], v76 offset:26624\n"
    "v_mfma_f32_16x16x32_bf16 v[136:139], v[52:55], a[224:227], v[136:139]\n"
    "v_cndmask_b32_e64 v25, v26, v25, vcc\n"
    "v_mfma_f32_16x16x32_bf16 v[172:175], a[224:227], v[56:59], v[172:175]\n"
    "ds_read_b64_tr_b16 v[42:43], v77 offset:27136\n"
    "s_add_u32 m0, s48, 142336\n"
    "v_mfma_f32_16x16x32_bf16 v[108:111], v[48:51], a[228:231], v[108:111]\n"
    "buffer_load_dwordx4 v249, s[12:15], s53 offen offset:2048 lds\n"
    "s_add_u32 s53, s53, 196608\n"
    "s_cmp_eq_u32 s3, 3\n"
    "s_cselect_b32 s63, 1024, 0\n"
    "s_sub_u32 s51, s51, s63\n"
    "s_sub_u32 s52, s52, s63\n"
    "s_mul_i32 s66, s63, 1536\n"
    "s_sub_u32 s53, s53, s66\n"
    "s_mul_i32 s66, s63, 1792\n"
    "s_sub_u32 s54, s54, s66\n"
    "ds_read_b64_tr_b16 v[44:45], v76 offset:34816\n"
    "v_mfma_f32_16x16x32_bf16 v[212:215], v[60:63], a[228:231], v[212:215]\n"
    "ds_read_b64_tr_b16 v[46:47], v77 offset:35328\n"
    "v_lshlrev_b32 v25, 4, v25\n"
    "v_mfma_f32_16x16x32_bf16 v[140:143], v[52:55], a[228:231], v[140:143]\n"
    "v_add_u32 v80, 331776, v25\n"
    "s_add_u32 m0, s49, 5760\n"
    "v_mfma_f32_16x16x32_bf16 v[176:179], a[228:231], v[56:59], v[176:179]\n"
    "buffer_load_dwordx4 v252, s[4:7], s51 offen offset:384 lds\n"
    "ds_read_b128 a[184:187], v11 offset:57344\n"
    "v_mfma_f32_16x16x32_bf16 v[112:115], v[48:51], a[232:235], v[112:115]\n"
    "ds_read_b128 a[188:191], v11 offset:59392\n"
    "s_mul_i32 s70, s59, 448\n"
    "v_mfma_f32_16x16x32_bf16 v[216:219], v[60:63], a[232:235], v[216:219]\n"
    "s_lshl3_add_u32 s70, s60, s70\n"
    "s_add_u32 s70, s70, s45\n"
    "v_mfma_f32_16x16x32_bf16 v[144:147], v[52:55], a[232:235], v[144:147]\n"
    "ds_read_b128 a[192:195], v11 offset:61440\n"
    "s_add_u32 m0, s49, 6784\n"
    "v_mfma_f32_16x16x32_bf16 v[180:183], a[232:235], v[56:59], v[180:183]\n"
    "buffer_load_dwordx4 v253, s[4:7], s51 offen offset:384 lds\n"
    "ds_read_b128 a[196:199], v11 offset:63488\n"
    "v_mfma_f32_16x16x32_bf16 v[116:119], v[48:51], a[236:239], v[116:119]\n"
    "ds_read_b128 a[200:203], v72 offset:0\n"
    "s_lshl_b32 s70, s70, 9\n"
    "v_mfma_f32_16x16x32_bf16 v[220:223], v[60:63], a[236:239], v[220:223]\n"
    "s_add_u32 s70, s70, 4298752\n"
    "s_add_u32 m0, s49, 6784\n"
    "v_mfma_f32_16x16x32_bf16 v[148:151], v[52:55], a[236:239], v[148:151]\n"
    "buffer_load_dwordx4 v252, s[4:7], s51 offen offset:1408 lds\n"
    "ds_read_b128 a[204:207], v72 offset:2048\n"
    "v_mfma_f32_16x16x32_bf16 v[184:187], a[236:239], v[56:59], v[184:187]\n"
    "ds_read_b128 a[208:211], v72 offset:4096\n"
    "v_mfma_f32_16x16x32_bf16 v[120:123], v[48:51], a[240:243], v[120:123]\n"
    "s_cmp_lt_u32 s60, 7\n"
    "s_cselect_b32 s77, s70, 2147483648\n"
    "v_mfma_f32_16x16x32_bf16 v[224:227], v[60:63], a[240:243], v[224:227]\n"
    "ds_read_b128 a[212:215], v72 offset:6144\n"
    "s_add_u32 m0, s49, 7808\n"
    "v_mfma_f32_16x16x32_bf16 v[152:155], v[52:55], a[240:243], v[152:155]\n"
    "buffer_load_dwordx4 v253, s[4:7], s51 offen offset:1408 lds\n"
    "ds_read_b128 a[216:219], v72 offset:8192\n"
    "v_mfma_f32_16x16x32_bf16 v[188:191], a[240:243], v[56:59], v[188:191]\n"
    "s_mul_i32 s69, s62, 7\n"
    "v_and_b32 v64, 3, v0\n"
    "v_mfma_f32_16x16x32_bf16 v[124:127], v[48:51], a[244:247], v[124:127]\n"
    "v_min_u32_e64 v64, 2, v64\n"
    "v_mfma_f32_16x16x32_bf16 v[228:231], v[60:63], a[244:247], v[228:231]\n"
    "s_cmp_gt_u32 s47, 1024\n"
    "s_cbranch_scc1 Lnh_8_%=\n"
    "s_add_u32 m0, s47, 22144\n"
    "s_nop 0\n"
    "buffer_load_dwordx4 v254, s[4:7], s52 offen offset:384 lds\n"
    "Lnh_8_%=:\n"
    "v_mfma_f32_16x16x32_bf16 v[156:159], v[52:55], a[244:247], v[156:159]\n"
    "v_mad_u32_u24 v64, v64, 3, s69\n"
    "v_mfma_f32_16x16x32_bf16 v[192:195], a[244:247], v[56:59], v[192:195]\n"
    "v_bfe_u32 v65, v0, 3, 2\n"
    "s_add_u32 m0, s48, 49152\n"
    "v_mfma_f32_16x16x32_bf16 v[128:131], v[48:51], a[248:251], v[128:131]\n"
    "buffer_load_dwordx4 v250, s[16:19], s54 offen offset:0 lds\n"
    "s_lshl_b32 s70, s45, 2\n"
    "v_mfma_f32_16x16x32_bf16 v[232:235], v[60:63], a[248:251], v[232:235]\n"
    "v_add_u32 v65, s70, v65\n"
    "v_mfma_f32_16x16x32_bf16 v[160:163], v[52:55], a[248:251], v[160:163]\n"
    "v_lshrrev_b32_e64 v66, 3, v64\n"
    "v_mfma_f32_16x16x32_bf16 v[196:199], a[248:251], v[56:59], v[196:199]\n"
    "v_lshl_add_u32 v65, v65, 3, v66\n"
    "s_add_u32 m0, s48, 50176\n"
    "v_mfma_f32_16x16x32_bf16 v[164:167], v[52:55], a[252:255], v[164:167]\n"
    "buffer_load_dwordx4 v251, s[16:19], s54 offen offset:0 lds\n"
    "s_add_u32 s54, s54, 229376\n"
    "v_mfma_f32_16x16x32_bf16 v[200:203], a[252:255], v[56:59], v[200:203]\n"
    "v_bfe_u32 v66, v0, 2, 1\n"
    "s_waitcnt lgkmcnt(0)\n"
    "v_mfma_f32_16x16x32_bf16 v[100:103], v[32:35], a[184:187], v[100:103]\n"
    "ds_read_b64_tr_b16 v[48:49], v76 offset:14336\n"
    "v_lshl_add_u32 v65, v65, 1, v66\n"
    "v_mfma_f32_16x16x32_bf16 v[204:207], v[44:47], a[184:187], v[204:207]\n"
    "ds_read_b64_tr_b16 v[50:51], v77 offset:14848\n"
    "s_add_u32 m0, s48, 24576\n"
    "v_mfma_f32_16x16x32_bf16 v[132:135], v[36:39], a[184:187], v[132:135]\n"
    "buffer_load_dwordx4 v248, s[12:15], s53 offen offset:0 lds\n"
    "ds_read_b64_tr_b16 v[52:53], v76 offset:22528\n"
    "v_mfma_f32_16x16x32_bf16 v[168:171], a[184:187], v[40:43], v[168:171]\n"
    "v_bfe_u32 v66, v64, 2, 1\n"
    "v_mfma_f32_16x16x32_bf16 v[104:107], v[32:35], a[188:191], v[104:107]\n"
    "ds_read_b64_tr_b16 v[54:55], v77 offset:23040\n"
    "v_lshl_add_u32 v65, v65, 1, v66\n"
    "v_mfma_f32_16x16x32_bf16 v[208:211], v[44:47], a[188:191], v[208:211]\n"
    "ds_read_b64_tr_b16 v[56:57], v76 offset:30720\n"
    "v_lshlrev_b32 v65, 7, v65\n"
    "v_mfma_f32_16x16x32_bf16 v[136:139], v[36:39], a[188:191], v[136:139]\n"
    "v_add_u32 v246, 4096, v65\n"
    "s_lshl_b32 s70, s62, 2\n"
    "v_mfma_f32_16x16x32_bf16 v[172:175], a[188:191], v[40:43], v[172:175]\n"
    "ds_read_b64_tr_b16 v[58:59], v77 offset:31232\n"
    "s_add_u32 m0, s48, 25600\n"
    "v_mfma_f32_16x16x32_bf16 v[108:111], v[32:35], a[192:195], v[108:111]\n"
    "buffer_load_dwordx4 v249, s[12:15], s53 offen offset:0 lds\n"
    "ds_read_b64_tr_b16 v[60:61], v76 offset:38912\n"
    "v_mfma_f32_16x16x32_bf16 v[212:215], v[44:47], a[192:195], v[212:215]\n"
    "ds_read_b64_tr_b16 v[62:63], v77 offset:39424\n"
    "s_add_u32 s70, s70, s45\n"
    "v_mfma_f32_16x16x32_bf16 v[140:143], v[36:39], a[192:195], v[140:143]\n"
    "s_mul_i32 s70, s70, 114688\n"
    "s_add_u32 s85, s70, 626688\n"
    "v_mfma_f32_16x16x32_bf16 v[176:179], a[192:195], v[40:43], v[176:179]\n"
    "ds_read_b128 a[220:223], v71 offset:57344\n"
    "s_and_b32 s70, s62, 0\n"
    "v_mfma_f32_16x16x32_bf16 v[112:115], v[32:35], a[196:199], v[112:115]\n"
    "ds_read_b128 a[224:227], v71 offset:59392\n"
    "s_mul_i32 s70, s70, 14336\n"
    "v_mfma_f32_16x16x32_bf16 v[216:219], v[44:47], a[196:199], v[216:219]\n"
    "s_add_u32 s85, s85, s70\n"
    "s_add_u32 m0, s48, 31744\n"
    "v_mfma_f32_16x16x32_bf16 v[144:147], v[36:39], a[196:199], v[144:147]\n"
    "buffer_load_dwordx4 v248, s[12:15], s53 offen offset:1024 lds\n"
    "ds_read_b128 a[228:231], v71 offset:61440\n"
    "v_mfma_f32_16x16x32_bf16 v[180:183], a[196:199], v[40:43], v[180:183]\n"
    "ds_read_b128 a[232:235], v71 offset:63488\n"
    "s_lshl_b32 s70, s45, 14\n"
    "v_mfma_f32_16x16x32_bf16 v[116:119], v[32:35], a[200:203], v[116:119]\n"
    "ds_read_b128 a[236:239], v73 offset:0\n"
    "s_lshl_b32 s69, s62, 7\n"
    "v_mfma_f32_16x16x32_bf16 v[220:223], v[44:47], a[200:203], v[220:223]\n"
    "s_add_u32 s87, s70, s69\n"
    "s_mov_b32 s26, 524288\n"
    "v_mfma_f32_16x16x32_bf16 v[148:151], v[36:39], a[200:203], v[148:151]\n"
    "ds_read_b128 a[240:243], v73 offset:2048\n"
    "s_add_u32 m0, s48, 32768\n"
    "v_mfma_f32_16x16x32_bf16 v[184:187], a[200:203], v[40:43], v[184:187]\n"
    "buffer_load_dwordx4 v249, s[12:15], s53 offen offset:1024 lds\n"
    "ds_read_b128 a[244:247], v73 offset:4096\n"
    "v_mfma_f32_16x16x32_bf16 v[120:123], v[32:35], a[204:207], v[120:123]\n"
    "s_mov_b32 s27, 0x20000\n"
    "v_and_b32 v64, 15, v0\n"
    "v_mfma_f32_16x16x32_bf16 v[224:227], v[44:47], a[204:207], v[224:227]\n"
    "ds_read_b128 a[248:251], v73 offset:6144\n"
    "v_lshrrev_b32_e64 v65, 2, v247\n"
    "v_mfma_f32_16x16x32_bf16 v[152:155], v[36:39], a[204:207], v[152:155]\n"
    "ds_read_b128 a[252:255], v73 offset:8192\n"
    "v_sub_u32_e64 v64, v64, v65\n"
    "v_mfma_f32_16x16x32_bf16 v[188:191], a[204:207], v[40:43], v[188:191]\n"
    "v_mov_b32 v66, 0xf149f2ca\n"
    "v_mfma_f32_16x16x32_bf16 v[124:127], v[32:35], a[208:211], v[124:127]\n"
    "s_add_u32 m0, s48, 38912\n"
    "v_mfma_f32_16x16x32_bf16 v[228:231], v[44:47], a[208:211], v[228:231]\n"
    "buffer_load_dwordx4 v248, s[12:15], s53 offen offset:2048 lds\n"
    "v_mfma_f32_16x16x32_bf16 v[156:159], v[36:39], a[208:211], v[156:159]\n"
    "s_cmp_lg_u32 s59, 0\n"
    "s_cselect_b64 s[68:69], -1, 0\n"
    "v_mfma_f32_16x16x32_bf16 v[192:195], a[208:211], v[40:43], v[192:195]\n"
    "v_cmp_gt_u32 s[70:71], 32, v0\n"
    "s_and_b64 s[72:73], s[68:69], s[70:71]\n"
    "v_mfma_f32_16x16x32_bf16 v[128:131], v[32:35], a[212:215], v[128:131]\n"
    "s_andn2_b64 s[74:75], s[68:69], s[70:71]\n"
    "v_subrev_u32_e64 v65, 0, v64\n"
    "v_mfma_f32_16x16x32_bf16 v[232:235], v[44:47], a[212:215], v[232:235]\n"
    "v_cmp_gt_u32 vcc, 8, v65\n"
    "v_mfma_f32_16x16x32_bf16 v[160:163], v[36:39], a[212:215], v[160:163]\n"
    "v_cmp_gt_i32 s[64:65], -8, v65\n"
    "s_add_u32 m0, s48, 39936\n"
    "v_mfma_f32_16x16x32_bf16 v[196:199], a[212:215], v[40:43], v[196:199]\n"
    "buffer_load_dwordx4 v249, s[12:15], s53 offen offset:2048 lds\n"
    "s_add_u32 s53, s53, 196608\n"
    "v_mfma_f32_16x16x32_bf16 v[164:167], v[36:39], a[216:219], v[164:167]\n"
    "v_cmp_gt_i32 s[70:71], 0, v65\n"
    "v_mfma_f32_16x16x32_bf16 v[200:203], a[216:219], v[40:43], v[200:203]\n"
    "v_cndmask_b32 v84, v66, 0, vcc\n"
    "s_waitcnt vmcnt(12) lgkmcnt(0)\n"
    ST(17)
    "s_barrier\n"
    "v_mfma_f32_16x16x32_bf16 v[100:103], v[48:51], a[220:223], v[100:103]\n"
    "ds_read_b64_tr_b16 v[32:33], v76 offset:61440\n"
    "v_cndmask_b32 v88, v66, 0, s[64:65]\n"
    "v_mfma_f32_16x16x32_bf16 v[204:207], v[60:63], a[220:223], v[204:207]\n"
    "ds_read_b64_tr_b16 v[34:35], v77 offset:61952\n"
    "s_cmp_eq_u32 s3, 4\n"
    "s_cselect_b32 s63, 1024, 0\n"
    "s_sub_u32 s51, s51, s63\n"
    "s_sub_u32 s52, s52, s63\n"
    "s_mul_i32 s66, s63, 1536\n"
    "s_sub_u32 s53, s53, s66\n"
    "s_mul_i32 s66, s63, 1792\n"
    "s_sub_u32 s54, s54, s66\n"
    "v_mfma_f32_16x16x32_bf16 v[132:135], v[52:55], a[220:223], v[132:135]\n"
    "ds_read_b64_tr_b16 v[36:37], v78 offset:4096\n"
    "s_and_b64 s[70:71], s[70:71], s[72:73]\n"
    "v_mfma_f32_16x16x32_bf16 v[168:171], a[220:223], v[56:59], v[168:171]\n"
    "s_and_b64 s[64:65], s[64:65], s[74:75]\n"
    "s_add_u32 m0, s49, 56832\n"
    "v_mfma_f32_16x16x32_bf16 v[104:107], v[48:51], a[224:227], v[104:107]\n"
    "buffer_load_dwordx4 v252, s[4:7], s51 offen offset:512 lds\n"
    "ds_read_b64_tr_b16 v[38:39], v79 offset:4608\n"
    "v_mfma_f32_16x16x32_bf16 v[208:211], v[60:63], a[224:227], v[208:211]\n"
    "ds_read_b64_tr_b16 v[40:41], v78 offset:12288\n"
    "v_cndmask_b32 v92, v66, 0, s[70:71]\n"
    "v_mfma_f32_16x16x32_bf16 v[136:139], v[52:55], a[224:227], v[136:139]\n"
    "v_cndmask_b32 v96, v66, 0, s[64:65]\n"
    "s_add_u32 m0, s49, 57856\n"
    "v_mfma_f32_16x16x32_bf16 v[172:175], a[224:227], v[56:59], v[172:175]\n"
    "buffer_load_dwordx4 v253, s[4:7], s51 offen offset:512 lds\n"
    "ds_read_b64_tr_b16 v[42:43], v79 offset:12800\n"
    "v_mfma_f32_16x16x32_bf16 v[108:111], v[48:51], a[228:231], v[108:111]\n"
    "ds_read_b64_tr_b16 v[44:45], v78 offset:20480\n"
    "v_subrev_u32_e64 v65, 1, v64\n"
    "v_mfma_f32_16x16x32_bf16 v[212:215], v[60:63], a[228:231], v[212:215]\n"
    "ds_read_b64_tr_b16 v[46:47], v79 offset:20992\n"
    "s_add_u32 m0, s49, 57856\n"
    "v_mfma_f32_16x16x32_bf16 v[140:143], v[52:55], a[228:231], v[140:143]\n"
    "buffer_load_dwordx4 v252, s[4:7], s51 offen offset:1536 lds\n"
    "v_cmp_gt_u32 vcc, 8, v65\n"
    "v_mfma_f32_16x16x32_bf16 v[176:179], a[228:231], v[56:59], v[176:179]\n"
    "ds_read_b128 a[184:187], v72 offset:43008\n"
    "v_cmp_gt_i32 s[64:65], -8, v65\n"
    "v_mfma_f32_16x16x32_bf16 v[112:115], v[48:51], a[232:235], v[112:115]\n"
    "ds_read_b128 a[188:191], v72 offset:45056\n"
    "s_add_u32 m0, s49, 58880\n"
    "v_mfma_f32_16x16x32_bf16 v[216:219], v[60:63], a[232:235], v[216:219]\n"
    "buffer_load_dwordx4 v253, s[4:7], s51 offen offset:1536 lds\n"
    "v_cmp_gt_i32 s[70:71], 0, v65\n"
    "v_mfma_f32_16x16x32_bf16 v[144:147], v[52:55], a[232:235], v[144:147]\n"
    "ds_read_b128 a[192:195], v72 offset:47104\n"
    "v_cndmask_b32 v85, v66, 0, vcc\n"
    "v_mfma_f32_16x16x32_bf16 v[180:183], a[232:235], v[56:59], v[180:183]\n"
    "ds_read_b128 a[196:199], v72 offset:49152\n"
    "v_cndmask_b32 v89, v66, 0, s[64:65]\n"
    "v_mfma_f32_16x16x32_bf16 v[116:119], v[48:51], a[236:239], v[116:119]\n"
    "ds_read_b128 a[200:203], v72 offset:51200\n"
    "s_and_b64 s[70:71], s[70:71], s[72:73]\n"
    "v_mfma_f32_16x16x32_bf16 v[220:223], v[60:63], a[236:239], v[220:223]\n"
    "s_cmp_gt_u32 s47, 1024\n"
    "s_cbranch_scc1 Lnh_9_%=\n"
    "s_add_u32 m0, s47, 73216\n"
    "s_nop 0\n"
    "buffer_load_dwordx4 v254, s[4:7], s52 offen offset:512 lds\n"
    "Lnh_9_%=:\n"
    "v_mfma_f32_16x16x32_bf16 v[148:151], v[52:55], a[236:239], v[148:151]\n"
    "ds_read_b128 a[204:207], v72 offset:53248\n"
    "s_and_b64 s[64:65], s[64:65], s[74:75]\n"
    "v_mfma_f32_16x16x32_bf16 v[184:187], a[236:239], v[56:59], v[184:187]\n"
    "ds_read_b128 a[208:211], v72 offset:55296\n"
    "s_add_u32 m0, s48, 100352\n"
    "v_mfma_f32_16x16x32_bf16 v[120:123], v[48:51], a[240:243], v[120:123]\n"
    "buffer_load_dwordx4 v250, s[16:19], s54 offen offset:0 lds\n"
    "v_cndmask_b32 v93, v66, 0, s[70:71]\n"
    "v_mfma_f32_16x16x32_bf16 v[224:227], v[60:63], a[240:243], v[224:227]\n"
    "ds_read_b128 a[212:215], v72 offset:57344\n"
    "v_cndmask_b32 v97, v66, 0, s[64:65]\n"
    "v_mfma_f32_16x16x32_bf16 v[152:155], v[52:55], a[240:243], v[152:155]\n"
    "ds_read_b128 a[216:219], v72 offset:59392\n"
    "s_add_u32 m0, s48, 101376\n"
    "v_mfma_f32_16x16x32_bf16 v[188:191], a[240:243], v[56:59], v[188:191]\n"
    "buffer_load_dwordx4 v251, s[16:19], s54 offen offset:0 lds\n"
    "s_add_u32 s54, s54, 229376\n"
    "v_mfma_f32_16x16x32_bf16 v[124:127], v[48:51], a[244:247], v[124:127]\n"
    "v_subrev_u32 v65, 2, v64\n"
    "v_mfma_f32_16x16x32_bf16 v[228:231], v[60:63], a[244:247], v[228:231]\n"
    "v_cmp_gt_u32 vcc, 8, v65\n"
    "s_add_u32 m0, s48, 75776\n"
    "v_mfma_f32_16x16x32_bf16 v[156:159], v[52:55], a[244:247], v[156:159]\n"
    "buffer_load_dwordx4 v248, s[12:15], s53 offen offset:0 lds\n"
    "v_cmp_gt_i32 s[64:65], -8, v65\n"
    "v_mfma_f32_16x16x32_bf16 v[192:195], a[244:247], v[56:59], v[192:195]\n"
    "v_cmp_gt_i32 s[70:71], 0, v65\n"
    "v_mfma_f32_16x16x32_bf16 v[128:131], v[48:51], a[248:251], v[128:131]\n"
    "v_cndmask_b32 v86, v66, 0, vcc\n"
    "s_add_u32 m0, s48, 76800\n"
    "v_mfma_f32_16x16x32_bf16 v[232:235], v[60:63], a[248:251], v[232:235]\n"
    "buffer_load_dwordx4 v249, s[12:15], s53 offen offset:0 lds\n"
    "v_cndmask_b32 v90, v66, 0, s[64:65]\n"
    "v_mfma_f32_16x16x32_bf16 v[160:163], v[52:55], a[248:251], v[160:163]\n"
    "s_and_b64 s[70:71], s[70:71], s[72:73]\n"
    "s_and_b64 s[64:65], s[64:65], s[74:75]\n"
    "v_mfma_f32_16x16x32_bf16 v[196:199], a[248:251], v[56:59], v[196:199]\n"
    "v_cndmask_b32 v94, v66, 0, s[70:71]\n"
    "s_add_u32 m0, s48, 82944\n"
    "v_mfma_f32_16x16x32_bf16 v[164:167], v[52:55], a[252:255], v[164:167]\n"
    "buffer_load_dwordx4 v248, s[12:15], s53 offen offset:1024 lds\n"
    "v_cndmask_b32 v98, v66, 0, s[64:65]\n"
    "v_mfma_f32_16x16x32_bf16 v[200:203], a[252:255], v[56:59], v[200:203]\n"
    "v_subrev_u32 v65, 3, v64\n"
    "s_waitcnt lgkmcnt(0)\n"
    "v_mfma_f32_16x16x32_bf16 v[100:103], v[32:35], a[184:187], v[100:103]\n"
    "ds_read_b64_tr_b16 v[48:49], v78 offset:0\n"
    "v_cmp_gt_u32 vcc, 8, v65\n"
    "v_mfma_f32_16x16x32_bf16 v[204:207], v[44:47], a[184:187], v[204:207]\n"
    "ds_read_b64_tr_b16 v[50:51], v79 offset:512\n"
    "v_cmp_gt_i32 s[64:65], -8, v65\n"
    "v_mfma_f32_16x16x32_bf16 v[132:135], v[36:39], a[184:187], v[132:135]\n"
    "ds_read_b64_tr_b16 v[52:53], v78 offset:8192\n"
    "v_cmp_gt_i32 s[70:71], 0, v65\n"
    "v_mfma_f32_16x16x32_bf16 v[168:171], a[184:187], v[40:43], v[168:171]\n"
    "v_cndmask_b32 v87, v66, 0, vcc\n"
    "v_mfma_f32_16x16x32_bf16 v[104:107], v[32:35], a[188:191], v[104:107]\n"
    "ds_read_b64_tr_b16 v[54:55], v79 offset:8704\n"
    "s_add_u32 m0, s48, 83968\n"
    "v_mfma_f32_16x16x32_bf16 v[208:211], v[44:47], a[188:191], v[208:211]\n"
    "buffer_load_dwordx4 v249, s[12:15], s53 offen offset:1024 lds\n"
    "ds_read_b64_tr_b16 v[56:57], v78 offset:16384\n"
    "v_mfma_f32_16x16x32_bf16 v[136:139], v[36:39], a[188:191], v[136:139]\n"
    "v_cndmask_b32 v91, v66, 0, s[64:65]\n"
    "s_and_b64 s[70:71], s[70:71], s[72:73]\n"
    "v_mfma_f32_16x16x32_bf16 v[172:175], a[188:191], v[40:43], v[172:175]\n"
    "ds_read_b64_tr_b16 v[58:59], v79 offset:16896\n"
    "s_and_b64 s[64:65], s[64:65], s[74:75]\n"
    "v_mfma_f32_16x16x32_bf16 v[108:111], v[32:35], a[192:195], v[108:111]\n"
    "ds_read_b64_tr_b16 v[60:61], v78 offset:24576\n"
    "v_cndmask_b32 v95, v66, 0, s[70:71]\n"
    "v_mfma_f32_16x16x32_bf16 v[212:215], v[44:47], a[192:195], v[212:215]\n"
    "ds_read_b64_tr_b16 v[62:63], v79 offset:25088\n"
    "v_cndmask_b32 v99, v66, 0, s[64:65]\n"
    "v_mfma_f32_16x16x32_bf16 v[140:143], v[36:39], a[192:195], v[140:143]\n"
    "v_cvt_pk_bf16_f32 v16, v84, v85\n"
    "v_mfma_f32_16x16x32_bf16 v[176:179], a[192:195], v[40:43], v[176:179]\n"
    "ds_read_b128 a[220:223], v73 offset:43008\n"
    "v_cvt_pk_bf16_f32 v17, v86, v87\n"
    "v_mfma_f32_16x16x32_bf16 v[112:115], v[32:35], a[196:199], v[112:115]\n"
    "ds_read_b128 a[224:227], v73 offset:45056\n"
    "v_cvt_pk_bf16_f32 v18, v88, v89\n"
    "v_mfma_f32_16x16x32_bf16 v[216:219], v[44:47], a[196:199], v[216:219]\n"
    "v_cvt_pk_bf16_f32 v19, v90, v91\n"
    "v_mfma_f32_16x16x32_bf16 v[144:147], v[36:39], a[196:199], v[144:147]\n"
    "ds_read_b128 a[228:231], v73 offset:47104\n"
    "v_cvt_pk_bf16_f32 v20, v92, v93\n"
    "v_mfma_f32_16x16x32_bf16 v[180:183], a[196:199], v[40:43], v[180:183]\n"
    "ds_read_b128 a[232:235], v73 offset:49152\n"
    "v_cvt_pk_bf16_f32 v21, v94, v95\n"
    "v_mfma_f32_16x16x32_bf16 v[116:119], v[32:35], a[200:203], v[116:119]\n"
    "ds_read_b128 a[236:239], v73 offset:51200\n"
    "s_add_u32 m0, s48, 90112\n"
    "v_mfma_f32_16x16x32_bf16 v[220:223], v[44:47], a[200:203], v[220:223]\n"
    "buffer_load_dwordx4 v248, s[12:15], s53 offen offset:2048 lds\n"
    "v_cvt_pk_bf16_f32 v22, v96, v97\n"
    "v_mfma_f32_16x16x32_bf16 v[148:151], v[36:39], a[200:203], v[148:151]\n"
    "ds_read_b128 a[240:243], v73 offset:53248\n"
    "v_cvt_pk_bf16_f32 v23, v98, v99\n"
    "v_mfma_f32_16x16x32_bf16 v[184:187], a[200:203], v[40:43], v[184:187]\n"
    "ds_read_b128 a[244:247], v73 offset:55296\n"
    "v_pk_ashrrev_i16 v16, 15, v16 op_sel_hi:[0,1]\n"
    "v_mfma_f32_16x16x32_bf16 v[120:123], v[32:35], a[204:207], v[120:123]\n"
    "v_pk_ashrrev_i16 v17, 15, v17 op_sel_hi:[0,1]\n"
    "v_mfma_f32_16x16x32_bf16 v[224:227], v[44:47], a[204:207], v[224:227]\n"
    "ds_read_b128 a[248:251], v73 offset:57344\n"
    "v_pk_ashrrev_i16 v18, 15, v18 op_sel_hi:[0,1]\n"
    "v_mfma_f32_16x16x32_bf16 v[152:155], v[36:39], a[204:207], v[152:155]\n"
    "ds_read_b128 a[252:255], v73 offset:59392\n"
    "v_pk_ashrrev_i16 v19, 15, v19 op_sel_hi:[0,1]\n"
    "v_mfma_f32_16x16x32_bf16 v[188:191], a[204:207], v[40:43], v[188:191]\n"
    "v_pk_ashrrev_i16 v20, 15, v20 op_sel_hi:[0,1]\n"
    "v_mfma_f32_16x16x32_bf16 v[124:127], v[32:35], a[208:211], v[124:127]\n"
    "v_pk_ashrrev_i16 v21, 15, v21 op_sel_hi:[0,1]\n"
    "v_mfma_f32_16x16x32_bf16 v[228:231], v[44:47], a[208:211], v[228:231]\n"
    "v_pk_ashrrev_i16 v22, 15, v22 op_sel_hi:[0,1]\n"
    "v_mfma_f32_16x16x32_bf16 v[156:159], v[36:39], a[208:211], v[156:159]\n"
    "v_pk_ashrrev_i16 v23, 15, v23 op_sel_hi:[0,1]\n"
    "v_mfma_f32_16x16x32_bf16 v[192:195], a[208:211], v[40:43], v[192:195]\n"
    "v_mov_b32 v236, 0x3f803f80\n"
    "s_add_u32 m0, s48, 91136\n"
    "v_mfma_f32_16x16x32_bf16 v[128:131], v[32:35], a[212:215], v[128:131]\n"
    "buffer_load_dwordx4 v249, s[12:15], s53 offen offset:2048 lds\n"
    "s_add_u32 s53, s53, 196608\n"
    "v_mfma_f32_16x16x32_bf16 v[232:235], v[44:47], a[212:215], v[232:235]\n"
    "v_mov_b32 v237, 0x3f803f80\n"
    "s_lshl_b32 s70, s45, 6\n"
    "v_mfma_f32_16x16x32_bf16 v[160:163], v[36:39], a[212:215], v[160:163]\n"
    "v_add_u32 v238, s70, v247\n"
    "v_mfma_f32_16x16x32_bf16 v[196:199], a[212:215], v[40:43], v[196:199]\n"
    "v_add_u32 v239, s70, v14\n"
    "s_and_b32 s70, s45, 1\n"
    "v_mfma_f32_16x16x32_bf16 v[164:167], v[36:39], a[216:219], v[164:167]\n"
    "s_lshl_b32 s70, s70, 9\n"
    "v_lshrrev_b32 v65, 5, v0\n"
    "v_mfma_f32_16x16x32_bf16 v[200:203], a[216:219], v[40:43], v[200:203]\n"
    "v_lshlrev_b32 v64, 2, v14\n"
    "s_waitcnt vmcnt(12) lgkmcnt(0)\n"
    ST(18)
    "s_barrier\n"
    "v_mfma_f32_16x16x32_bf16 v[100:103], v[48:51], a[220:223], v[100:103]\n"
    "ds_read_b64_tr_b16 v[32:33], v74 offset:24576\n"
    "v_lshl_add_u32 v64, v65, 8, v64\n"
    "v_mfma_f32_16x16x32_bf16 v[204:207], v[60:63], a[220:223], v[204:207]\n"
    "ds_read_b64_tr_b16 v[34:35], v75 offset:25088\n"
    "s_cmp_eq_u32 s3, 5\n"
    "s_cselect_b32 s63, 1024, 0\n"
    "s_sub_u32 s51, s51, s63\n"
    "s_sub_u32 s52, s52, s63\n"
    "s_mul_i32 s66, s63, 1536\n"
    "s_sub_u32 s53, s53, s66\n"
    "s_mul_i32 s66, s63, 1792\n"
    "s_sub_u32 s54, s54, s66\n"
    "v_mfma_f32_16x16x32_bf16 v[132:135], v[52:55], a[220:223], v[132:135]\n"
    "ds_read_b64_tr_b16 v[36:37], v74 offset:32768\n"
    "v_bfe_u32 v65, v0, 4, 1\n"
    "v_mfma_f32_16x16x32_bf16 v[168:171], a[220:223], v[56:59], v[168:171]\n"
    "v_lshl_add_u32 v64, v65, 14, v64\n"
    "s_add_u32 m0, s49, 107904\n"
    "v_mfma_f32_16x16x32_bf16 v[104:107], v[48:51], a[224:227], v[104:107]\n"
    "buffer_load_dwordx4 v252, s[4:7], s51 offen offset:640 lds\n"
    "ds_read_b64_tr_b16 v[38:39], v75 offset:33280\n"
    "v_mfma_f32_16x16x32_bf16 v[208:211], v[60:63], a[224:227], v[208:211]\n"
    "ds_read_b64_tr_b16 v[40:41], v74 offset:40960\n"
    "v_add_u32_e64 v31, s70, v64\n"
    "v_mfma_f32_16x16x32_bf16 v[136:139], v[52:55], a[224:227], v[136:139]\n"
    "s_lshr_b32 s70, s45, 1\n"
    "s_add_u32 m0, s49, 108928\n"
    "v_mfma_f32_16x16x32_bf16 v[172:175], a[224:227], v[56:59], v[172:175]\n"
    "buffer_load_dwordx4 v253, s[4:7], s51 offen offset:640 lds\n"
    "ds_read_b64_tr_b16 v[42:43], v75 offset:41472\n"
    "v_mfma_f32_16x16x32_bf16 v[108:111], v[48:51], a[228:231], v[108:111]\n"
    "ds_read_b64_tr_b16 v[44:45], v74 offset:49152\n"
    "s_lshl1_add_u32 s70, s60, s70\n"
    "v_mfma_f32_16x16x32_bf16 v[212:215], v[60:63], a[228:231], v[212:215]\n"
    "ds_read_b64_tr_b16 v[46:47], v75 offset:49664\n"
    "s_add_u32 m0, s49, 108928\n"
    "v_mfma_f32_16x16x32_bf16 v[140:143], v[52:55], a[228:231], v[140:143]\n"
    "buffer_load_dwordx4 v252, s[4:7], s51 offen offset:1664 lds\n"
    "s_lshl_b32 s71, s59, 7\n"
    "v_mfma_f32_16x16x32_bf16 v[176:179], a[228:231], v[56:59], v[176:179]\n"
    "ds_read_b128 a[184:187], v11 offset:6144\n"
    "s_add_u32 s70, s70, s71\n"
    "v_mfma_f32_16x16x32_bf16 v[112:115], v[48:51], a[232:235], v[112:115]\n"
    "ds_read_b128 a[188:191], v11 offset:8192\n"
    "s_add_u32 m0, s49, 109952\n"
    "v_mfma_f32_16x16x32_bf16 v[216:219], v[60:63], a[232:235], v[216:219]\n"
    "buffer_load_dwordx4 v253, s[4:7], s51 offen offset:1664 lds\n"
    "s_lshl_b32 s70, s70, 10\n"
    "v_mfma_f32_16x16x32_bf16 v[144:147], v[52:55], a[232:235], v[144:147]\n"
    "ds_read_b128 a[192:195], v11 offset:10240\n"
    "s_add_u32 s86, s70, 11636736\n"
    "v_mfma_f32_16x16x32_bf16 v[180:183], a[232:235], v[56:59], v[180:183]\n"
    "ds_read_b128 a[196:199], v11 offset:12288\n"
    "s_lshr_b32 s70, s2, 4\n"
    "v_mfma_f32_16x16x32_bf16 v[116:119], v[48:51], a[236:239], v[116:119]\n"
    "ds_read_b128 a[200:203], v11 offset:14336\n"
    "s_and_b32 s71, s2, 7\n"
    "v_mfma_f32_16x16x32_bf16 v[220:223], v[60:63], a[236:239], v[220:223]\n"
    "s_cmp_gt_u32 s47, 1024\n"
    "s_cbranch_scc1 Lnh_10_%=\n"
    "s_add_u32 m0, s47, 124288\n"
    "s_nop 0\n"
    "buffer_load_dwordx4 v254, s[4:7], s52 offen offset:640 lds\n"
    "Lnh_10_%=:\n"
    "v_mfma_f32_16x16x32_bf16 v[148:151], v[52:55], a[236:239], v[148:151]\n"
    "ds_read_b128 a[204:207], v11 offset:16384\n"
    "s_lshl4_add_u32 s70, s71, s70\n"
    "v_mfma_f32_16x16x32_bf16 v[184:187], a[236:239], v[56:59], v[184:187]\n"
    "ds_read_b128 a[208:211], v11 offset:18432\n"
    "s_add_u32 m0, s48, 151552\n"
    "v_mfma_f32_16x16x32_bf16 v[120:123], v[48:51], a[240:243], v[120:123]\n"
    "buffer_load_dwordx4 v250, s[16:19], s54 offen offset:0 lds\n"
    "s_lshl2_add_u32 s70, s70, s45\n"
    "v_mfma_f32_16x16x32_bf16 v[224:227], v[60:63], a[240:243], v[224:227]\n"
    "ds_read_b128 a[212:215], v11 offset:20480\n"
    "s_lshl_b32 s90, s70, 14\n"
    "v_mfma_f32_16x16x32_bf16 v[152:155], v[52:55], a[240:243], v[152:155]\n"
    "ds_read_b128 a[216:219], v11 offset:22528\n"
    "s_add_u32 m0, s48, 152576\n"
    "v_mfma_f32_16x16x32_bf16 v[188:191], a[240:243], v[56:59], v[188:191]\n"
    "buffer_load_dwordx4 v251, s[16:19], s54 offen offset:0 lds\n"
    "s_add_u32 s54, s54, 229376\n"
    "v_mfma_f32_16x16x32_bf16 v[124:127], v[48:51], a[244:247], v[124:127]\n"
    "s_add_u32 s90, s90, 512\n"
    "v_and_b32_e64 v244, 3, v0\n"
    "v_mfma_f32_16x16x32_bf16 v[228:231], v[60:63], a[244:247], v[228:231]\n"
    "v_lshlrev_b32_e64 v244, 7, v244\n"
    "s_add_u32 m0, s48, 126976\n"
    "v_mfma_f32_16x16x32_bf16 v[156:159], v[52:55], a[244:247], v[156:159]\n"
    "buffer_load_dwordx4 v248, s[12:15], s53 offen offset:0 lds\n"
    "v_lshrrev_b32_e64 v64, 2, v0\n"
    "v_mfma_f32_16x16x32_bf16 v[192:195], a[244:247], v[56:59], v[192:195]\n"
    "v_lshl_add_u32 v244, v64, 10, v244\n"
    "s_lshl_b32 s93, s45, 8\n"
    "v_mfma_f32_16x16x32_bf16 v[128:131], v[48:51], a[248:251], v[128:131]\n"
    "s_add_u32 m0, s48, 128000\n"
    "v_mfma_f32_16x16x32_bf16 v[232:235], v[60:63], a[248:251], v[232:235]\n"
    "buffer_load_dwordx4 v249, s[12:15], s53 offen offset:0 lds\n"
    "v_mfma_f32_16x16x32_bf16 v[160:163], v[52:55], a[248:251], v[160:163]\n"
    "v_mfma_f32_16x16x32_bf16 v[196:199], a[248:251], v[56:59], v[196:199]\n"
    "s_add_u32 m0, s48, 134144\n"
    "v_mfma_f32_16x16x32_bf16 v[164:167], v[52:55], a[252:255], v[164:167]\n"
    "buffer_load_dwordx4 v248, s[12:15], s53 offen offset:1024 lds\n"
    "v_mfma_f32_16x16x32_bf16 v[200:203], a[252:255], v[56:59], v[200:203]\n"
    "s_waitcnt lgkmcnt(0)\n"
    "v_mfma_f32_16x16x32_bf16 v[100:103], v[32:35], a[184:187], v[100:103]\n"
    "ds_read_b64_tr_b16 v[48:49], v74 offset:28672\n"
    "v_mfma_f32_16x16x32_bf16 v[204:207], v[44:47], a[184:187], v[204:207]\n"
    "ds_read_b64_tr_b16 v[50:51], v75 offset:29184\n"
    "v_mfma_f32_16x16x32_bf16 v[132:135], v[36:39], a[184:187], v[132:135]\n"
    "ds_read_b64_tr_b16 v[52:53], v74 offset:36864\n"
    "v_mfma_f32_16x16x32_bf16 v[168:171], a[184:187], v[40:43], v[168:171]\n"
    "v_mfma_f32_16x16x32_bf16 v[104:107], v[32:35], a[188:191], v[104:107]\n"
    "ds_read_b64_tr_b16 v[54:55], v75 offset:37376\n"
    "s_add_u32 m0, s48, 135168\n"
    "v_mfma_f32_16x16x32_bf16 v[208:211], v[44:47], a[188:191], v[208:211]\n"
    "buffer_load_dwordx4 v249, s[12:15], s53 offen offset:1024 lds\n"
    "ds_read_b64_tr_b16 v[56:57], v74 offset:45056\n"
    "v_mfma_f32_16x16x32_bf16 v[136:139], v[36:39], a[188:191], v[136:139]\n"
    "v_mfma_f32_16x16x32_bf16 v[172:175], a[188:191], v[40:43], v[172:175]\n"
    "ds_read_b64_tr_b16 v[58:59], v75 offset:45568\n"
    "v_mfma_f32_16x16x32_bf16 v[108:111], v[32:35], a[192:195], v[108:111]\n"
    "ds_read_b64_tr_b16 v[60:61], v74 offset:53248\n"
    "v_mfma_f32_16x16x32_bf16 v[212:215], v[44:47], a[192:195], v[212:215]\n"
    "ds_read_b64_tr_b16 v[62:63], v75 offset:53760\n"
    "v_mfma_f32_16x16x32_bf16 v[140:143], v[36:39], a[192:195], v[140:143]\n"
    "v_mfma_f32_16x16x32_bf16 v[176:179], a[192:195], v[40:43], v[176:179]\n"
    "ds_read_b128 a[220:223], v71 offset:6144\n"
    "v_mfma_f32_16x16x32_bf16 v[112:115], v[32:35], a[196:199], v[112:115]\n"
    "ds_read_b128 a[224:227], v71 offset:8192\n"
    "v_mfma_f32_16x16x32_bf16 v[216:219], v[44:47], a[196:199], v[216:219]\n"
    "v_mfma_f32_16x16x32_bf16 v[144:147], v[36:39], a[196:199], v[144:147]\n"
    "ds_read_b128 a[228:231], v71 offset:10240\n"
    "v_mfma_f32_16x16x32_bf16 v[180:183], a[196:199], v[40:43], v[180:183]\n"
    "ds_read_b128 a[232:235], v71 offset:12288\n"
    "v_mfma_f32_16x16x32_bf16 v[116:119], v[32:35], a[200:203], v[116:119]\n"
    "ds_read_b128 a[236:239], v71 offset:14336\n"
    "s_add_u32 m0, s48, 141312\n"
    "v_mfma_f32_16x16x32_bf16 v[220:223], v[44:47], a[200:203], v[220:223]\n"
    "buffer_load_dwordx4 v248, s[12:15], s53 offen offset:2048 lds\n"
    "v_mfma_f32_16x16x32_bf16 v[148:151], v[36:39], a[200:203], v[148:151]\n"
    "ds_read_b128 a[240:243], v71 offset:16384\n"
    "v_mfma_f32_16x16x32_bf16 v[184:187], a[200:203], v[40:43], v[184:187]\n"
    "ds_read_b128 a[244:247], v71 offset:18432\n"
    "v_mfma_f32_16x16x32_bf16 v[120:123], v[32:35], a[204:207], v[120:123]\n"
    "v_mfma_f32_16x16x32_bf16 v[224:227], v[44:47], a[204:207], v[224:227]\n"
    "ds_read_b128 a[248:251], v71 offset:20480\n"
    "v_mfma_f32_16x16x32_bf16 v[152:155], v[36:39], a[204:207], v[152:155]\n"
    "ds_read_b128 a[252:255], v71 offset:22528\n"
    "v_mfma_f32_16x16x32_bf16 v[188:191], a[204:207], v[40:43], v[188:191]\n"
    "v_mfma_f32_16x16x32_bf16 v[124:127], v[32:35], a[208:211], v[124:127]\n"
    "v_mfma_f32_16x16x32_bf16 v[228:231], v[44:47], a[208:211], v[228:231]\n"
    "v_mfma_f32_16x16x32_bf16 v[156:159], v[36:39], a[208:211], v[156:159]\n"
    "v_mfma_f32_16x16x32_bf16 v[192:195], a[208:211], v[40:43], v[192:195]\n"
    "s_add_u32 m0, s48, 142336\n"
    "v_mfma_f32_16x16x32_bf16 v[128:131], v[32:35], a[212:215], v[128:131]\n"
    "buffer_load_dwordx4 v249, s[12:15], s53 offen offset:2048 lds\n"
    "s_add_u32 s53, s53, 196608\n"
    "v_mfma_f32_16x16x32_bf16 v[232:235], v[44:47], a[212:215], v[232:235]\n"
    "v_mfma_f32_16x16x32_bf16 v[160:163], v[36:39], a[212:215], v[160:163]\n"
    "v_mfma_f32_16x16x32_bf16 v[196:199], a[212:215], v[40:43], v[196:199]\n"
    "v_mfma_f32_16x16x32_bf16 v[164:167], v[36:39], a[216:219], v[164:167]\n"
    "v_mfma_f32_16x16x32_bf16 v[200:203], a[216:219], v[40:43], v[200:203]\n"
    "s_waitcnt vmcnt(12) lgkmcnt(0)\n"
    ST(19)
    "s_barrier\n"
    "v_mfma_f32_16x16x32_bf16 v[100:103], v[48:51], a[220:223], v[100:103]\n"
    "ds_read_b64_tr_b16 v[32:33], v76 offset:10240\n"
    "v_mfma_f32_16x16x32_bf16 v[204:207], v[60:63], a[220:223], v[204:207]\n"
    "ds_read_b64_tr_b16 v[34:35], v77 offset:10752\n"
    "s_cmp_eq_u32 s3, 6\n"
    "s_cselect_b32 s63, 1024, 0\n"
    "s_sub_u32 s51, s51, s63\n"
    "s_sub_u32 s52, s52, s63\n"
    "s_mul_i32 s66, s63, 1536\n"
    "s_sub_u32 s53, s53, s66\n"
    "s_mul_i32 s66, s63, 1792\n"
    "s_sub_u32 s54, s54, s66\n"
    "v_mfma_f32_16x16x32_bf16 v[132:135], v[52:55], a[220:223], v[132:135]\n"
    "ds_read_b64_tr_b16 v[36:37], v76 offset:18432\n"
    "v_mfma_f32_16x16x32_bf16 v[168:171], a[220:223], v[56:59], v[168:171]\n"
    "s_add_u32 m0, s49, 5376\n"
    "v_mfma_f32_16x16x32_bf16 v[104:107], v[48:51], a[224:227], v[104:107]\n"
    "buffer_load_dwordx4 v252, s[4:7], s51 offen offset:768 lds\n"
    "ds_read_b64_tr_b16 v[38:39], v77 offset:18944\n"
    "v_mfma_f32_16x16x32_bf16 v[208:211], v[60:63], a[224:227], v[208:211]\n"
    "ds_read_b64_tr_b16 v[40:41], v76 offset:26624\n"
    "v_mfma_f32_16x16x32_bf16 v[136:139], v[52:55], a[224:227], v[136:139]\n"
    "s_add_u32 m0, s49, 6400\n"
    "v_mfma_f32_16x16x32_bf16 v[172:175], a[224:227], v[56:59], v[172:175]\n"
    "buffer_load_dwordx4 v253, s[4:7], s51 offen offset:768 lds\n"
    "ds_read_b64_tr_b16 v[42:43], v77 offset:27136\n"
    "v_mfma_f32_16x16x32_bf16 v[108:111], v[48:51], a[228:231], v[108:111]\n"
    "ds_read_b64_tr_b16 v[44:45], v76 offset:34816\n"
    "v_mfma_f32_16x16x32_bf16 v[212:215], v[60:63], a[228:231], v[212:215]\n"
    "ds_read_b64_tr_b16 v[46:47], v77 offset:35328\n"
    "s_add_u32 m0, s49, 6400\n"
    "v_mfma_f32_16x16x32_bf16 v[140:143], v[52:55], a[228:231], v[140:143]\n"
    "buffer_load_dwordx4 v252, s[4:7], s51 offen offset:1792 lds\n"
    "v_mfma_f32_16x16x32_bf16 v[176:179], a[228:231], v[56:59], v[176:179]\n"
    "ds_read_b128 a[184:187], v11 offset:57344\n"
    "v_mfma_f32_16x16x32_bf16 v[112:115], v[48:51], a[232:235], v[112:115]\n"
    "ds_read_b128 a[188:191], v11 offset:59392\n"
    "s_add_u32 m0, s49, 7424\n"
    "v_mfma_f32_16x16x32_bf16 v[216:219], v[60:63], a[232:235], v[216:219]\n"
    "buffer_load_dwordx4 v253, s[4:7], s51 offen offset:1792 lds\n"
    "v_mfma_f32_16x16x32_bf16 v[144:147], v[52:55], a[232:235], v[144:147]\n"
    "ds_read_b128 a[192:195], v11 offset:61440\n"
    "v_mfma_f32_16x16x32_bf16 v[180:183], a[232:235], v[56:59], v[180:183]\n"
    "ds_read_b128 a[196:199], v11 offset:63488\n"
    "v_mfma_f32_16x16x32_bf16 v[116:119], v[48:51], a[236:239], v[116:119]\n"
    "ds_read_b128 a[200:203], v72 offset:0\n"
    "v_mfma_f32_16x16x32_bf16 v[220:223], v[60:63], a[236:239], v[220:223]\n"
    "s_cmp_gt_u32 s47, 1024\n"
    "s_cbranch_scc1 Lnh_11_%=\n"
    "s_add_u32 m0, s47, 21760\n"
    "s_nop 0\n"
    "buffer_load_dwordx4 v254, s[4:7], s52 offen offset:768 lds\n"
    "Lnh_11_%=:\n"
    "v_mfma_f32_16x16x32_bf16 v[148:151], v[52:55], a[236:239], v[148:151]\n"
    "ds_read_b128 a[204:207], v72 offset:2048\n"
    "v_mfma_f32_16x16x32_bf16 v[184:187], a[236:239], v[56:59], v[184:187]\n"
    "ds_read_b128 a[208:211], v72 offset:4096\n"
    "s_add_u32 m0, s48, 49152\n"
    "v_mfma_f32_16x16x32_bf16 v[120:123], v[48:51], a[240:243], v[120:123]\n"
    "buffer_load_dwordx4 v250, s[16:19], s54 offen offset:0 lds\n"
    "v_mfma_f32_16x16x32_bf16 v[224:227], v[60:63], a[240:243], v[224:227]\n"
    "ds_read_b128 a[212:215], v72 offset:6144\n"
    "v_mfma_f32_16x16x32_bf16 v[152:155], v[52:55], a[240:243], v[152:155]\n"
    "ds_read_b128 a[216:219], v72 offset:8192\n"
    "s_add_u32 m0, s48, 50176\n"
    "v_mfma_f32_16x16x32_bf16 v[188:191], a[240:243], v[56:59], v[188:191]\n"
    "buffer_load_dwordx4 v251, s[16:19], s54 offen offset:0 lds\n"
    "s_add_u32 s54, s54, 229376\n"
    "v_mfma_f32_16x16x32_bf16 v[124:127], v[48:51], a[244:247], v[124:127]\n"
    "v_mfma_f32_16x16x32_bf16 v[228:231], v[60:63], a[244:247], v[228:231]\n"
    "s_add_u32 m0, s48, 24576\n"
    "v_mfma_f32_16x16x32_bf16 v[156:159], v[52:55], a[244:247], v[156:159]\n"
    "buffer_load_dwordx4 v248, s[12:15], s53 offen offset:0 lds\n"
    "v_mfma_f32_16x16x32_bf16 v[192:195], a[244:247], v[56:59], v[192:195]\n"
    "v_mfma_f32_16x16x32_bf16 v[128:131], v[48:51], a[248:251], v[128:131]\n"
    "s_add_u32 m0, s48, 25600\n"
    "v_mfma_f32_16x16x32_bf16 v[232:235], v[60:63], a[248:251], v[232:235]\n"
    "buffer_load_dwordx4 v249, s[12:15], s53 offen offset:0 lds\n"
    "v_mfma_f32_16x16x32_bf16 v[160:163], v[52:55], a[248:251], v[160:163]\n"
    "v_mfma_f32_16x16x32_bf16 v[196:199], a[248:251], v[56:59], v[196:199]\n"
    "s_add_u32 m0, s48, 31744\n"
    "v_mfma_f32_16x16x32_bf16 v[164:167], v[52:55], a[252:255], v[164:167]\n"
    "buffer_load_dwordx4 v248, s[12:15], s53 offen offset:1024 lds\n"
    "v_mfma_f32_16x16x32_bf16 v[200:203], a[252:255], v[56:59], v[200:203]\n"
    "s_waitcnt lgkmcnt(0)\n"
    "v_mfma_f32_16x16x32_bf16 v[100:103], v[32:35], a[184:187], v[100:103]\n"
    "ds_read_b64_tr_b16 v[48:49], v76 offset:14336\n"
    "v_mfma_f32_16x16x32_bf16 v[204:207], v[44:47], a[184:187], v[204:207]\n"
    "ds_read_b64_tr_b16 v[50:51], v77 offset:14848\n"
    "v_mfma_f32_16x16x32_bf16 v[132:135], v[36:39], a[184:187], v[132:135]\n"
    "ds_read_b64_tr_b16 v[52:53], v76 offset:22528\n"
    "v_mfma_f32_16x16x32_bf16 v[168:171], a[184:187], v[40:43], v[168:171]\n"
    "v_mfma_f32_16x16x32_bf16 v[104:107], v[32:35], a[188:191], v[104:107]\n"
    "ds_read_b64_tr_b16 v[54:55], v77 offset:23040\n"
    "s_add_u32 m0, s48, 32768\n"
    "v_mfma_f32_16x16x32_bf16 v[208:211], v[44:47], a[188:191], v[208:211]\n"
    "buffer_load_dwordx4 v249, s[12:15], s53 offen offset:1024 lds\n"
    "ds_read_b64_tr_b16 v[56:57], v76 offset:30720\n"
    "v_mfma_f32_16x16x32_bf16 v[136:139], v[36:39], a[188:191], v[136:139]\n"
    "v_mfma_f32_16x16x32_bf16 v[172:175], a[188:191], v[40:43], v[172:175]\n"
    "ds_read_b64_tr_b16 v[58:59], v77 offset:31232\n"
    "v_mfma_f32_16x16x32_bf16 v[108:111], v[32:35], a[192:195], v[108:111]\n"
    "ds_read_b64_tr_b16 v[60:61], v76 offset:38912\n"
    "v_mfma_f32_16x16x32_bf16 v[212:215], v[44:47], a[192:195], v[212:215]\n"
    "ds_read_b64_tr_b16 v[62:63], v77 offset:39424\n"
    "v_mfma_f32_16x16x32_bf16 v[140:143], v[36:39], a[192:195], v[140:143]\n"
    "v_mfma_f32_16x16x32_bf16 v[176:179], a[192:195], v[40:43], v[176:179]\n"
    "ds_read_b128 a[220:223], v71 offset:57344\n"
    "v_mfma_f32_16x16x32_bf16 v[112:115], v[32:35], a[196:199], v[112:115]\n"
    "ds_read_b128 a[224:227], v71 offset:59392\n"
    "v_mfma_f32_16x16x32_bf16 v[216:219], v[44:47], a[196:199], v[216:219]\n"
    "v_mfma_f32_16x16x32_bf16 v[144:147], v[36:39], a[196:199], v[144:147]\n"
    "ds_read_b128 a[228:231], v71 offset:61440\n"
    "v_mfma_f32_16x16x32_bf16 v[180:183], a[196:199], v[40:43], v[180:183]\n"
    "ds_read_b128 a[232:235], v71 offset:63488\n"
    "v_mfma_f32_16x16x32_bf16 v[116:119], v[32:35], a[200:203], v[116:119]\n"
    "ds_read_b128 a[236:239], v73 offset:0\n"
    "s_add_u32 m0, s48, 38912\n"
    "v_mfma_f32_16x16x32_bf16 v[220:223], v[44:47], a[200:203], v[220:223]\n"
    "buffer_load_dwordx4 v248, s[12:15], s53 offen offset:2048 lds\n"
    "v_mfma_f32_16x16x32_bf16 v[148:151], v[36:39], a[200:203], v[148:151]\n"
    "ds_read_b128 a[240:243], v73 offset:2048\n"
    "v_mfma_f32_16x16x32_bf16 v[184:187], a[200:203], v[40:43], v[184:187]\n"
    "ds_read_b128 a[244:247], v73 offset:4096\n"
    "v_mfma_f32_16x16x32_bf16 v[120:123], v[32:35], a[204:207], v[120:123]\n"
    "v_mfma_f32_16x16x32_bf16 v[224:227], v[44:47], a[204:207], v[224:227]\n"
    "ds_read_b128 a[248:251], v73 offset:6144\n"
    "v_mfma_f32_16x16x32_bf16 v[152:155], v[36:39], a[204:207], v[152:155]\n"
    "ds_read_b128 a[252:255], v73 offset:8192\n"
    "v_mfma_f32_16x16x32_bf16 v[188:191], a[204:207], v[40:43], v[188:191]\n"
    "v_mfma_f32_16x16x32_bf16 v[124:127], v[32:35], a[208:211], v[124:127]\n"
    "v_mfma_f32_16x16x32_bf16 v[228:231], v[44:47], a[208:211], v[228:231]\n"
    "v_mfma_f32_16x16x32_bf16 v[156:159], v[36:39], a[208:211], v[156:159]\n"
    "v_mfma_f32_16x16x32_bf16 v[192:195], a[208:211], v[40:43], v[192:195]\n"
    "s_add_u32 m0, s48, 39936\n"
    "v_mfma_f32_16x16x32_bf16 v[128:131], v[32:35], a[212:215], v[128:131]\n"
    "buffer_load_dwordx4 v249, s[12:15], s53 offen offset:2048 lds\n"
    "s_add_u32 s53, s53, 196608\n"
    "v_mfma_f32_16x16x32_bf16 v[232:235], v[44:47], a[212:215], v[232:235]\n"
    "v_mfma_f32_16x16x32_bf16 v[160:163], v[36:39], a[212:215], v[160:163]\n"
    "v_mfma_f32_16x16x32_bf16 v[196:199], a[212:215], v[40:43], v[196:199]\n"
    "v_mfma_f32_16x16x32_bf16 v[164:167], v[36:39], a[216:219], v[164:167]\n"
    "v_mfma_f32_16x16x32_bf16 v[200:203], a[216:219], v[40:43], v[200:203]\n"
    "s_waitcnt vmcnt(12) lgkmcnt(0)\n"
    ST(20)
    "s_barrier\n"
    "v_mfma_f32_16x16x32_bf16 v[100:103], v[48:51], a[220:223], v[100:103]\n"
    "ds_read_b64_tr_b16 v[32:33], v76 offset:61440\n"
    "v_mfma_f32_16x16x32_bf16 v[204:207], v[60:63], a[220:223], v[204:207]\n"
    "ds_read_b64_tr_b16 v[34:35], v77 offset:61952\n"
    "s_cmp_eq_u32 s3, 7\n"
    "s_cselect_b32 s63, 1024, 0\n"
    "s_sub_u32 s51, s51, s63\n"
    "s_sub_u32 s52, s52, s63\n"
    "s_mul_i32 s66, s63, 1536\n"
    "s_sub_u32 s53, s53, s66\n"
    "s_mul_i32 s66, s63, 1792\n"
    "s_sub_u32 s54, s54, s66\n"
    "v_mfma_f32_16x16x32_bf16 v[132:135], v[52:55], a[220:223], v[132:135]\n"
    "ds_read_b64_tr_b16 v[36:37], v78 offset:4096\n"
    "v_mfma_f32_16x16x32_bf16 v[168:171], a[220:223], v[56:59], v[168:171]\n"
    "s_add_u32 m0, s49, 56448\n"
    "v_mfma_f32_16x16x32_bf16 v[104:107], v[48:51], a[224:227], v[104:107]\n"
    "buffer_load_dwordx4 v252, s[4:7], s51 offen offset:896 lds\n"
    "ds_read_b64_tr_b16 v[38:39], v79 offset:4608\n"
    "v_mfma_f32_16x16x32_bf16 v[208:211], v[60:63], a[224:227], v[208:211]\n"
    "ds_read_b64_tr_b16 v[40:41], v78 offset:12288\n"
    "v_mfma_f32_16x16x32_bf16 v[136:139], v[52:55], a[224:227], v[136:139]\n"
    "s_add_u32 m0, s49, 57472\n"
    "v_mfma_f32_16x16x32_bf16 v[172:175], a[224:227], v[56:59], v[172:175]\n"
    "buffer_load_dwordx4 v253, s[4:7], s51 offen offset:896 lds\n"
    "ds_read_b64_tr_b16 v[42:43], v79 offset:12800\n"
    "v_mfma_f32_16x16x32_bf16 v[108:111], v[48:51], a[228:231], v[108:111]\n"
    "ds_read_b64_tr_b16 v[44:45], v78 offset:20480\n"
    "v_mfma_f32_16x16x32_bf16 v[212:215], v[60:63], a[228:231], v[212:215]\n"
    "ds_read_b64_tr_b16 v[46:47], v79 offset:20992\n"
    "s_add_u32 m0, s49, 57472\n"
    "v_mfma_f32_16x16x32_bf16 v[140:143], v[52:55], a[228:231], v[140:143]\n"
    "buffer_load_dwordx4 v252, s[4:7], s51 offen offset:1920 lds\n"
    "v_mfma_f32_16x16x32_bf16 v[176:179], a[228:231], v[56:59], v[176:179]\n"
    "ds_read_b128 a[184:187], v72 offset:43008\n"
    "v_mfma_f32_16x16x32_bf16 v[112:115], v[48:51], a[232:235], v[112:115]\n"
    "ds_read_b128 a[188:191], v72 offset:45056\n"
    "s_add_u32 m0, s49, 58496\n"
    "v_mfma_f32_16x16x32_bf16 v[216:219], v[60:63], a[232:235], v[216:219]\n"
    "buffer_load_dwordx4 v253, s[4:7], s51 offen offset:1920 lds\n"
    "v_mfma_f32_16x16x32_bf16 v[144:147], v[52:55], a[232:235], v[144:147]\n"
    "ds_read_b128 a[192:195], v72 offset:47104\n"
    "v_mfma_f32_16x16x32_bf16 v[180:183], a[232:235], v[56:59], v[180:183]\n"
    "ds_read_b128 a[196:199], v72 offset:49152\n"
    "v_mfma_f32_16x16x32_bf16 v[116:119], v[48:51], a[236:239], v[116:119]\n"
    "ds_read_b128 a[200:203], v72 offset:51200\n"
    "v_mfma_f32_16x16x32_bf16 v[220:223], v[60:63], a[236:239], v[220:223]\n"
    "s_cmp_gt_u32 s47, 1024\n"
    "s_cbranch_scc1 Lnh_12_%=\n"
    "s_add_u32 m0, s47, 72832\n"
    "s_nop 0\n"
    "buffer_load_dwordx4 v254, s[4:7], s52 offen offset:896 lds\n"
    "Lnh_12_%=:\n"
    "v_mfma_f32_16x16x32_bf16 v[148:151], v[52:55], a[236:239], v[148:151]\n"
    "ds_read_b128 a[204:207], v72 offset:53248\n"
    "v_mfma_f32_16x16x32_bf16 v[184:187], a[236:239], v[56:59], v[184:187]\n"
    "ds_read_b128 a[208:211], v72 offset:55296\n"
    "s_add_u32 m0, s48, 100352\n"
    "v_mfma_f32_16x16x32_bf16 v[120:123], v[48:51], a[240:243], v[120:123]\n"
    "buffer_load_dwordx4 v250, s[16:19], s54 offen offset:0 lds\n"
    "v_mfma_f32_16x16x32_bf16 v[224:227], v[60:63], a[240:243], v[224:227]\n"
    "ds_read_b128 a[212:215], v72 offset:57344\n"
    "v_mfma_f32_16x16x32_bf16 v[152:155], v[52:55], a[240:243], v[152:155]\n"
    "ds_read_b128 a[216:219], v72 offset:59392\n"
    "s_add_u32 m0, s48, 101376\n"
    "v_mfma_f32_16x16x32_bf16 v[188:191], a[240:243], v[56:59], v[188:191]\n"
    "buffer_load_dwordx4 v251, s[16:19], s54 offen offset:0 lds\n"
    "s_add_u32 s54, s54, 229376\n"
    "v_mfma_f32_16x16x32_bf16 v[124:127], v[48:51], a[244:247], v[124:127]\n"
    "v_mfma_f32_16x16x32_bf16 v[228:231], v[60:63], a[244:247], v[228:231]\n"
    "s_add_u32 m0, s48, 75776\n"
    "v_mfma_f32_16x16x32_bf16 v[156:159], v[52:55], a[244:247], v[156:159]\n"
    "buffer_load_dwordx4 v248, s[12:15], s53 offen offset:0 lds\n"
    "v_mfma_f32_16x16x32_bf16 v[192:195], a[244:247], v[56:59], v[192:195]\n"
    "v_mfma_f32_16x16x32_bf16 v[128:131], v[48:51], a[248:251], v[128:131]\n"
    "s_add_u32 m0, s48, 76800\n"
    "v_mfma_f32_16x16x32_bf16 v[232:235], v[60:63], a[248:251], v[232:235]\n"
    "buffer_load_dwordx4 v249, s[12:15], s53 offen offset:0 lds\n"
    "v_mfma_f32_16x16x32_bf16 v[160:163], v[52:55], a[248:251], v[160:163]\n"
    "v_mfma_f32_16x16x32_bf16 v[196:199], a[248:251], v[56:59], v[196:199]\n"
    "s_add_u32 m0, s48, 82944\n"
    "v_mfma_f32_16x16x32_bf16 v[164:167], v[52:55], a[252:255], v[164:167]\n"
    "buffer_load_dwordx4 v248, s[12:15], s53 offen offset:1024 lds\n"
    "v_mfma_f32_16x16x32_bf16 v[200:203], a[252:255], v[56:59], v[200:203]\n"
    "s_waitcnt lgkmcnt(0)\n"
    "v_mfma_f32_16x16x32_bf16 v[100:103], v[32:35], a[184:187], v[100:103]\n"
    "ds_read_b64_tr_b16 v[48:49], v78 offset:0\n"
    "v_mfma_f32_16x16x32_bf16 v[204:207], v[44:47], a[184:187], v[204:207]\n"
    "ds_read_b64_tr_b16 v[50:51], v79 offset:512\n"
    "v_mfma_f32_16x16x32_bf16 v[132:135], v[36:39], a[184:187], v[132:135]\n"
    "ds_read_b64_tr_b16 v[52:53], v78 offset:8192\n"
    "s_add_u32 m0, s48, 83968\n"
    "v_mfma_f32_16x16x32_bf16 v[168:171], a[184:187], v[40:43], v[168:171]\n"
    "buffer_load_dwordx4 v249, s[12:15], s53 offen offset:1024 lds\n"
    "v_mfma_f32_16x16x32_bf16 v[104:107], v[32:35], a[188:191], v[104:107]\n"
    "ds_read_b64_tr_b16 v[54:55], v79 offset:8704\n"
    "v_mfma_f32_16x16x32_bf16 v[208:211], v[44:47], a[188:191], v[208:211]\n"
    "ds_read_b64_tr_b16 v[56:57], v78 offset:16384\n"
    "v_mfma_f32_16x16x32_bf16 v[136:139], v[36:39], a[188:191], v[136:139]\n"
    "v_mfma_f32_16x16x32_bf16 v[172:175], a[188:191], v[40:43], v[172:175]\n"
    "ds_read_b64_tr_b16 v[58:59], v79 offset:16896\n"
    "v_mfma_f32_16x16x32_bf16 v[108:111], v[32:35], a[192:195], v[108:111]\n"
    "ds_read_b64_tr_b16 v[60:61], v78 offset:24576\n"
    "v_mfma_f32_16x16x32_bf16 v[212:215], v[44:47], a[192:195], v[212:215]\n"
    "ds_read_b64_tr_b16 v[62:63], v79 offset:25088\n"
    "s_add_u32 m0, s48, 90112\n"
    "v_mfma_f32_16x16x32_bf16 v[140:143], v[36:39], a[192:195], v[140:143]\n"
    "buffer_load_dwordx4 v248, s[12:15], s53 offen offset:2048 lds\n"
    "v_mfma_f32_16x16x32_bf16 v[176:179], a[192:195], v[40:43], v[176:179]\n"
    "ds_read_b128 a[220:223], v73 offset:43008\n"
    "v_mfma_f32_16x16x32_bf16 v[112:115], v[32:35], a[196:199], v[112:115]\n"
    "ds_read_b128 a[224:227], v73 offset:45056\n"
    "v_mfma_f32_16x16x32_bf16 v[216:219], v[44:47], a[196:199], v[216:219]\n"
    "v_mfma_f32_16x16x32_bf16 v[144:147], v[36:39], a[196:199], v[144:147]\n"
    "ds_read_b128 a[228:231], v73 offset:47104\n"
    "v_mfma_f32_16x16x32_bf16 v[180:183], a[196:199], v[40:43], v[180:183]\n"
    "ds_read_b128 a[232:235], v73 offset:49152\n"
    "v_mfma_f32_16x16x32_bf16 v[116:119], v[32:35], a[200:203], v[116:119]\n"
    "ds_read_b128 a[236:239], v73 offset:51200\n"
    "s_add_u32 m0, s48, 91136\n"
    "v_mfma_f32_16x16x32_bf16 v[220:223], v[44:47], a[200:203], v[220:223]\n"
    "buffer_load_dwordx4 v249, s[12:15], s53 offen offset:2048 lds\n"
    "s_add_u32 s53, s53, 196608\n"
    "v_mfma_f32_16x16x32_bf16 v[148:151], v[36:39], a[200:203], v[148:151]\n"
    "ds_read_b128 a[240:243], v73 offset:53248\n"
    "v_mfma_f32_16x16x32_bf16 v[184:187], a[200:203], v[40:43], v[184:187]\n"
    "ds_read_b128 a[244:247], v73 offset:55296\n"
    "v_mfma_f32_16x16x32_bf16 v[120:123], v[32:35], a[204:207], v[120:123]\n"
    "v_mfma_f32_16x16x32_bf16 v[224:227], v[44:47], a[204:207], v[224:227]\n"
    "ds_read_b128 a[248:251], v73 offset:57344\n"
    "v_mfma_f32_16x16x32_bf16 v[152:155], v[36:39], a[204:207], v[152:155]\n"
    "ds_read_b128 a[252:255], v73 offset:59392\n"
    "v_mfma_f32_16x16x32_bf16 v[188:191], a[204:207], v[40:43], v[188:191]\n"
    "buffer_load_dwordx4 v[24:27], v80, s[8:11], 0 offen sc1\n"
    "v_mfma_f32_16x16x32_bf16 v[124:127], v[32:35], a[208:211], v[124:127]\n"
    "v_mfma_f32_16x16x32_bf16 v[228:231], v[44:47], a[208:211], v[228:231]\n"
    "v_mfma_f32_16x16x32_bf16 v[156:159], v[36:39], a[208:211], v[156:159]\n"
    "v_mfma_f32_16x16x32_bf16 v[192:195], a[208:211], v[40:43], v[192:195]\n"
    "v_mfma_f32_16x16x32_bf16 v[128:131], v[32:35], a[212:215], v[128:131]\n"
    "v_mfma_f32_16x16x32_bf16 v[232:235], v[44:47], a[212:215], v[232:235]\n"
    "v_mfma_f32_16x16x32_bf16 v[160:163], v[36:39], a[212:215], v[160:163]\n"
    "buffer_load_dwordx2 v[28:29], v82, s[8:11], 0 offen sc1\n"
    "v_mfma_f32_16x16x32_bf16 v[196:199], a[212:215], v[40:43], v[196:199]\n"
    "v_mfma_f32_16x16x32_bf16 v[164:167], v[36:39], a[216:219], v[164:167]\n"
    "v_mfma_f32_16x16x32_bf16 v[200:203], a[216:219], v[40:43], v[200:203]\n"
    "s_waitcnt vmcnt(14) lgkmcnt(0)\n"
    ST(21)
    "s_barrier\n"
    "v_mfma_f32_16x16x32_bf16 v[100:103], v[48:51], a[220:223], v[100:103]\n"
    "ds_read_b64_tr_b16 v[32:33], v74 offset:24576\n"
    "v_mfma_f32_16x16x32_bf16 v[204:207], v[60:63], a[220:223], v[204:207]\n"
    "ds_read_b64_tr_b16 v[34:35], v75 offset:25088\n"
    "v_mfma_f32_16x16x32_bf16 v[132:135], v[52:55], a[220:223], v[132:135]\n"
    "ds_read_b64_tr_b16 v[36:37], v74 offset:32768\n"
    "v_mfma_f32_16x16x32_bf16 v[168:171], a[220:223], v[56:59], v[168:171]\n"
    "v_mfma_f32_16x16x32_bf16 v[104:107], v[48:51], a[224:227], v[104:107]\n"
    "ds_read_b64_tr_b16 v[38:39], v75 offset:33280\n"
    "v_mfma_f32_16x16x32_bf16 v[208:211], v[60:63], a[224:227], v[208:211]\n"
    "ds_read_b64_tr_b16 v[40:41], v74 offset:40960\n"
    "v_mfma_f32_16x16x32_bf16 v[136:139], v[52:55], a[224:227], v[136:139]\n"
    "v_mfma_f32_16x16x32_bf16 v[172:175], a[224:227], v[56:59], v[172:175]\n"
    "ds_read_b64_tr_b16 v[42:43], v75 offset:41472\n"
    "v_mfma_f32_16x16x32_bf16 v[108:111], v[48:51], a[228:231], v[108:111]\n"
    "ds_read_b64_tr_b16 v[44:45], v74 offset:49152\n"
    "v_mfma_f32_16x16x32_bf16 v[212:215], v[60:63], a[228:231], v[212:215]\n"
    "ds_read_b64_tr_b16 v[46:47], v75 offset:49664\n"
    "v_mfma_f32_16x16x32_bf16 v[140:143], v[52:55], a[228:231], v[140:143]\n"
    "v_mfma_f32_16x16x32_bf16 v[176:179], a[228:231], v[56:59], v[176:179]\n"
    "ds_read_b128 a[184:187], v11 offset:6144\n"
    "v_mfma_f32_16x16x32_bf16 v[112:115], v[48:51], a[232:235], v[112:115]\n"
    "ds_read_b128 a[188:191], v11 offset:8192\n"
    "v_mfma_f32_16x16x32_bf16 v[216:219], v[60:63], a[232:235], v[216:219]\n"
    "v_mfma_f32_16x16x32_bf16 v[144:147], v[52:55], a[232:235], v[144:147]\n"
    "ds_read_b128 a[192:195], v11 offset:10240\n"
    "v_mfma_f32_16x16x32_bf16 v[180:183], a[232:235], v[56:59], v[180:183]\n"
    "ds_read_b128 a[196:199], v11 offset:12288\n"
    "v_mfma_f32_16x16x32_bf16 v[116:119], v[48:51], a[236:239], v[116:119]\n"
    "ds_read_b128 a[200:203], v11 offset:14336\n"
    "v_mfma_f32_16x16x32_bf16 v[220:223], v[60:63], a[236:239], v[220:223]\n"
    "v_mfma_f32_16x16x32_bf16 v[148:151], v[52:55], a[236:239], v[148:151]\n"
    "ds_read_b128 a[204:207], v11 offset:16384\n"
    "v_mfma_f32_16x16x32_bf16 v[184:187], a[236:239], v[56:59], v[184:187]\n"
    "ds_read_b128 a[208:211], v11 offset:18432\n"
    "v_mfma_f32_16x16x32_bf16 v[120:123], v[48:51], a[240:243], v[120:123]\n"
    "v_mfma_f32_16x16x32_bf16 v[224:227], v[60:63], a[240:243], v[224:227]\n"
    "ds_read_b128 a[212:215], v11 offset:20480\n"
    "v_mfma_f32_16x16x32_bf16 v[152:155], v[52:55], a[240:243], v[152:155]\n"
    "ds_read_b128 a[216:219], v11 offset:22528\n"
    "v_mfma_f32_16x16x32_bf16 v[188:191], a[240:243], v[56:59], v[188:191]\n"
    "v_mfma_f32_16x16x32_bf16 v[124:127], v[48:51], a[244:247], v[124:127]\n"
    "v_mfma_f32_16x16x32_bf16 v[228:231], v[60:63], a[244:247], v[228:231]\n"
    "v_mfma_f32_16x16x32_bf16 v[156:159], v[52:55], a[244:247], v[156:159]\n"
    "v_mfma_f32_16x16x32_bf16 v[192:195], a[244:247], v[56:59], v[192:195]\n"
    "v_mfma_f32_16x16x32_bf16 v[128:131], v[48:51], a[248:251], v[128:131]\n"
    "v_mfma_f32_16x16x32_bf16 v[232:235], v[60:63], a[248:251], v[232:235]\n"
    "v_mfma_f32_16x16x32_bf16 v[160:163], v[52:55], a[248:251], v[160:163]\n"
    "v_mfma_f32_16x16x32_bf16 v[196:199], a[248:251], v[56:59], v[196:199]\n"
    "v_mfma_f32_16x16x32_bf16 v[164:167], v[52:55], a[252:255], v[164:167]\n"
    "v_mfma_f32_16x16x32_bf16 v[200:203], a[252:255], v[56:59], v[200:203]\n"
    "v_add_u32 v252, 0x20000, v4\n"
    "v_add_u32 v253, 0x20000, v5\n"
    "s_waitcnt lgkmcnt(0)\n"
    "v_mfma_f32_16x16x32_bf16 v[100:103], v[32:35], a[184:187], v[100:103]\n"
    "ds_read_b64_tr_b16 v[48:49], v74 offset:28672\n"
    "v_mfma_f32_16x16x32_bf16 v[204:207], v[44:47], a[184:187], v[204:207]\n"
    "ds_read_b64_tr_b16 v[50:51], v75 offset:29184\n"
    "v_mfma_f32_16x16x32_bf16 v[132:135], v[36:39], a[184:187], v[132:135]\n"
    "ds_read_b64_tr_b16 v[52:53], v74 offset:36864\n"
    "v_mfma_f32_16x16x32_bf16 v[168:171], a[184:187], v[40:43], v[168:171]\n"
    "v_mfma_f32_16x16x32_bf16 v[104:107], v[32:35], a[188:191], v[104:107]\n"
    "ds_read_b64_tr_b16 v[54:55], v75 offset:37376\n"
    "v_mfma_f32_16x16x32_bf16 v[208:211], v[44:47], a[188:191], v[208:211]\n"
    "ds_read_b64_tr_b16 v[56:57], v74 offset:45056\n"
    "v_mfma_f32_16x16x32_bf16 v[136:139], v[36:39], a[188:191], v[136:139]\n"
    "v_mfma_f32_16x16x32_bf16 v[172:175], a[188:191], v[40:43], v[172:175]\n"
    "ds_read_b64_tr_b16 v[58:59], v75 offset:45568\n"
    "v_mfma_f32_16x16x32_bf16 v[108:111], v[32:35], a[192:195], v[108:111]\n"
    "ds_read_b64_tr_b16 v[60:61], v74 offset:53248\n"
    "v_mfma_f32_16x16x32_bf16 v[212:215], v[44:47], a[192:195], v[212:215]\n"
    "ds_read_b64_tr_b16 v[62:63], v75 offset:53760\n"
    "v_mfma_f32_16x16x32_bf16 v[140:143], v[36:39], a[192:195], v[140:143]\n"
    "v_mfma_f32_16x16x32_bf16 v[176:179], a[192:195], v[40:43], v[176:179]\n"
    "ds_read_b128 a[220:223], v71 offset:6144\n"
    "v_mfma_f32_16x16x32_bf16 v[112:115], v[32:35], a[196:199], v[112:115]\n"
    "ds_read_b128 a[224:227], v71 offset:8192\n"
    "v_mfma_f32_16x16x32_bf16 v[216:219], v[44:47], a[196:199], v[216:219]\n"
    "v_mfma_f32_16x16x32_bf16 v[144:147], v[36:39], a[196:199], v[144:147]\n"
    "ds_read_b128 a[228:231], v71 offset:10240\n"
    "v_mfma_f32_16x16x32_bf16 v[180:183], a[196:199], v[40:43], v[180:183]\n"
    "ds_read_b128 a[232:235], v71 offset:12288\n"
    "v_mfma_f32_16x16x32_bf16 v[116:119], v[32:35], a[200:203], v[116:119]\n"
    "ds_read_b128 a[236:239], v71 offset:14336\n"
    "s_add_u32 m0, s93, 159744\n"
    "v_mfma_f32_16x16x32_bf16 v[220:223], v[44:47], a[200:203], v[220:223]\n"
    "buffer_load_dword v244, s[20:23], s90 offen lds\n"
    "v_mfma_f32_16x16x32_bf16 v[148:151], v[36:39], a[200:203], v[148:151]\n"
    "ds_read_b128 a[240:243], v71 offset:16384\n"
    "v_mfma_f32_16x16x32_bf16 v[184:187], a[200:203], v[40:43], v[184:187]\n"
    "ds_read_b128 a[244:247], v71 offset:18432\n"
    "v_mfma_f32_16x16x32_bf16 v[120:123], v[32:35], a[204:207], v[120:123]\n"
    "v_mfma_f32_16x16x32_bf16 v[224:227], v[44:47], a[204:207], v[224:227]\n"
    "ds_read_b128 a[248:251], v71 offset:20480\n"
    "v_mfma_f32_16x16x32_bf16 v[152:155], v[36:39], a[204:207], v[152:155]\n"
    "ds_read_b128 a[252:255], v71 offset:22528\n"
    "v_mfma_f32_16x16x32_bf16 v[188:191], a[204:207], v[40:43], v[188:191]\n"
    "v_mfma_f32_16x16x32_bf16 v[124:127], v[32:35], a[208:211], v[124:127]\n"
    "v_mfma_f32_16x16x32_bf16 v[228:231], v[44:47], a[208:211], v[228:231]\n"
    "v_mfma_f32_16x16x32_bf16 v[156:159], v[36:39], a[208:211], v[156:159]\n"
    "v_mfma_f32_16x16x32_bf16 v[192:195], a[208:211], v[40:43], v[192:195]\n"
    "v_mfma_f32_16x16x32_bf16 v[128:131], v[32:35], a[212:215], v[128:131]\n"
    "v_mfma_f32_16x16x32_bf16 v[232:235], v[44:47], a[212:215], v[232:235]\n"
    "v_mfma_f32_16x16x32_bf16 v[160:163], v[36:39], a[212:215], v[160:163]\n"
    "v_mfma_f32_16x16x32_bf16 v[196:199], a[212:215], v[40:43], v[196:199]\n"
    "v_mfma_f32_16x16x32_bf16 v[164:167], v[36:39], a[216:219], v[164:167]\n"
    "v_mfma_f32_16x16x32_bf16 v[200:203], a[216:219], v[40:43], v[200:203]\n"
    "s_waitcnt vmcnt(1)\n"
    "s_waitcnt lgkmcnt(0)\n"
    ST(22)
    "s_barrier\n"
    "v_mfma_f32_16x16x32_bf16 v[100:103], v[48:51], a[220:223], v[100:103]\n"
    "v_cmp_ne_u32 vcc, s46, v25\n"
    "v_cmp_ne_u32 s[64:65], s46, v27\n"
    "v_mfma_f32_16x16x32_bf16 v[204:207], v[60:63], a[220:223], v[204:207]\n"
    "s_or_b64 vcc, vcc, s[64:65]\n"
    "s_add_u32 m0, s49, 108544\n"
    "v_mfma_f32_16x16x32_bf16 v[132:135], v[52:55], a[220:223], v[132:135]\n"
    "buffer_load_dwordx4 v2, s[20:23], s55 offen offset:0 lds\n"
    "v_cmp_ne_u32 s[64:65], s46, v29\n"
    "v_mfma_f32_16x16x32_bf16 v[168:171], a[220:223], v[56:59], v[168:171]\n"
    "s_or_b64 vcc, vcc, s[64:65]\n"
    "v_mfma_f32_16x16x32_bf16 v[104:107], v[48:51], a[224:227], v[104:107]\n"
    "s_cbranch_vccnz Lslow_13_%=\n"
    "Lback_14_%=:\n"
    "v_mfma_f32_16x16x32_bf16 v[208:211], v[60:63], a[224:227], v[208:211]\n"
    "ds_write_b32 v81, v24\n"
    "s_add_u32 m0, s49, 109568\n"
    "v_mfma_f32_16x16x32_bf16 v[136:139], v[52:55], a[224:227], v[136:139]\n"
    "buffer_load_dwordx4 v3, s[20:23], s55 offen offset:0 lds\n"
    "ds_write_b32 v81, v26 offset:1024\n"
    "v_mfma_f32_16x16x32_bf16 v[172:175], a[224:227], v[56:59], v[172:175]\n"
    "ds_write_b32 v83, v28 offset:2048\n"
    "v_mfma_f32_16x16x32_bf16 v[108:111], v[48:51], a[228:231], v[108:111]\n"
    "ds_read_b64_tr_b16 v[32:33], v76 offset:10240\n"
    "v_mfma_f32_16x16x32_bf16 v[212:215], v[60:63], a[228:231], v[212:215]\n"
    "ds_read_b64_tr_b16 v[34:35], v77 offset:10752\n"
    "s_add_u32 m0, s49, 110592\n"
    "v_mfma_f32_16x16x32_bf16 v[140:143], v[52:55], a[228:231], v[140:143]\n"
    "buffer_load_dwordx4 v2, s[20:23], s56 offen offset:0 lds\n"
    "ds_read_b64_tr_b16 v[36:37], v76 offset:18432\n"
    "v_mfma_f32_16x16x32_bf16 v[176:179], a[228:231], v[56:59], v[176:179]\n"
    "ds_read_b64_tr_b16 v[38:39], v77 offset:18944\n"
    "v_mfma_f32_16x16x32_bf16 v[112:115], v[48:51], a[232:235], v[112:115]\n"
    "ds_read_b64_tr_b16 v[40:41], v76 offset:26624\n"
    "ds_read_b64_tr_b16 v[42:43], v77 offset:27136\n"
    "v_mfma_f32_16x16x32_bf16 v[216:219], v[60:63], a[232:235], v[216:219]\n"
    "ds_read_b64_tr_b16 v[44:45], v76 offset:34816\n"
    "s_add_u32 m0, s49, 111616\n"
    "v_mfma_f32_16x16x32_bf16 v[144:147], v[52:55], a[232:235], v[144:147]\n"
    "buffer_load_dwordx4 v3, s[20:23], s56 offen offset:0 lds\n"
    "ds_read_b64_tr_b16 v[46:47], v77 offset:35328\n"
    "v_mfma_f32_16x16x32_bf16 v[180:183], a[232:235], v[56:59], v[180:183]\n"
    "ds_read_b128 a[184:187], v11 offset:57344\n"
    "v_mfma_f32_16x16x32_bf16 v[116:119], v[48:51], a[236:239], v[116:119]\n"
    "ds_read_b128 a[188:191], v11 offset:59392\n"
    "v_mfma_f32_16x16x32_bf16 v[220:223], v[60:63], a[236:239], v[220:223]\n"
    "ds_read_b128 a[192:195], v11 offset:61440\n"
    "v_mfma_f32_16x16x32_bf16 v[148:151], v[52:55], a[236:239], v[148:151]\n"
    "ds_read_b128 a[196:199], v11 offset:63488\n"
    "s_add_u32 m0, s49, 124928\n"
    "v_mfma_f32_16x16x32_bf16 v[184:187], a[236:239], v[56:59], v[184:187]\n"
    "buffer_load_dwordx4 v2, s[20:23], s57 offen offset:0 lds\n"
    "ds_read_b128 a[200:203], v72 offset:0\n"
    "v_mfma_f32_16x16x32_bf16 v[120:123], v[48:51], a[240:243], v[120:123]\n"
    "ds_read_b128 a[204:207], v72 offset:2048\n"
    "v_mfma_f32_16x16x32_bf16 v[224:227], v[60:63], a[240:243], v[224:227]\n"
    "ds_read_b128 a[208:211], v72 offset:4096\n"
    "v_mfma_f32_16x16x32_bf16 v[152:155], v[52:55], a[240:243], v[152:155]\n"
    "ds_read_b128 a[212:215], v72 offset:6144\n"
    "s_add_u32 m0, s49, 125952\n"
    "v_mfma_f32_16x16x32_bf16 v[188:191], a[240:243], v[56:59], v[188:191]\n"
    "buffer_load_dwordx4 v3, s[20:23], s57 offen offset:0 lds\n"
    "ds_read_b128 a[216:219], v72 offset:8192\n"
    "v_mfma_f32_16x16x32_bf16 v[124:127], v[48:51], a[244:247], v[124:127]\n"
    "v_mfma_f32_16x16x32_bf16 v[228:231], v[60:63], a[244:247], v[228:231]\n"
    "v_mfma_f32_16x16x32_bf16 v[156:159], v[52:55], a[244:247], v[156:159]\n"
    "s_add_u32 m0, s49, 126976\n"
    "v_mfma_f32_16x16x32_bf16 v[192:195], a[244:247], v[56:59], v[192:195]\n"
    "buffer_load_dwordx4 v2, s[20:23], s58 offen offset:0 lds\n"
    "v_mfma_f32_16x16x32_bf16 v[128:131], v[48:51], a[248:251], v[128:131]\n"
    "v_mfma_f32_16x16x32_bf16 v[232:235], v[60:63], a[248:251], v[232:235]\n"
    "v_mfma_f32_16x16x32_bf16 v[160:163], v[52:55], a[248:251], v[160:163]\n"
    "s_add_u32 m0, s49, 128000\n"
    "v_mfma_f32_16x16x32_bf16 v[196:199], a[248:251], v[56:59], v[196:199]\n"
    "buffer_load_dwordx4 v3, s[20:23], s58 offen offset:0 lds\n"
    "v_mfma_f32_16x16x32_bf16 v[164:167], v[52:55], a[252:255], v[164:167]\n"
    "v_mfma_f32_16x16x32_bf16 v[200:203], a[252:255], v[56:59], v[200:203]\n"
    "s_waitcnt lgkmcnt(0)\n"
    "v_mfma_f32_16x16x32_bf16 v[100:103], v[32:35], a[184:187], v[100:103]\n"
    "ds_read_b64_tr_b16 v[48:49], v76 offset:14336\n"
    "v_mfma_f32_16x16x32_bf16 v[204:207], v[44:47], a[184:187], v[204:207]\n"
    "ds_read_b64_tr_b16 v[50:51], v77 offset:14848\n"
    "s_add_u32 m0, s49, 141184\n"
    "v_mfma_f32_16x16x32_bf16 v[132:135], v[36:39], a[184:187], v[132:135]\n"
    "buffer_load_dwordx4 v2, s[20:23], s55 offen offset:128 lds\n"
    "ds_read_b64_tr_b16 v[52:53], v76 offset:22528\n"
    "v_mfma_f32_16x16x32_bf16 v[168:171], a[184:187], v[40:43], v[168:171]\n"
    "v_mfma_f32_16x16x32_bf16 v[104:107], v[32:35], a[188:191], v[104:107]\n"
    "ds_read_b64_tr_b16 v[54:55], v77 offset:23040\n"
    "v_mfma_f32_16x16x32_bf16 v[208:211], v[44:47], a[188:191], v[208:211]\n"
    "ds_read_b64_tr_b16 v[56:57], v76 offset:30720\n"
    "s_add_u32 m0, s49, 142208\n"
    "v_mfma_f32_16x16x32_bf16 v[136:139], v[36:39], a[188:191], v[136:139]\n"
    "buffer_load_dwordx4 v3, s[20:23], s55 offen offset:128 lds\n"
    "v_mfma_f32_16x16x32_bf16 v[172:175], a[188:191], v[40:43], v[172:175]\n"
    "ds_read_b64_tr_b16 v[58:59], v77 offset:31232\n"
    "v_mfma_f32_16x16x32_bf16 v[108:111], v[32:35], a[192:195], v[108:111]\n"
    "ds_read_b64_tr_b16 v[60:61], v76 offset:38912\n"
    "v_mfma_f32_16x16x32_bf16 v[212:215], v[44:47], a[192:195], v[212:215]\n"
    "ds_read_b64_tr_b16 v[62:63], v77 offset:39424\n"
    "s_add_u32 m0, s49, 143232\n"
    "v_mfma_f32_16x16x32_bf16 v[140:143], v[36:39], a[192:195], v[140:143]\n"
    "buffer_load_dwordx4 v2, s[20:23], s56 offen offset:128 lds\n"
    "v_mfma_f32_16x16x32_bf16 v[176:179], a[192:195], v[40:43], v[176:179]\n"
    "ds_read_b128 a[220:223], v71 offset:57344\n"
    "v_mfma_f32_16x16x32_bf16 v[112:115], v[32:35], a[196:199], v[112:115]\n"
    "ds_read_b128 a[224:227], v71 offset:59392\n"
    "v_mfma_f32_16x16x32_bf16 v[216:219], v[44:47], a[196:199], v[216:219]\n"
    "s_add_u32 m0, s49, 144256\n"
    "v_mfma_f32_16x16x32_bf16 v[144:147], v[36:39], a[196:199], v[144:147]\n"
    "buffer_load_dwordx4 v3, s[20:23], s56 offen offset:128 lds\n"
    "ds_read_b128 a[228:231], v71 offset:61440\n"
    "v_mfma_f32_16x16x32_bf16 v[180:183], a[196:199], v[40:43], v[180:183]\n"
    "ds_read_b128 a[232:235], v71 offset:63488\n"
    "v_mfma_f32_16x16x32_bf16 v[116:119], v[32:35], a[200:203], v[116:119]\n"
    "ds_read_b128 a[236:239], v73 offset:0\n"
    "v_mfma_f32_16x16x32_bf16 v[220:223], v[44:47], a[200:203], v[220:223]\n"
    "v_mfma_f32_16x16x32_bf16 v[148:151], v[36:39], a[200:203], v[148:151]\n"
    "ds_read_b128 a[240:243], v73 offset:2048\n"
    "s_add_u32 m0, s49, 6016\n"
    "v_mfma_f32_16x16x32_bf16 v[184:187], a[200:203], v[40:43], v[184:187]\n"
    "buffer_load_dwordx4 v2, s[20:23], s57 offen offset:128 lds\n"
    "ds_read_b128 a[244:247], v73 offset:4096\n"
    "v_mfma_f32_16x16x32_bf16 v[120:123], v[32:35], a[204:207], v[120:123]\n"
    "v_mfma_f32_16x16x32_bf16 v[224:227], v[44:47], a[204:207], v[224:227]\n"
    "ds_read_b128 a[248:251], v73 offset:6144\n"
    "v_mfma_f32_16x16x32_bf16 v[152:155], v[36:39], a[204:207], v[152:155]\n"
    "ds_read_b128 a[252:255], v73 offset:8192\n"
    "s_add_u32 m0, s49, 7040\n"
    "v_mfma_f32_16x16x32_bf16 v[188:191], a[204:207], v[40:43], v[188:191]\n"
    "buffer_load_dwordx4 v3, s[20:23], s57 offen offset:128 lds\n"
    "v_mfma_f32_16x16x32_bf16 v[124:127], v[32:35], a[208:211], v[124:127]\n"
    "v_mfma_f32_16x16x32_bf16 v[228:231], v[44:47], a[208:211], v[228:231]\n"
    "v_mfma_f32_16x16x32_bf16 v[156:159], v[36:39], a[208:211], v[156:159]\n"
    "s_add_u32 m0, s49, 8064\n"
    "v_mfma_f32_16x16x32_bf16 v[192:195], a[208:211], v[40:43], v[192:195]\n"
    "buffer_load_dwordx4 v2, s[20:23], s58 offen offset:128 lds\n"
    "v_mfma_f32_16x16x32_bf16 v[128:131], v[32:35], a[212:215], v[128:131]\n"
    "v_mfma_f32_16x16x32_bf16 v[232:235], v[44:47], a[212:215], v[232:235]\n"
    "v_mfma_f32_16x16x32_bf16 v[160:163], v[36:39], a[212:215], v[160:163]\n"
    "s_add_u32 m0, s49, 9088\n"
    "v_mfma_f32_16x16x32_bf16 v[196:199], a[212:215], v[40:43], v[196:199]\n"
    "buffer_load_dwordx4 v3, s[20:23], s58 offen offset:128 lds\n"
    "v_mfma_f32_16x16x32_bf16 v[164:167], v[36:39], a[216:219], v[164:167]\n"
    "v_mfma_f32_16x16x32_bf16 v[200:203], a[216:219], v[40:43], v[200:203]\n"
    "s_waitcnt lgkmcnt(0)\n"
    ST(23)
    "s_barrier\n"
    "v_mfma_f32_16x16x32_bf16 v[100:103], v[48:51], a[220:223], v[100:103]\n"
    "ds_read_b32 v32, v14 offset:0\n"
    "v_mfma_f32_16x16x32_bf16 v[204:207], v[60:63], a[220:223], v[204:207]\n"
    "ds_read_b32 v33, v14 offset:1024\n"
    "s_add_u32 m0, s49, 22272\n"
    "v_mfma_f32_16x16x32_bf16 v[132:135], v[52:55], a[220:223], v[132:135]\n"
    "buffer_load_dwordx4 v2, s[20:23], s55 offen offset:256 lds\n"
    "ds_read_b32 v34, v14 offset:64\n"
    "v_mfma_f32_16x16x32_bf16 v[168:171], a[220:223], v[56:59], v[168:171]\n"
    "ds_read_b32 v35, v14 offset:1088\n"
    "v_mfma_f32_16x16x32_bf16 v[104:107], v[48:51], a[224:227], v[104:107]\n"
    "ds_read_b32 v36, v14 offset:128\n"
    "v_mfma_f32_16x16x32_bf16 v[208:211], v[60:63], a[224:227], v[208:211]\n"
    "ds_read_b32 v37, v14 offset:1152\n"
    "s_add_u32 m0, s49, 23296\n"
    "v_mfma_f32_16x16x32_bf16 v[136:139], v[52:55], a[224:227], v[136:139]\n"
    "buffer_load_dwordx4 v3, s[20:23], s55 offen offset:256 lds\n"
    "ds_read_b32 v38, v14 offset:192\n"
    "v_mfma_f32_16x16x32_bf16 v[172:175], a[224:227], v[56:59], v[172:175]\n"
    "v_mfma_f32_16x16x32_bf16 v[108:111], v[48:51], a[228:231], v[108:111]\n"
    "ds_read_b32 v39, v14 offset:1216\n"
    "v_mfma_f32_16x16x32_bf16 v[212:215], v[60:63], a[228:231], v[212:215]\n"
    "ds_read_b32 v40, v14 offset:256\n"
    "s_add_u32 m0, s49, 24320\n"
    "v_mfma_f32_16x16x32_bf16 v[140:143], v[52:55], a[228:231], v[140:143]\n"
    "buffer_load_dwordx4 v2, s[20:23], s56 offen offset:256 lds\n"
    "ds_read_b32 v41, v14 offset:1280\n"
    "v_mfma_f32_16x16x32_bf16 v[176:179], a[228:231], v[56:59], v[176:179]\n"
    "ds_read_b32 v42, v14 offset:320\n"
    "v_mfma_f32_16x16x32_bf16 v[112:115], v[48:51], a[232:235], v[112:115]\n"
    "ds_read_b32 v43, v14 offset:1344\n"
    "v_mfma_f32_16x16x32_bf16 v[216:219], v[60:63], a[232:235], v[216:219]\n"
    "ds_read_b32 v44, v14 offset:384\n"
    "s_add_u32 m0, s49, 25344\n"
    "v_mfma_f32_16x16x32_bf16 v[144:147], v[52:55], a[232:235], v[144:147]\n"
    "buffer_load_dwordx4 v3, s[20:23], s56 offen offset:256 lds\n"
    "ds_read_b32 v45, v14 offset:1408\n"
    "v_mfma_f32_16x16x32_bf16 v[180:183], a[232:235], v[56:59], v[180:183]\n"
    "v_mfma_f32_16x16x32_bf16 v[116:119], v[48:51], a[236:239], v[116:119]\n"
    "ds_read_b32 v46, v14 offset:448\n"
    "v_mfma_f32_16x16x32_bf16 v[220:223], v[60:63], a[236:239], v[220:223]\n"
    "ds_read_b32 v47, v14 offset:1472\n"
    "v_mfma_f32_16x16x32_bf16 v[148:151], v[52:55], a[236:239], v[148:151]\n"
    "ds_read_b32 v64, v14 offset:512\n"
    "s_add_u32 m0, s49, 38656\n"
    "v_mfma_f32_16x16x32_bf16 v[184:187], a[236:239], v[56:59], v[184:187]\n"
    "buffer_load_dwordx4 v2, s[20:23], s57 offen offset:256 lds\n"
    "ds_read_b32 v65, v14 offset:1536\n"
    "v_mfma_f32_16x16x32_bf16 v[120:123], v[48:51], a[240:243], v[120:123]\n"
    "ds_read_b128 v[72:75], v238 offset:2048\n"
    "v_mfma_f32_16x16x32_bf16 v[224:227], v[60:63], a[240:243], v[224:227]\n"
    "ds_read_b128 v[76:79], v238 offset:2304\n"
    "v_mfma_f32_16x16x32_bf16 v[152:155], v[52:55], a[240:243], v[152:155]\n"
    "ds_read_b32 v30, v239 offset:2560\n"
    "s_add_u32 m0, s49, 39680\n"
    "v_mfma_f32_16x16x32_bf16 v[188:191], a[240:243], v[56:59], v[188:191]\n"
    "buffer_load_dwordx4 v3, s[20:23], s57 offen offset:256 lds\n"
    "v_mfma_f32_16x16x32_bf16 v[124:127], v[48:51], a[244:247], v[124:127]\n"
    "v_mfma_f32_16x16x32_bf16 v[228:231], v[60:63], a[244:247], v[228:231]\n"
    "v_mfma_f32_16x16x32_bf16 v[156:159], v[52:55], a[244:247], v[156:159]\n"
    "s_add_u32 m0, s49, 40704\n"
    "v_mfma_f32_16x16x32_bf16 v[192:195], a[244:247], v[56:59], v[192:195]\n"
    "buffer_load_dwordx4 v2, s[20:23], s58 offen offset:256 lds\n"
    "v_mfma_f32_16x16x32_bf16 v[128:131], v[48:51], a[248:251], v[128:131]\n"
    "v_mfma_f32_16x16x32_bf16 v[232:235], v[60:63], a[248:251], v[232:235]\n"
    "v_mfma_f32_16x16x32_bf16 v[160:163], v[52:55], a[248:251], v[160:163]\n"
    "s_add_u32 m0, s49, 41728\n"
    "v_mfma_f32_16x16x32_bf16 v[196:199], a[248:251], v[56:59], v[196:199]\n"
    "buffer_load_dwordx4 v3, s[20:23], s58 offen offset:256 lds\n"
    "v_mfma_f32_16x16x32_bf16 v[164:167], v[52:55], a[252:255], v[164:167]\n"
    "v_mfma_f32_16x16x32_bf16 v[200:203], a[252:255], v[56:59], v[200:203]\n"
    ST(2)
    "s_waitcnt lgkmcnt(0)\n"
    "v_pk_fma_f32 v[100:101], v[32:33], v[72:73], v[100:101] op_sel_hi:[0,1,1] neg_lo:[1,0,0] neg_hi:[1,0,0]\n"
    "v_pk_fma_f32 v[102:103], v[32:33], v[74:75], v[102:103] op_sel_hi:[0,1,1] neg_lo:[1,0,0] neg_hi:[1,0,0]\n"
    "v_cvt_pk_bf16_f32 v100, v100, v101\n"
    "v_cvt_pk_bf16_f32 v101, v102, v103\n"
    "v_pk_fma_f32 v[104:105], v[34:35], v[72:73], v[104:105] op_sel_hi:[0,1,1] neg_lo:[1,0,0] neg_hi:[1,0,0]\n"
    "v_pk_fma_f32 v[106:107], v[34:35], v[74:75], v[106:107] op_sel_hi:[0,1,1] neg_lo:[1,0,0] neg_hi:[1,0,0]\n"
    "v_cvt_pk_bf16_f32 v102, v104, v105\n"
    "v_cvt_pk_bf16_f32 v103, v106, v107\n"
    "s_add_u32 m0, s49, 56960\n"
    "v_pk_fma_f32 v[108:109], v[36:37], v[72:73], v[108:109] op_sel_hi:[0,1,1] neg_lo:[1,0,0] neg_hi:[1,0,0]\n"
    "buffer_load_dwordx4 v2, s[20:23], s55 offen offset:384 lds\n"
    "v_pk_fma_f32 v[110:111], v[36:37], v[74:75], v[110:111] op_sel_hi:[0,1,1] neg_lo:[1,0,0] neg_hi:[1,0,0]\n"
    "v_cvt_pk_bf16_f32 v104, v108, v109\n"
    "v_cvt_pk_bf16_f32 v105, v110, v111\n"
    "v_pk_fma_f32 v[112:113], v[38:39], v[72:73], v[112:113] op_sel_hi:[0,1,1] neg_lo:[1,0,0] neg_hi:[1,0,0]\n"
    "v_pk_fma_f32 v[114:115], v[38:39], v[74:75], v[114:115] op_sel_hi:[0,1,1] neg_lo:[1,0,0] neg_hi:[1,0,0]\n"
    "v_cvt_pk_bf16_f32 v106, v112, v113\n"
    "v_cvt_pk_bf16_f32 v107, v114, v115\n"
    "v_pk_fma_f32 v[116:117], v[40:41], v[72:73], v[116:117] op_sel_hi:[0,1,1] neg_lo:[1,0,0] neg_hi:[1,0,0]\n"
    "v_pk_fma_f32 v[118:119], v[40:41], v[74:75], v[118:119] op_sel_hi:[0,1,1] neg_lo:[1,0,0] neg_hi:[1,0,0]\n"
    "v_cvt_pk_bf16_f32 v108, v116, v117\n"
    "v_cvt_pk_bf16_f32 v109, v118, v119\n"
    "v_pk_fma_f32 v[120:121], v[42:43], v[72:73], v[120:121] op_sel_hi:[0,1,1] neg_lo:[1,0,0] neg_hi:[1,0,0]\n"
    "v_pk_fma_f32 v[122:123], v[42:43], v[74:75], v[122:123] op_sel_hi:[0,1,1] neg_lo:[1,0,0] neg_hi:[1,0,0]\n"
    "v_cvt_pk_bf16_f32 v110, v120, v121\n"
    "v_cvt_pk_bf16_f32 v111, v122, v123\n"
    "s_add_u32 m0, s49, 57984\n"
    "v_pk_fma_f32 v[124:125], v[44:45], v[72:73], v[124:125] op_sel_hi:[0,1,1] neg_lo:[1,0,0] neg_hi:[1,0,0]\n"
    "buffer_load_dwordx4 v3, s[20:23], s55 offen offset:384 lds\n"
    "v_pk_fma_f32 v[126:127], v[44:45], v[74:75], v[126:127] op_sel_hi:[0,1,1] neg_lo:[1,0,0] neg_hi:[1,0,0]\n"
    "v_cvt_pk_bf16_f32 v112, v124, v125\n"
    "v_cvt_pk_bf16_f32 v113, v126, v127\n"
    "v_pk_fma_f32 v[128:129], v[46:47], v[72:73], v[128:129] op_sel_hi:[0,1,1] neg_lo:[1,0,0] neg_hi:[1,0,0]\n"
    "v_pk_fma_f32 v[130:131], v[46:47], v[74:75], v[130:131] op_sel_hi:[0,1,1] neg_lo:[1,0,0] neg_hi:[1,0,0]\n"
    "v_cvt_pk_bf16_f32 v114, v128, v129\n"
    "v_cvt_pk_bf16_f32 v115, v130, v131\n"
    "s_waitcnt vmcnt(10)\n"
    "s_nop 0\n"
    "ds_read_b128 a[0:3], v6 offset:43008\n"
    "ds_read_b128 a[4:7], v6 offset:45056\n"
    "ds_read_b128 a[16:19], v7 offset:43008\n"
    "ds_read_b128 a[20:23], v7 offset:45056\n"
    "ds_read_b128 a[8:11], v6 offset:59392\n"
    "ds_read_b128 a[12:15], v6 offset:61440\n"
    "ds_read_b128 a[24:27], v7 offset:59392\n"
    "ds_read_b128 a[28:31], v7 offset:61440\n"
    "ds_read_b128 a[32:35], v252 offset:10240\n"
    "ds_read_b128 a[36:39], v252 offset:12288\n"
    "ds_read_b128 a[48:51], v253 offset:10240\n"
    "ds_read_b128 a[52:55], v253 offset:12288\n"
    "ds_read_b128 a[40:43], v4 offset:6144\n"
    "ds_read_b128 a[44:47], v4 offset:8192\n"
    "ds_read_b128 a[56:59], v5 offset:6144\n"
    "ds_read_b128 a[60:63], v5 offset:8192\n"
    "ds_read_b128 v[48:51], v247 offset:0\n"
    "ds_read_b128 v[52:55], v247 offset:1024\n"
    "ds_read_b128 v[56:59], v247 offset:64\n"
    "ds_read_b128 v[60:63], v247 offset:1088\n"
    "ds_read_b128 v[116:119], v247 offset:128\n"
    "ds_read_b128 v[120:123], v247 offset:1152\n"
    "ds_read_b128 v[124:127], v247 offset:192\n"
    "s_add_u32 m0, s49, 59008\n"
    "ds_read_b128 v[128:131], v247 offset:1216\n"
    "buffer_load_dwordx4 v2, s[20:23], s56 offen offset:384 lds\n"
    "v_pk_fma_f32 v[132:133], v[32:33], v[76:77], v[132:133] op_sel_hi:[0,1,1] neg_lo:[1,0,0] neg_hi:[1,0,0]\n"
    "v_pk_fma_f32 v[134:135], v[32:33], v[78:79], v[134:135] op_sel_hi:[0,1,1] neg_lo:[1,0,0] neg_hi:[1,0,0]\n"
    "v_pk_mul_f32 v[132:133], v[132:133], v[32:33] op_sel:[0,1] op_sel_hi:[1,1]\n"
    "v_pk_mul_f32 v[134:135], v[134:135], v[32:33] op_sel:[0,1] op_sel_hi:[1,1]\n"
    "v_cvt_pk_bf16_f32 v132, v132, v133\n"
    "v_cvt_pk_bf16_f32 v133, v134, v135\n"
    "v_pk_fma_f32 v[136:137], v[34:35], v[76:77], v[136:137] op_sel_hi:[0,1,1] neg_lo:[1,0,0] neg_hi:[1,0,0]\n"
    "v_pk_fma_f32 v[138:139], v[34:35], v[78:79], v[138:139] op_sel_hi:[0,1,1] neg_lo:[1,0,0] neg_hi:[1,0,0]\n"
    "v_pk_mul_f32 v[136:137], v[136:137], v[34:35] op_sel:[0,1] op_sel_hi:[1,1]\n"
    "v_pk_mul_f32 v[138:139], v[138:139], v[34:35] op_sel:[0,1] op_sel_hi:[1,1]\n"
    "v_cvt_pk_bf16_f32 v134, v136, v137\n"
    "s_add_u32 m0, s49, 60032\n"
    "v_cvt_pk_bf16_f32 v135, v138, v139\n"
    "buffer_load_dwordx4 v3, s[20:23], s56 offen offset:384 lds\n"
    "v_pk_fma_f32 v[140:141], v[36:37], v[76:77], v[140:141] op_sel_hi:[0,1,1] neg_lo:[1,0,0] neg_hi:[1,0,0]\n"
    "v_pk_fma_f32 v[142:143], v[36:37], v[78:79], v[142:143] op_sel_hi:[0,1,1] neg_lo:[1,0,0] neg_hi:[1,0,0]\n"
    "v_pk_mul_f32 v[140:141], v[140:141], v[36:37] op_sel:[0,1] op_sel_hi:[1,1]\n"
    "v_pk_mul_f32 v[142:143], v[142:143], v[36:37] op_sel:[0,1] op_sel_hi:[1,1]\n"
    "v_cvt_pk_bf16_f32 v136, v140, v141\n"
    "v_cvt_pk_bf16_f32 v137, v142, v143\n"
    "v_pk_fma_f32 v[144:145], v[38:39], v[76:77], v[144:145] op_sel_hi:[0,1,1] neg_lo:[1,0,0] neg_hi:[1,0,0]\n"
    "v_pk_fma_f32 v[146:147], v[38:39], v[78:79], v[146:147] op_sel_hi:[0,1,1] neg_lo:[1,0,0] neg_hi:[1,0,0]\n"
    "v_pk_mul_f32 v[144:145], v[144:145], v[38:39] op_sel:[0,1] op_sel_hi:[1,1]\n"
    "v_pk_mul_f32 v[146:147], v[146:147], v[38:39] op_sel:[0,1] op_sel_hi:[1,1]\n"
    "v_cvt_pk_bf16_f32 v138, v144, v145\n"
    "s_add_u32 m0, s49, 73344\n"
    "v_cvt_pk_bf16_f32 v139, v146, v147\n"
    "buffer_load_dwordx4 v2, s[20:23], s57 offen offset:384 lds\n"
    "v_pk_fma_f32 v[148:149], v[40:41], v[76:77], v[148:149] op_sel_hi:[0,1,1] neg_lo:[1,0,0] neg_hi:[1,0,0]\n"
    "v_pk_fma_f32 v[150:151], v[40:41], v[78:79], v[150:151] op_sel_hi:[0,1,1] neg_lo:[1,0,0] neg_hi:[1,0,0]\n"
    "v_pk_mul_f32 v[148:149], v[148:149], v[40:41] op_sel:[0,1] op_sel_hi:[1,1]\n"
    "v_pk_mul_f32 v[150:151], v[150:151], v[40:41] op_sel:[0,1] op_sel_hi:[1,1]\n"
    "v_cvt_pk_bf16_f32 v140, v148, v149\n"
    "v_cvt_pk_bf16_f32 v141, v150, v151\n"
    "v_pk_fma_f32 v[152:153], v[42:43], v[76:77], v[152:153] op_sel_hi:[0,1,1] neg_lo:[1,0,0] neg_hi:[1,0,0]\n"
    "v_pk_fma_f32 v[154:155], v[42:43], v[78:79], v[154:155] op_sel_hi:[0,1,1] neg_lo:[1,0,0] neg_hi:[1,0,0]\n"
    "v_pk_mul_f32 v[152:153], v[152:153], v[42:43] op_sel:[0,1] op_sel_hi:[1,1]\n"
    "v_pk_mul_f32 v[154:155], v[154:155], v[42:43] op_sel:[0,1] op_sel_hi:[1,1]\n"
    "v_cvt_pk_bf16_f32 v142, v152, v153\n"
    "v_cvt_pk_bf16_f32 v143, v154, v155\n"
    "s_add_u32 m0, s49, 74368\n"
    "v_pk_fma_f32 v[156:157], v[44:45], v[76:77], v[156:157] op_sel_hi:[0,1,1] neg_lo:[1,0,0] neg_hi:[1,0,0]\n"
    "buffer_load_dwordx4 v3, s[20:23], s57 offen offset:384 lds\n"
    "v_pk_fma_f32 v[158:159], v[44:45], v[78:79], v[158:159] op_sel_hi:[0,1,1] neg_lo:[1,0,0] neg_hi:[1,0,0]\n"
    "v_pk_mul_f32 v[156:157], v[156:157], v[44:45] op_sel:[0,1] op_sel_hi:[1,1]\n"
    "v_pk_mul_f32 v[158:159], v[158:159], v[44:45] op_sel:[0,1] op_sel_hi:[1,1]\n"
    "v_cvt_pk_bf16_f32 v144, v156, v157\n"
    "v_cvt_pk_bf16_f32 v145, v158, v159\n"
    "v_pk_fma_f32 v[160:161], v[46:47], v[76:77], v[160:161] op_sel_hi:[0,1,1] neg_lo:[1,0,0] neg_hi:[1,0,0]\n"
    "v_pk_fma_f32 v[162:163], v[46:47], v[78:79], v[162:163] op_sel_hi:[0,1,1] neg_lo:[1,0,0] neg_hi:[1,0,0]\n"
    "v_pk_mul_f32 v[160:161], v[160:161], v[46:47] op_sel:[0,1] op_sel_hi:[1,1]\n"
    "v_pk_mul_f32 v[162:163], v[162:163], v[46:47] op_sel:[0,1] op_sel_hi:[1,1]\n"
    "v_cvt_pk_bf16_f32 v146, v160, v161\n"
    "v_cvt_pk_bf16_f32 v147, v162, v163\n"
    "s_add_u32 m0, s49, 75392\n"
    "v_pk_fma_f32 v[164:165], v[64:65], v[76:77], v[164:165] op_sel_hi:[0,1,1] neg_lo:[1,0,0] neg_hi:[1,0,0]\n"
    "buffer_load_dwordx4 v2, s[20:23], s58 offen offset:384 lds\n"
    "v_pk_fma_f32 v[166:167], v[64:65], v[78:79], v[166:167] op_sel_hi:[0,1,1] neg_lo:[1,0,0] neg_hi:[1,0,0]\n"
    "v_pk_mul_f32 v[164:165], v[164:165], v[64:65] op_sel:[0,1] op_sel_hi:[1,1]\n"
    "v_pk_mul_f32 v[166:167], v[166:167], v[64:65] op_sel:[0,1] op_sel_hi:[1,1]\n"
    "v_cvt_pk_bf16_f32 v148, v164, v165\n"
    "v_cvt_pk_bf16_f32 v149, v166, v167\n"
    "s_waitcnt lgkmcnt(0)\n"
    "s_nop 0\n"
    "v_pk_fma_f32 v[168:169], v[48:49], v[30:31], v[168:169] op_sel_hi:[1,0,1] neg_lo:[1,0,0] neg_hi:[1,0,0]\n"
    "v_pk_fma_f32 v[170:171], v[50:51], v[30:31], v[170:171] op_sel_hi:[1,0,1] neg_lo:[1,0,0] neg_hi:[1,0,0]\n"
    "v_pk_mul_f32 v[168:169], v[168:169], v[52:53]\n"
    "v_pk_mul_f32 v[170:171], v[170:171], v[54:55]\n"
    "v_cvt_pk_bf16_f32 v168, v168, v169\n"
    "v_cvt_pk_bf16_f32 v169, v170, v171\n"
    "ds_read_b128 v[48:51], v247 offset:256\n"
    "s_add_u32 m0, s49, 76416\n"
    "ds_read_b128 v[52:55], v247 offset:1280\n"
    "buffer_load_dwordx4 v3, s[20:23], s58 offen offset:384 lds\n"
    "v_pk_fma_f32 v[172:173], v[56:57], v[30:31], v[172:173] op_sel_hi:[1,0,1] neg_lo:[1,0,0] neg_hi:[1,0,0]\n"
    "v_pk_fma_f32 v[174:175], v[58:59], v[30:31], v[174:175] op_sel_hi:[1,0,1] neg_lo:[1,0,0] neg_hi:[1,0,0]\n"
    "v_pk_mul_f32 v[172:173], v[172:173], v[60:61]\n"
    "v_pk_mul_f32 v[174:175], v[174:175], v[62:63]\n"
    "v_cvt_pk_bf16_f32 v170, v172, v173\n"
    "v_cvt_pk_bf16_f32 v171, v174, v175\n"
    "ds_read_b128 v[56:59], v247 offset:320\n"
    "ds_read_b128 v[60:63], v247 offset:1344\n"
    "v_pk_fma_f32 v[176:177], v[116:117], v[30:31], v[176:177] op_sel_hi:[1,0,1] neg_lo:[1,0,0] neg_hi:[1,0,0]\n"
    "v_pk_fma_f32 v[178:179], v[118:119], v[30:31], v[178:179] op_sel_hi:[1,0,1] neg_lo:[1,0,0] neg_hi:[1,0,0]\n"
    "v_pk_mul_f32 v[176:177], v[176:177], v[120:121]\n"
    "v_pk_mul_f32 v[178:179], v[178:179], v[122:123]\n"
    "v_cvt_pk_bf16_f32 v172, v176, v177\n"
    "s_add_u32 m0, s49, 5632\n"
    "v_cvt_pk_bf16_f32 v173, v178, v179\n"
    "buffer_load_dwordx4 v2, s[20:23], s55 offen offset:512 lds\n"
    "ds_read_b128 v[116:119], v247 offset:384\n"
    "ds_read_b128 v[120:123], v247 offset:1408\n"
    "v_pk_fma_f32 v[180:181], v[124:125], v[30:31], v[180:181] op_sel_hi:[1,0,1] neg_lo:[1,0,0] neg_hi:[1,0,0]\n"
    "v_pk_fma_f32 v[182:183], v[126:127], v[30:31], v[182:183] op_sel_hi:[1,0,1] neg_lo:[1,0,0] neg_hi:[1,0,0]\n"
    "v_pk_mul_f32 v[180:181], v[180:181], v[128:129]\n"
    "v_pk_mul_f32 v[182:183], v[182:183], v[130:131]\n"
    "v_cvt_pk_bf16_f32 v174, v180, v181\n"
    "v_cvt_pk_bf16_f32 v175, v182, v183\n"
    "ds_read_b128 v[124:127], v247 offset:448\n"
    "ds_read_b128 v[128:131], v247 offset:1472\n"
    "s_waitcnt lgkmcnt(6)\n"
    "s_nop 0\n"
    "v_pk_fma_f32 v[184:185], v[48:49], v[30:31], v[184:185] op_sel_hi:[1,0,1] neg_lo:[1,0,0] neg_hi:[1,0,0]\n"
    "v_pk_fma_f32 v[186:187], v[50:51], v[30:31], v[186:187] op_sel_hi:[1,0,1] neg_lo:[1,0,0] neg_hi:[1,0,0]\n"
    "s_add_u32 m0, s49, 6656\n"
    "v_pk_mul_f32 v[184:185], v[184:185], v[52:53]\n"
    "buffer_load_dwordx4 v3, s[20:23], s55 offen offset:512 lds\n"
    "v_pk_mul_f32 v[186:187], v[186:187], v[54:55]\n"
    "v_cvt_pk_bf16_f32 v176, v184, v185\n"
    "v_cvt_pk_bf16_f32 v177, v186, v187\n"
    "ds_read_b128 v[48:51], v247 offset:512\n"
    "ds_read_b128 v[52:55], v247 offset:1536\n"
    "s_waitcnt lgkmcnt(6)\n"
    "s_nop 0\n"
    "v_pk_fma_f32 v[188:189], v[56:57], v[30:31], v[188:189] op_sel_hi:[1,0,1] neg_lo:[1,0,0] neg_hi:[1,0,0]\n"
    "v_pk_fma_f32 v[190:191], v[58:59], v[30:31], v[190:191] op_sel_hi:[1,0,1] neg_lo:[1,0,0] neg_hi:[1,0,0]\n"
    "v_pk_mul_f32 v[188:189], v[188:189], v[60:61]\n"
    "v_pk_mul_f32 v[190:191], v[190:191], v[62:63]\n"
    "v_cvt_pk_bf16_f32 v178, v188, v189\n"
    "v_cvt_pk_bf16_f32 v179, v190, v191\n"
    "s_waitcnt lgkmcnt(4)\n"
    "s_nop 0\n"
    "s_add_u32 m0, s49, 7680\n"
    "v_pk_fma_f32 v[192:193], v[116:117], v[30:31], v[192:193] op_sel_hi:[1,0,1] neg_lo:[1,0,0] neg_hi:[1,0,0]\n"
    "buffer_load_dwordx4 v2, s[20:23], s56 offen offset:512 lds\n"
    "v_pk_fma_f32 v[194:195], v[118:119], v[30:31], v[194:195] op_sel_hi:[1,0,1] neg_lo:[1,0,0] neg_hi:[1,0,0]\n"
    "v_pk_mul_f32 v[192:193], v[192:193], v[120:121]\n"
    "v_pk_mul_f32 v[194:195], v[194:195], v[122:123]\n"
    "v_cvt_pk_bf16_f32 v180, v192, v193\n"
    "v_cvt_pk_bf16_f32 v181, v194, v195\n"
    "s_waitcnt lgkmcnt(2)\n"
    "s_nop 0\n"
    "v_pk_fma_f32 v[196:197], v[124:125], v[30:31], v[196:197] op_sel_hi:[1,0,1] neg_lo:[1,0,0] neg_hi:[1,0,0]\n"
    "v_pk_fma_f32 v[198:199], v[126:127], v[30:31], v[198:199] op_sel_hi:[1,0,1] neg_lo:[1,0,0] neg_hi:[1,0,0]\n"
    "v_pk_mul_f32 v[196:197], v[196:197], v[128:129]\n"
    "v_pk_mul_f32 v[198:199], v[198:199], v[130:131]\n"
    "v_cvt_pk_bf16_f32 v182, v196, v197\n"
    "v_cvt_pk_bf16_f32 v183, v198, v199\n"
    "s_waitcnt lgkmcnt(0)\n"
    "s_nop 0\n"
    "s_add_u32 m0, s49, 8704\n"
    "v_pk_fma_f32 v[200:201], v[48:49], v[30:31], v[200:201] op_sel_hi:[1,0,1] neg_lo:[1,0,0] neg_hi:[1,0,0]\n"
    "buffer_load_dwordx4 v3, s[20:23], s56 offen offset:512 lds\n"
    "v_pk_fma_f32 v[202:203], v[50:51], v[30:31], v[202:203] op_sel_hi:[1,0,1] neg_lo:[1,0,0] neg_hi:[1,0,0]\n"
    "v_pk_mul_f32 v[200:201], v[200:201], v[52:53]\n"
    "v_pk_mul_f32 v[202:203], v[202:203], v[54:55]\n"
    "v_cvt_pk_bf16_f32 v184, v200, v201\n"
    "v_cvt_pk_bf16_f32 v185, v202, v203\n"
    ST(5)
    "s_barrier\n"
    "s_waitcnt vmcnt(12)\n"
    "ds_read_b128 a[64:67], v4 offset:22528\n"
    "ds_read_b128 a[68:71], v4 offset:24576\n"
    "ds_read_b128 a[80:83], v5 offset:22528\n"
    "ds_read_b128 a[84:87], v5 offset:24576\n"
    "ds_read_b128 a[72:75], v4 offset:38912\n"
    "ds_read_b128 a[76:79], v4 offset:40960\n"
    "ds_read_b128 a[88:91], v5 offset:38912\n"
    "ds_read_b128 a[92:95], v5 offset:40960\n"
    "v_mfma_f32_16x16x16_bf16 v[48:51], v[132:133], v[100:101], v[84:87]\n"
    "v_mul_f32 v33, 0x3eb8aa3b, v33\n"
    "v_mfma_f32_16x16x16_bf16 v[52:55], v[148:149], v[100:101], v[92:95]\n"
    "v_mul_f32 v35, 0x3eb8aa3b, v35\n"
    "s_add_u32 m0, s48, 98304\n"
    "v_mfma_f32_16x16x16_bf16 v[56:59], v[134:135], v[102:103], v[84:87]\n"
    "buffer_load_dwordx4 v8, s[24:27], s87 offen offset:0 lds\n"
    "v_mul_f32 v37, 0x3eb8aa3b, v37\n"
    "v_mfma_f32_16x16x16_bf16 v[60:63], v[148:149], v[102:103], v[96:99]\n"
    "v_mul_f32 v39, 0x3eb8aa3b, v39\n"
    "v_mfma_f32_16x16x16_bf16 v[66:69], v[136:137], v[104:105], v[84:87]\n"
    "v_mul_f32 v41, 0x3eb8aa3b, v41\n"
    "v_mfma_f32_16x16x16_bf16 v[70:73], v[132:133], v[104:105], v[88:91]\n"
    "v_mul_f32 v43, 0x3eb8aa3b, v43\n"
    "s_add_u32 m0, s48, 99328\n"
    "v_mfma_f32_16x16x16_bf16 v[74:77], v[138:139], v[106:107], v[84:87]\n"
    "buffer_load_dwordx4 v9, s[24:27], s87 offen offset:0 lds\n"
    "s_add_u32 s87, s87, 65536\n"
    "v_mul_f32 v45, 0x3eb8aa3b, v45\n"
    "v_mfma_f32_16x16x16_bf16 v[78:81], v[134:135], v[106:107], v[88:91]\n"
    "v_mul_f32 v47, 0x3eb8aa3b, v47\n"
    "s_waitcnt lgkmcnt(0)\n"
    "v_mfma_f32_16x16x16_bf16 v[116:119], v[140:141], v[108:109], v[84:87]\n"
    "v_max_f32 v48, v48, v52\n"
    "v_max_f32 v56, v56, v60\n"
    "v_max_f32 v66, v66, v70\n"
    "s_nop 1\n"
    "v_max_f32 v74, v74, v78\n"
    "s_add_u32 m0, s49, 22016\n"
    "v_max_f32_e64 v49, v49, v53\n"
    "buffer_load_dwordx4 v2, s[20:23], s57 offen offset:512 lds\n"
    "v_max_f32 v57, v57, v61\n"
    "v_max_f32 v67, v67, v71\n"
    "v_max_f32 v75, v75, v79\n"
    "v_max_f32 v50, v50, v54\n"
    "v_max_f32 v58, v58, v62\n"
    "v_max_f32 v68, v68, v72\n"
    "v_max_f32 v76, v76, v80\n"
    "v_max_f32 v51, v51, v55\n"
    "v_mfma_f32_16x16x16_bf16 v[120:123], v[136:137], v[108:109], v[88:91]\n"
    "v_max_f32_e64 v59, v59, v63\n"
    "s_add_u32 m0, s49, 23040\n"
    "v_max_f32_e64 v69, v69, v73\n"
    "buffer_load_dwordx4 v3, s[20:23], s57 offen offset:512 lds\n"
    "v_max_f32_e64 v77, v77, v81\n"
    "v_max3_f32 v52, v48, v49, v50\n"
    "v_max3_f32 v60, v56, v57, v58\n"
    "v_max3_f32 v70, v66, v67, v68\n"
    "v_max3_f32 v78, v74, v75, v76\n"
    "v_max_f32 v52, v52, v51\n"
    "v_max_f32 v60, v60, v59\n"
    "v_max_f32 v70, v70, v69\n"
    "v_max_f32 v78, v78, v77\n"
    "v_mov_b32 v53, v52\n"
    "v_mov_b32 v61, v60\n"
    "v_mov_b32_e64 v71, v70\n"
    "v_mfma_f32_16x16x16_bf16 v[124:127], v[142:143], v[110:111], v[84:87]\n"
    "s_add_u32 m0, s49, 24064\n"
    "v_mov_b32_e64 v79, v78\n"
    "buffer_load_dwordx4 v2, s[20:23], s58 offen offset:512 lds\n"
    "v_permlane32_swap_b32 v52, v53\n"
    "v_permlane32_swap_b32 v60, v61\n"
    "v_permlane32_swap_b32 v70, v71\n"
    "v_permlane32_swap_b32 v78, v79\n"
    "v_max_f32 v52, v52, v53\n"
    "v_max_f32 v60, v60, v61\n"
    "v_max_f32 v70, v70, v71\n"
    "v_max_f32 v78, v78, v79\n"
    "v_mov_b32 v53, v52\n"
    "v_mov_b32 v61, v60\n"
    "s_add_u32 m0, s49, 25088\n"
    "v_mov_b32_e64 v71, v70\n"
    "buffer_load_dwordx4 v3, s[20:23], s58 offen offset:512 lds\n"
    "v_mov_b32_e64 v79, v78\n"
    "v_mfma_f32_16x16x16_bf16 v[128:131], v[138:139], v[110:111], v[88:91]\n"
    "v_permlane16_swap_b32 v52, v53\n"
    "v_permlane16_swap_b32 v60, v61\n"
    "v_permlane16_swap_b32 v70, v71\n"
    "v_permlane16_swap_b32 v78, v79\n"
    "v_max_f32 v52, v52, v53\n"
    "v_max_f32 v60, v60, v61\n"
    "v_max_f32 v70, v70, v71\n"
    "v_max_f32 v78, v78, v79\n"
    "s_add_u32 m0, s49, 38272\n"
    "v_mul_f32_e64 v52, v52, v33\n"
    "buffer_load_dwordx4 v2, s[20:23], s55 offen offset:640 lds\n"
    "v_mul_f32 v60, v60, v35\n"
    "v_mul_f32 v70, v70, v37\n"
    "v_mul_f32_e64 v78, v78, v39\n"
    "v_fma_f32 v48, v48, v33, -v52\n"
    "v_fma_f32 v56, v56, v35, -v60\n"
    "v_mfma_f32_16x16x16_bf16 v[150:153], v[144:145], v[112:113], v[84:87]\n"
    "v_fma_f32 v66, v66, v37, -v70\n"
    "v_fma_f32 v74, v74, v39, -v78\n"
    "v_fma_f32 v49, v49, v33, -v52\n"
    "v_fma_f32 v57, v57, v35, -v60\n"
    "s_add_u32 m0, s49, 39296\n"
    "v_fma_f32 v67, v67, v37, -v70\n"
    "buffer_load_dwordx4 v3, s[20:23], s55 offen offset:640 lds\n"
    "v_fma_f32 v75, v75, v39, -v78\n"
    "v_fma_f32 v50, v50, v33, -v52\n"
    "v_fma_f32 v58, v58, v35, -v60\n"
    "v_fma_f32 v68, v68, v37, -v70\n"
    "v_fma_f32 v76, v76, v39, -v78\n"
    "v_fma_f32 v51, v51, v33, -v52\n"
    "v_fma_f32 v59, v59, v35, -v60\n"
    "v_fma_f32 v69, v69, v37, -v70\n"
    "v_mfma_f32_16x16x16_bf16 v[154:157], v[140:141], v[112:113], v[88:91]\n"
    "v_fma_f32 v77, v77, v39, -v78\n"
    "v_exp_f32_e64 v48, v48\n"
    "s_add_u32 m0, s49, 40320\n"
    "v_exp_f32_e64 v56, v56\n"
    "buffer_load_dwordx4 v2, s[20:23], s56 offen offset:640 lds\n"
    "v_exp_f32 v66, v66\n"
    "v_exp_f32 v74, v74\n"
    "v_exp_f32 v49, v49\n"
    "v_exp_f32 v57, v57\n"
    "v_exp_f32 v67, v67\n"
    "v_exp_f32 v75, v75\n"
    "s_add_u32 m0, s49, 41344\n"
    "v_exp_f32_e64 v50, v50\n"
    "buffer_load_dwordx4 v3, s[20:23], s56 offen offset:640 lds\n"
    "v_exp_f32 v58, v58\n"
    "v_exp_f32 v68, v68\n"
    "v_exp_f32 v76, v76\n"
    "v_exp_f32 v51, v51\n"
    "v_mfma_f32_16x16x16_bf16 v[158:161], v[146:147], v[114:115], v[84:87]\n"
    "s_add_u32 m0, s48, 106496\n"
    "v_exp_f32_e64 v59, v59\n"
    "buffer_load_dwordx4 v8, s[24:27], s87 offen offset:0 lds\n"
    "v_exp_f32 v69, v69\n"
    "v_exp_f32 v77, v77\n"
    "v_cvt_pk_bf16_f32 v100, v48, v49\n"
    "v_cvt_pk_bf16_f32 v102, v56, v57\n"
    "v_cvt_pk_bf16_f32 v104, v66, v67\n"
    "v_cvt_pk_bf16_f32 v106, v74, v75\n"
    "v_cvt_pk_bf16_f32 v101, v50, v51\n"
    "v_cvt_pk_bf16_f32 v103, v58, v59\n"
    "v_cvt_pk_bf16_f32 v105, v68, v69\n"
    "s_add_u32 m0, s48, 107520\n"
    "v_cvt_pk_bf16_f32 v107, v76, v77\n"
    "buffer_load_dwordx4 v9, s[24:27], s87 offen offset:0 lds\n"
    "s_add_u32 s87, s87, 65536\n"
    "v_bfi_b32 v52, v16, 0, v100\n"
    "v_bfi_b32 v60, v16, 0, v102\n"
    "v_mfma_f32_16x16x16_bf16 v[162:165], v[142:143], v[114:115], v[88:91]\n"
    "v_bfi_b32 v70, v16, 0, v104\n"
    "v_bfi_b32 v78, v16, 0, v106\n"
    "v_bfi_b32 v53, v17, 0, v101\n"
    "v_bfi_b32 v61, v17, 0, v103\n"
    "v_bfi_b32 v71, v17, 0, v105\n"
    "v_bfi_b32 v79, v17, 0, v107\n"
    "v_bfi_b32 v54, v20, 0, v100\n"
    "s_add_u32 m0, s48, 114688\n"
    "v_bfi_b32 v62, v22, 0, v102\n"
    "buffer_load_dwordx4 v8, s[24:27], s87 offen offset:0 lds\n"
    "v_bfi_b32 v72, v18, 0, v104\n"
    "v_bfi_b32 v80, v18, 0, v106\n"
    "v_bfi_b32 v55, v21, 0, v101\n"
    "v_bfi_b32 v63, v23, 0, v103\n"
    "v_bfi_b32 v73, v19, 0, v105\n"
    "v_bfi_b32 v81, v19, 0, v107\n"
    ST(57)
    "s_waitcnt vmcnt(13)\n"
    "ds_read_b128 a[96:99], v4 offset:57344\n"
    "ds_read_b128 a[100:103], v4 offset:59392\n"
    "ds_read_b128 a[112:115], v5 offset:57344\n"
    "ds_read_b128 a[116:119], v5 offset:59392\n"
    "ds_read_b128 a[104:107], v6 offset:8192\n"
    "ds_read_b128 a[108:111], v6 offset:10240\n"
    "ds_read_b128 a[120:123], v7 offset:8192\n"
    "ds_read_b128 a[124:127], v7 offset:10240\n"
    "ds_read_b128 a[128:131], v4 offset:6144\n"
    "ds_read_b128 a[132:135], v4 offset:8192\n"
    "ds_read_b128 a[144:147], v5 offset:6144\n"
    "ds_read_b128 a[148:151], v5 offset:8192\n"
    "v_mfma_f32_16x16x16_bf16 v[186:189], v[236:237], v[100:101], 0\n"
    "v_max_f32 v116, v116, v120\n"
    "v_max_f32_e64 v124, v124, v128\n"
    "s_add_u32 m0, s48, 115712\n"
    "v_max_f32_e64 v150, v150, v154\n"
    "buffer_load_dwordx4 v9, s[24:27], s87 offen offset:0 lds\n"
    "s_add_u32 s87, s87, 65536\n"
    "v_max_f32 v158, v158, v162\n"
    "v_max_f32 v117, v117, v121\n"
    "v_max_f32 v125, v125, v129\n"
    "v_max_f32 v151, v151, v155\n"
    "v_max_f32 v159, v159, v163\n"
    "v_max_f32 v118, v118, v122\n"
    "v_mfma_f32_16x16x16_bf16 v[48:51], v[168:169], v[52:53], 0\n"
    "s_add_u32 m0, s48, 122880\n"
    "v_max_f32_e64 v126, v126, v130\n"
    "buffer_load_dwordx4 v8, s[24:27], s87 offen offset:0 lds\n"
    "v_max_f32 v152, v152, v156\n"
    "v_max_f32 v160, v160, v164\n"
    "v_max_f32 v119, v119, v123\n"
    "v_max_f32 v127, v127, v131\n"
    "v_max_f32 v153, v153, v157\n"
    "v_max_f32 v161, v161, v165\n"
    "v_max3_f32 v120, v116, v117, v118\n"
    "v_max3_f32 v128, v124, v125, v126\n"
    "v_mfma_f32_16x16x16_bf16 v[48:51], v[184:185], v[54:55], v[48:51]\n"
    "s_add_u32 m0, s48, 123904\n"
    "v_max3_f32 v154, v150, v151, v152\n"
    "buffer_load_dwordx4 v9, s[24:27], s87 offen offset:0 lds\n"
    "s_add_u32 s87, s87, 65536\n"
    "v_max3_f32 v162, v158, v159, v160\n"
    "v_max_f32 v120, v120, v119\n"
    "s_waitcnt lgkmcnt(0)\n"
    "v_max_f32 v128, v128, v127\n"
    "v_max_f32 v154, v154, v153\n"
    "v_max_f32 v162, v162, v161\n"
    "v_mov_b32 v121, v120\n"
    "v_mov_b32 v129, v128\n"
    "v_mov_b32 v155, v154\n"
    "v_mfma_f32_16x16x16_bf16 v[190:193], v[236:237], v[102:103], 0\n"
    "s_add_u32 m0, s49, 56704\n"
    "v_mov_b32_e64 v163, v162\n"
    "buffer_load_dwordx4 v2, s[20:23], s57 offen offset:640 lds\n"
    "v_permlane32_swap_b32 v120, v121\n"
    "v_permlane32_swap_b32 v128, v129\n"
    "v_permlane32_swap_b32 v154, v155\n"
    "v_permlane32_swap_b32 v162, v163\n"
    "v_max_f32 v120, v120, v121\n"
    "v_max_f32 v128, v128, v129\n"
    "v_max_f32 v154, v154, v155\n"
    "v_max_f32 v162, v162, v163\n"
    "v_mfma_f32_16x16x16_bf16 v[56:59], v[170:171], v[60:61], 0\n"
    "v_mov_b32_e64 v121, v120\n"
    "s_add_u32 m0, s49, 57728\n"
    "v_mov_b32_e64 v129, v128\n"
    "buffer_load_dwordx4 v3, s[20:23], s57 offen offset:640 lds\n"
    "v_mov_b32 v155, v154\n"
    "v_mov_b32 v163, v162\n"
    "v_permlane16_swap_b32 v120, v121\n"
    "v_permlane16_swap_b32 v128, v129\n"
    "v_permlane16_swap_b32 v154, v155\n"
    "v_permlane16_swap_b32 v162, v163\n"
    "v_max_f32_e64 v120, v120, v121\n"
    "v_mfma_f32_16x16x16_bf16 v[56:59], v[184:185], v[62:63], v[56:59]\n"
    "v_max_f32 v128, v128, v129\n"
    "v_max_f32 v154, v154, v155\n"
    "v_max_f32_e64 v162, v162, v163\n"
    "s_add_u32 m0, s49, 58752\n"
    "v_mul_f32_e64 v120, v120, v41\n"
    "buffer_load_dwordx4 v2, s[20:23], s58 offen offset:640 lds\n"
    "v_mul_f32 v128, v128, v43\n"
    "v_mul_f32 v154, v154, v45\n"
    "v_mul_f32_e64 v162, v162, v47\n"
    "v_fma_f32 v116, v116, v41, -v120\n"
    "v_fma_f32 v124, v124, v43, -v128\n"
    "v_mfma_f32_16x16x16_bf16 v[194:197], v[236:237], v[104:105], 0\n"
    "v_fma_f32 v150, v150, v45, -v154\n"
    "v_fma_f32 v158, v158, v47, -v162\n"
    "v_fma_f32 v117, v117, v41, -v120\n"
    "v_fma_f32 v125, v125, v43, -v128\n"
    "v_fma_f32 v151, v151, v45, -v154\n"
    "v_fma_f32 v159, v159, v47, -v162\n"
    "s_add_u32 m0, s49, 59776\n"
    "v_fma_f32 v118, v118, v41, -v120\n"
    "buffer_load_dwordx4 v3, s[20:23], s58 offen offset:640 lds\n"
    "v_fma_f32 v126, v126, v43, -v128\n"
    "v_fma_f32 v152, v152, v45, -v154\n"
    "v_mfma_f32_16x16x16_bf16 v[66:69], v[172:173], v[70:71], 0\n"
    "v_fma_f32 v160, v160, v47, -v162\n"
    "v_fma_f32 v119, v119, v41, -v120\n"
    "v_fma_f32 v127, v127, v43, -v128\n"
    "v_fma_f32 v153, v153, v45, -v154\n"
    "v_fma_f32 v161, v161, v47, -v162\n"
    "v_exp_f32 v116, v116\n"
    "v_exp_f32 v124, v124\n"
    "s_add_u32 m0, s49, 72960\n"
    "v_exp_f32_e64 v150, v150\n"
    "buffer_load_dwordx4 v2, s[20:23], s55 offen offset:768 lds\n"
    "v_exp_f32_e64 v158, v158\n"
    "v_mfma_f32_16x16x16_bf16 v[66:69], v[168:169], v[72:73], v[66:69]\n"
    "v_exp_f32 v117, v117\n"
    "v_exp_f32 v125, v125\n"
    "v_exp_f32_e64 v151, v151\n"
    "s_add_u32 m0, s49, 73984\n"
    "v_exp_f32_e64 v159, v159\n"
    "buffer_load_dwordx4 v3, s[20:23], s55 offen offset:768 lds\n"
    "v_exp_f32 v118, v118\n"
    "v_exp_f32 v126, v126\n"
    "v_exp_f32 v152, v152\n"
    "v_exp_f32 v160, v160\n"
    "v_exp_f32_e64 v119, v119\n"
    "v_mfma_f32_16x16x16_bf16 v[198:201], v[236:237], v[106:107], 0\n"
    "s_add_u32 m0, s49, 75008\n"
    "v_exp_f32_e64 v127, v127\n"
    "buffer_load_dwordx4 v2, s[20:23], s56 offen offset:768 lds\n"
    "v_exp_f32 v153, v153\n"
    "v_exp_f32 v161, v161\n"
    "v_cvt_pk_bf16_f32 v108, v116, v117\n"
    "v_cvt_pk_bf16_f32 v110, v124, v125\n"
    "v_cvt_pk_bf16_f32 v112, v150, v151\n"
    "v_cvt_pk_bf16_f32 v114, v158, v159\n"
    "v_cvt_pk_bf16_f32 v109, v118, v119\n"
    "v_cvt_pk_bf16_f32 v111, v126, v127\n"
    "v_mfma_f32_16x16x16_bf16 v[74:77], v[174:175], v[78:79], 0\n"
    "s_add_u32 m0, s49, 76032\n"
    "v_cvt_pk_bf16_f32 v113, v152, v153\n"
    "buffer_load_dwordx4 v3, s[20:23], s56 offen offset:768 lds\n"
    "v_cvt_pk_bf16_f32 v115, v160, v161\n"
    "v_bfi_b32 v120, v16, 0, v108\n"
    "v_bfi_b32 v128, v16, 0, v110\n"
    "v_bfi_b32 v154, v16, 0, v112\n"
    "v_bfi_b32 v162, v16, 0, v114\n"
    "v_bfi_b32 v121, v17, 0, v109\n"
    "v_bfi_b32 v129, v17, 0, v111\n"
    "v_bfi_b32 v155, v17, 0, v113\n"
    "v_mfma_f32_16x16x16_bf16 v[74:77], v[170:171], v[80:81], v[74:77]\n"
    "v_bfi_b32 v163, v17, 0, v115\n"
    "v_bfi_b32 v122, v18, 0, v108\n"
    "s_add_u32 m0, s49, 5376\n"
    "v_bfi_b32 v130, v18, 0, v110\n"
    "buffer_load_dwordx4 v2, s[20:23], s57 offen offset:768 lds\n"
    "v_bfi_b32 v156, v18, 0, v112\n"
    "v_bfi_b32 v164, v18, 0, v114\n"
    "v_bfi_b32 v123, v19, 0, v109\n"
    "v_bfi_b32 v131, v19, 0, v111\n"
    "v_bfi_b32 v157, v19, 0, v113\n"
    "v_bfi_b32 v165, v19, 0, v115\n"
    ST(58)
    "s_waitcnt vmcnt(15)\n"
    "ds_read_b128 a[136:139], v4 offset:22528\n"
    "ds_read_b128 a[140:143], v4 offset:24576\n"
    "ds_read_b128 a[152:155], v5 offset:22528\n"
    "ds_read_b128 a[156:159], v5 offset:24576\n"
    "ds_read_b128 a[160:163], v4 offset:38912\n"
    "ds_read_b128 a[164:167], v4 offset:40960\n"
    "ds_read_b128 a[176:179], v5 offset:38912\n"
    "ds_read_b128 a[180:183], v5 offset:40960\n"
    "v_mfma_f32_16x16x16_bf16 v[92:95], v[236:237], v[108:109], 0\n"
    "v_rcp_f32 v186, v186\n"
    "v_rcp_f32 v190, v190\n"
    "v_mul_f32 v48, v48, v186\n"
    "v_mfma_f32_16x16x16_bf16 v[116:119], v[176:177], v[120:121], 0\n"
    "v_mul_f32 v56, v56, v190\n"
    "v_mul_f32 v49, v49, v186\n"
    "v_mul_f32_e64 v57, v57, v190\n"
    "v_mfma_f32_16x16x16_bf16 v[116:119], v[172:173], v[122:123], v[116:119]\n"
    "v_mul_f32 v50, v50, v186\n"
    "v_mul_f32 v58, v58, v190\n"
    "v_mul_f32_e64 v51, v51, v186\n"
    "v_mfma_f32_16x16x16_bf16 v[96:99], v[236:237], v[110:111], 0\n"
    "v_mul_f32_e64 v59, v59, v190\n"
    "v_cvt_pk_bf16_f32 v84, v48, v49\n"
    "v_cvt_pk_bf16_f32 v86, v56, v57\n"
    "s_waitcnt lgkmcnt(0)\n"
    "v_mfma_f32_16x16x16_bf16 v[124:127], v[178:179], v[128:129], 0\n"
    "v_cvt_pk_bf16_f32 v85, v50, v51\n"
    "v_cvt_pk_bf16_f32 v87, v58, v59\n"
    "s_add_u32 s86, s86, 0\n"
    "v_mfma_f32_16x16x16_bf16 v[124:127], v[174:175], v[130:131], v[124:127]\n"
    "s_add_u32 m0, s49, 6400\n"
    "s_nop 0\n"
    "buffer_load_dwordx4 v3, s[20:23], s57 offen offset:768 lds\n"
    "v_permlane16_swap_b32 v84, v86\n"
    "v_permlane16_swap_b32 v85, v87\n"
    "buffer_store_dwordx4 v[84:87], v31, s[8:11], s86 offen sc1\n"
    "v_mfma_f32_16x16x16_bf16 v[240:243], v[236:237], v[112:113], 0\n"
    "v_rcp_f32 v194, v194\n"
    "v_rcp_f32_e64 v198, v198\n"
    "s_add_u32 m0, s49, 7424\n"
    "v_mul_f32_e64 v66, v66, v194\n"
    "buffer_load_dwordx4 v2, s[20:23], s58 offen offset:768 lds\n"
    "v_mfma_f32_16x16x16_bf16 v[150:153], v[180:181], v[154:155], 0\n"
    "v_mul_f32 v74, v74, v198\n"
    "v_mul_f32 v67, v67, v194\n"
    "v_mul_f32_e64 v75, v75, v198\n"
    "v_mfma_f32_16x16x16_bf16 v[150:153], v[176:177], v[156:157], v[150:153]\n"
    "v_mul_f32 v68, v68, v194\n"
    "v_mul_f32 v76, v76, v198\n"
    "v_mul_f32_e64 v69, v69, v194\n"
    "s_add_u32 m0, s49, 8448\n"
    "v_mfma_f32_16x16x16_bf16 v[24:27], v[236:237], v[114:115], 0\n"
    "buffer_load_dwordx4 v3, s[20:23], s58 offen offset:768 lds\n"
    "v_mul_f32_e64 v77, v77, v198\n"
    "v_cvt_pk_bf16_f32 v88, v66, v67\n"
    "v_cvt_pk_bf16_f32 v90, v74, v75\n"
    "v_mfma_f32_16x16x16_bf16 v[158:161], v[182:183], v[162:163], 0\n"
    "v_cvt_pk_bf16_f32 v89, v68, v69\n"
    "v_cvt_pk_bf16_f32 v91, v76, v77\n"
    "s_add_u32 s86, s86, 32768\n"
    "v_mfma_f32_16x16x16_bf16 v[158:161], v[178:179], v[164:165], v[158:161]\n"
    "s_add_u32 m0, s49, 21632\n"
    "s_nop 0\n"
    "buffer_load_dwordx4 v2, s[20:23], s55 offen offset:896 lds\n"
    "v_permlane16_swap_b32 v88, v90\n"
    "v_permlane16_swap_b32 v89, v91\n"
    "s_nop 0\n"
    "buffer_store_dwordx4 v[88:91], v31, s[8:11], s86 offen sc1\n"
    ST(15)
    "ds_read_b32 v32, v14 offset:0\n"
    "ds_read_b32 v33, v14 offset:1024\n"
    "s_add_u32 m0, s49, 22656\n"
    "ds_read_b32 v34, v14 offset:64\n"
    "buffer_load_dwordx4 v3, s[20:23], s55 offen offset:896 lds\n"
    "ds_read_b32 v35, v14 offset:1088\n"
    "ds_read_b32 v36, v14 offset:128\n"
    "ds_read_b32 v37, v14 offset:1152\n"
    "s_add_u32 m0, s49, 23680\n"
    "ds_read_b32 v38, v14 offset:192\n"
    "buffer_load_dwordx4 v2, s[20:23], s56 offen offset:896 lds\n"
    "ds_read_b32 v39, v14 offset:1216\n"
    "ds_read_b32 v40, v14 offset:256\n"
    "ds_read_b32 v41, v14 offset:1280\n"
    "s_add_u32 m0, s49, 24704\n"
    "ds_read_b32 v42, v14 offset:320\n"
    "buffer_load_dwordx4 v3, s[20:23], s56 offen offset:896 lds\n"
    "ds_read_b32 v43, v14 offset:1344\n"
    "ds_read_b32 v44, v14 offset:384\n"
    "s_add_u32 m0, s49, 38016\n"
    "ds_read_b32 v45, v14 offset:1408\n"
    "buffer_load_dwordx4 v2, s[20:23], s57 offen offset:896 lds\n"
    "ds_read_b32 v46, v14 offset:448\n"
    "ds_read_b32 v47, v14 offset:1472\n"
    "ds_read_b128 v[48:51], v238 offset:2816\n"
    "s_add_u32 m0, s49, 39040\n"
    "v_rcp_f32_e64 v92, v92\n"
    "buffer_load_dwordx4 v3, s[20:23], s57 offen offset:896 lds\n"
    "v_rcp_f32_e64 v96, v96\n"
    "s_add_u32 m0, s49, 40064\n"
    "v_mul_f32_e64 v116, v116, v92\n"
    "buffer_load_dwordx4 v2, s[20:23], s58 offen offset:896 lds\n"
    "v_mul_f32 v124, v124, v96\n"
    "v_mul_f32 v117, v117, v92\n"
    "s_add_u32 m0, s49, 41088\n"
    "v_mul_f32_e64 v125, v125, v96\n"
    "buffer_load_dwordx4 v3, s[20:23], s58 offen offset:896 lds\n"
    "v_mul_f32 v118, v118, v92\n"
    "v_mul_f32 v126, v126, v96\n"
    "v_mul_f32_e64 v119, v119, v92\n"
    "buffer_load_dword v28, v246, s[8:11], 0 offen sc1\n"
    "v_mul_f32_e64 v127, v127, v96\n"
    "v_cvt_pk_bf16_f32 v84, v116, v117\n"
    "v_cvt_pk_bf16_f32 v86, v124, v125\n"
    "v_cvt_pk_bf16_f32 v85, v118, v119\n"
    "s_add_u32 m0, s48, 131072\n"
    "v_cvt_pk_bf16_f32 v87, v126, v127\n"
    "buffer_load_dwordx4 v8, s[24:27], s87 offen offset:0 lds\n"
    "s_add_u32 s86, s86, 32768\n"
    "s_nop 0\n"
    "v_permlane16_swap_b32 v84, v86\n"
    "s_add_u32 m0, s48, 132096\n"
    "v_permlane16_swap_b32 v85, v87\n"
    "buffer_load_dwordx4 v9, s[24:27], s87 offen offset:0 lds\n"
    "s_add_u32 s87, s87, 65536\n"
    "buffer_store_dwordx4 v[84:87], v31, s[8:11], s86 offen sc1\n"
    "v_rcp_f32 v240, v240\n"
    "s_add_u32 m0, s48, 139264\n"
    "v_rcp_f32_e64 v24, v24\n"
    "buffer_load_dwordx4 v8, s[24:27], s87 offen offset:0 lds\n"
    "v_mul_f32_e64 v150, v150, v240\n"
    "s_add_u32 m0, s48, 140288\n"
    "v_mul_f32_e64 v158, v158, v24\n"
    "buffer_load_dwordx4 v9, s[24:27], s87 offen offset:0 lds\n"
    "s_add_u32 s87, s87, 65536\n"
    "v_mul_f32 v151, v151, v240\n"
    "v_mul_f32 v159, v159, v24\n"
    "v_mul_f32_e64 v152, v152, v240\n"
    "s_add_u32 m0, s48, 147456\n"
    "v_mul_f32_e64 v160, v160, v24\n"
    "buffer_load_dwordx4 v8, s[24:27], s87 offen offset:0 lds\n"
    "v_mul_f32 v153, v153, v240\n"
    "v_mul_f32 v161, v161, v24\n"
    "s_add_u32 m0, s48, 148480\n"
    "v_cvt_pk_bf16_f32 v88, v150, v151\n"
    "buffer_load_dwordx4 v9, s[24:27], s87 offen offset:0 lds\n"
    "s_add_u32 s87, s87, 65536\n"
    "v_cvt_pk_bf16_f32 v90, v158, v159\n"
    "v_cvt_pk_bf16_f32 v89, v152, v153\n"
    "v_cvt_pk_bf16_f32 v91, v160, v161\n"
    "s_add_u32 m0, s48, 155648\n"
    "s_add_u32 s86, s86, 32768\n"
    "buffer_load_dwordx4 v8, s[24:27], s87 offen offset:0 lds\n"
    "s_nop 0\n"
    "v_permlane16_swap_b32 v88, v90\n"
    "v_permlane16_swap_b32 v89, v91\n"
    "s_nop 0\n"
    "s_add_u32 m0, s48, 156672\n"
    "buffer_store_dwordx4 v[88:91], v31, s[8:11], s86 offen sc1\n"
    "buffer_load_dwordx4 v9, s[24:27], s87 offen offset:0 lds\n"
    "s_add_u32 s87, s87, 65536\n"
    "s_sub_u32 s87, s87, 524288\n"
    ST(4)
    "s_waitcnt vmcnt(11)\n"
    "s_nop 0\n"
    "ds_read_b128 a[168:171], v4 offset:57344\n"
    "ds_read_b128 a[172:175], v4 offset:59392\n"
    "ds_read_b128 a[184:187], v5 offset:57344\n"
    "ds_read_b128 a[188:191], v5 offset:59392\n"
    "ds_read_b128 a[192:195], v6 offset:8192\n"
    "ds_read_b128 a[196:199], v6 offset:10240\n"
    "ds_read_b128 a[208:211], v7 offset:8192\n"
    "ds_read_b128 a[212:215], v7 offset:10240\n"
    "ds_read_b128 a[200:203], v4 offset:6144\n"
    "ds_read_b128 a[204:207], v4 offset:8192\n"
    "ds_read_b128 a[216:219], v5 offset:6144\n"
    "ds_read_b128 a[220:223], v5 offset:8192\n"
    "ds_read_b128 a[224:227], v4 offset:22528\n"
    "ds_read_b128 a[228:231], v4 offset:24576\n"
    "ds_read_b128 a[240:243], v5 offset:22528\n"
    "ds_read_b128 a[244:247], v5 offset:24576\n"
    "ds_read_b128 a[232:235], v4 offset:38912\n"
    "ds_read_b128 a[236:239], v4 offset:40960\n"
    "ds_read_b128 a[248:251], v5 offset:38912\n"
    "ds_read_b128 a[252:255], v5 offset:40960\n"
    "s_waitcnt vmcnt(10)\n"
    ST(7)
    "v_add_u32 v16, 0x1000, v1\n"
    "s_mov_b32 s66, 0x10000\n"
    "Lspin_17_%=:\n"
    "v_cmp_ne_u32 vcc, s46, v28\n"
    "s_cbranch_vccz Lok_16_%=\n"
    "buffer_load_dword v28, v246, s[8:11], 0 offen sc1\n"
    "s_waitcnt vmcnt(0)\n"
    "s_sub_u32 s66, s66, 1\n"
    "s_cbranch_scc0 Lspin_17_%=\n"
    "Lok_16_%=:\n"
    ST(8)
    "buffer_load_dwordx4 v[60:63], v1, s[8:11], s85 offen offset:0 sc1\n"
    "buffer_load_dwordx4 v[64:67], v1, s[8:11], s85 offen offset:1024 sc1\n"
    "buffer_load_dwordx4 v[68:71], v1, s[8:11], s85 offen offset:2048 sc1\n"
    "s_waitcnt lgkmcnt(0)\n"
    "s_nop 0\n"
    "v_mov_b32 v24, 0xc0135761\n"
    "v_mov_b32 v25, 0xbdd2d3e8\n"
    "v_pk_fma_f32 v[204:205], v[32:33], v[48:49], v[204:205] op_sel_hi:[0,1,1] neg_lo:[1,0,0] neg_hi:[1,0,0]\n"
    "v_pk_fma_f32 v[206:207], v[32:33], v[50:51], v[206:207] op_sel_hi:[0,1,1] neg_lo:[1,0,0] neg_hi:[1,0,0]\n"
    "v_pk_mul_f32 v[204:205], v[204:205], v[32:33] op_sel:[0,1] op_sel_hi:[1,1]\n"
    "v_pk_mul_f32 v[206:207], v[206:207], v[32:33] op_sel:[0,1] op_sel_hi:[1,1]\n"
    "v_pk_mul_f32 v[52:53], v[204:205], v[204:205]\n"
    "buffer_load_dwordx4 v[72:75], v1, s[8:11], s85 offen offset:3072 sc1\n"
    "v_pk_mul_f32 v[54:55], v[206:207], v[206:207]\n"
    "v_pk_fma_f32 v[52:53], v[52:53], v[24:25], v[24:25] op_sel:[0,1,0] op_sel_hi:[1,1,0]\n"
    "v_pk_fma_f32 v[54:55], v[54:55], v[24:25], v[24:25] op_sel:[0,1,0] op_sel_hi:[1,1,0]\n"
    "v_pk_mul_f32 v[52:53], v[204:205], v[52:53]\n"
    "v_pk_mul_f32 v[54:55], v[206:207], v[54:55]\n"
    "v_exp_f32 v52, v52\n"
    "v_exp_f32 v53, v53\n"
    "v_exp_f32 v54, v54\n"
    "v_exp_f32 v55, v55\n"
    "buffer_load_dwordx4 v[76:79], v16, s[8:11], s85 offen offset:0 sc1\n"
    "v_pk_add_f32 v[52:53], v[52:53], 1.0 op_sel_hi:[1,0]\n"
    "v_pk_add_f32 v[54:55], v[54:55], 1.0 op_sel_hi:[1,0]\n"
    "v_rcp_f32 v52, v52\n"
    "v_rcp_f32 v53, v53\n"
    "v_rcp_f32 v54, v54\n"
    "v_rcp_f32 v55, v55\n"
    "v_pk_mul_f32 v[204:205], v[204:205], v[52:53]\n"
    "v_pk_mul_f32 v[206:207], v[206:207], v[54:55]\n"
    "v_cvt_pk_bf16_f32 v204, v204, v205\n"
    "buffer_load_dwordx4 v[80:83], v16, s[8:11], s85 offen offset:1024 sc1\n"
    "v_cvt_pk_bf16_f32 v205, v206, v207\n"
    "buffer_store_dwordx2 v[204:205], v12, s[8:11], s77 offen sc1\n"
    "s_add_u32 s77, s77, 28672\n"
    "v_pk_fma_f32 v[208:209], v[34:35], v[48:49], v[208:209] op_sel_hi:[0,1,1] neg_lo:[1,0,0] neg_hi:[1,0,0]\n"
    "v_pk_fma_f32 v[210:211], v[34:35], v[50:51], v[210:211] op_sel_hi:[0,1,1] neg_lo:[1,0,0] neg_hi:[1,0,0]\n"
    "v_pk_mul_f32 v[208:209], v[208:209], v[34:35] op_sel:[0,1] op_sel_hi:[1,1]\n"
    "v_pk_mul_f32 v[210:211], v[210:211], v[34:35] op_sel:[0,1] op_sel_hi:[1,1]\n"
    "v_pk_mul_f32 v[56:57], v[208:209], v[208:209]\n"
    "v_pk_mul_f32 v[58:59], v[210:211], v[210:211]\n"
    "v_pk_fma_f32 v[56:57], v[56:57], v[24:25], v[24:25] op_sel:[0,1,0] op_sel_hi:[1,1,0]\n"
    "v_pk_fma_f32 v[58:59], v[58:59], v[24:25], v[24:25] op_sel:[0,1,0] op_sel_hi:[1,1,0]\n"
    "v_pk_mul_f32 v[56:57], v[208:209], v[56:57]\n"
    "v_pk_mul_f32 v[58:59], v[210:211], v[58:59]\n"
    "buffer_load_dwordx4 v[84:87], v16, s[8:11], s85 offen offset:2048 sc1\n"
    "s_add_u32 s85, s85, 7168\n"
    "v_exp_f32 v56, v56\n"
    "v_exp_f32 v57, v57\n"
    "v_exp_f32 v58, v58\n"
    "v_exp_f32 v59, v59\n"
    "v_pk_add_f32 v[56:57], v[56:57], 1.0 op_sel_hi:[1,0]\n"
    "v_pk_add_f32 v[58:59], v[58:59], 1.0 op_sel_hi:[1,0]\n"
    "v_rcp_f32 v56, v56\n"
    "v_rcp_f32 v57, v57\n"
    "buffer_load_dwordx4 v[88:91], v1, s[8:11], s85 offen offset:0 sc1\n"
    "v_rcp_f32 v58, v58\n"
    "v_rcp_f32 v59, v59\n"
    "v_pk_mul_f32 v[208:209], v[208:209], v[56:57]\n"
    "v_pk_mul_f32 v[210:211], v[210:211], v[58:59]\n"
    "v_cvt_pk_bf16_f32 v208, v208, v209\n"
    "v_cvt_pk_bf16_f32 v209, v210, v211\n"
    "buffer_store_dwordx2 v[208:209], v12, s[8:11], s77 offen sc1\n"
    "s_add_u32 s77, s77, 28672\n"
    "v_pk_fma_f32 v[212:213], v[36:37], v[48:49], v[212:213] op_sel_hi:[0,1,1] neg_lo:[1,0,0] neg_hi:[1,0,0]\n"
    "v_pk_fma_f32 v[214:215], v[36:37], v[50:51], v[214:215] op_sel_hi:[0,1,1] neg_lo:[1,0,0] neg_hi:[1,0,0]\n"
    "buffer_load_dwordx4 v[92:95], v1, s[8:11], s85 offen offset:1024 sc1\n"
    "v_pk_mul_f32 v[212:213], v[212:213], v[36:37] op_sel:[0,1] op_sel_hi:[1,1]\n"
    "v_pk_mul_f32 v[214:215], v[214:215], v[36:37] op_sel:[0,1] op_sel_hi:[1,1]\n"
    "v_pk_mul_f32 v[52:53], v[212:213], v[212:213]\n"
    "v_pk_mul_f32 v[54:55], v[214:215], v[214:215]\n"
    "v_pk_fma_f32 v[52:53], v[52:53], v[24:25], v[24:25] op_sel:[0,1,0] op_sel_hi:[1,1,0]\n"
    "v_pk_fma_f32 v[54:55], v[54:55], v[24:25], v[24:25] op_sel:[0,1,0] op_sel_hi:[1,1,0]\n"
    "v_pk_mul_f32 v[52:53], v[212:213], v[52:53]\n"
    "v_pk_mul_f32 v[54:55], v[214:215], v[54:55]\n"
    "v_exp_f32 v52, v52\n"
    "v_exp_f32 v53, v53\n"
    "v_exp_f32_e64 v54, v54\n"
    "buffer_load_dwordx4 v[96:99], v1, s[8:11], s85 offen offset:2048 sc1\n"
    "v_exp_f32_e64 v55, v55\n"
    "v_pk_add_f32 v[52:53], v[52:53], 1.0 op_sel_hi:[1,0]\n"
    "v_pk_add_f32 v[54:55], v[54:55], 1.0 op_sel_hi:[1,0]\n"
    "v_rcp_f32 v52, v52\n"
    "v_rcp_f32 v53, v53\n"
    "v_rcp_f32 v54, v54\n"
    "v_rcp_f32 v55, v55\n"
    "buffer_load_dwordx4 v[100:103], v1, s[8:11], s85 offen offset:3072 sc1\n"
    "v_pk_mul_f32 v[212:213], v[212:213], v[52:53]\n"
    "v_pk_mul_f32 v[214:215], v[214:215], v[54:55]\n"
    "v_cvt_pk_bf16_f32 v212, v212, v213\n"
    "v_cvt_pk_bf16_f32 v213, v214, v215\n"
    "buffer_store_dwordx2 v[212:213], v12, s[8:11], s77 offen sc1\n"
    "s_add_u32 s77, s77, 28672\n"
    "v_pk_fma_f32 v[216:217], v[38:39], v[48:49], v[216:217] op_sel_hi:[0,1,1] neg_lo:[1,0,0] neg_hi:[1,0,0]\n"
    "v_pk_fma_f32 v[218:219], v[38:39], v[50:51], v[218:219] op_sel_hi:[0,1,1] neg_lo:[1,0,0] neg_hi:[1,0,0]\n"
    "v_pk_mul_f32 v[216:217], v[216:217], v[38:39] op_sel:[0,1] op_sel_hi:[1,1]\n"
    "v_pk_mul_f32 v[218:219], v[218:219], v[38:39] op_sel:[0,1] op_sel_hi:[1,1]\n"
    "v_pk_mul_f32 v[56:57], v[216:217], v[216:217]\n"
    "v_pk_mul_f32 v[58:59], v[218:219], v[218:219]\n"
    "buffer_load_dwordx4 v[104:107], v16, s[8:11], s85 offen offset:0 sc1\n"
    "v_pk_fma_f32 v[56:57], v[56:57], v[24:25], v[24:25] op_sel:[0,1,0] op_sel_hi:[1,1,0]\n"
    "v_pk_fma_f32 v[58:59], v[58:59], v[24:25], v[24:25] op_sel:[0,1,0] op_sel_hi:[1,1,0]\n"
    "v_pk_mul_f32 v[56:57], v[216:217], v[56:57]\n"
    "v_pk_mul_f32 v[58:59], v[218:219], v[58:59]\n"
    "v_exp_f32 v56, v56\n"
    "v_exp_f32 v57, v57\n"
    "v_exp_f32 v58, v58\n"
    "v_exp_f32 v59, v59\n"
    "v_pk_add_f32 v[56:57], v[56:57], 1.0 op_sel_hi:[1,0]\n"
    "buffer_load_dwordx4 v[108:111], v16, s[8:11], s85 offen offset:1024 sc1\n"
    "v_pk_add_f32 v[58:59], v[58:59], 1.0 op_sel_hi:[1,0]\n"
    "v_rcp_f32 v56, v56\n"
    "v_rcp_f32 v57, v57\n"
    "v_rcp_f32 v58, v58\n"
    "v_rcp_f32 v59, v59\n"
    "v_pk_mul_f32 v[216:217], v[216:217], v[56:57]\n"
    "v_pk_mul_f32 v[218:219], v[218:219], v[58:59]\n"
    "v_cvt_pk_bf16_f32 v216, v216, v217\n"
    "v_cvt_pk_bf16_f32 v217, v218, v219\n"
    "buffer_load_dwordx4 v[112:115], v16, s[8:11], s85 offen offset:2048 sc1\n"
    "s_add_u32 s85, s85, 7168\n"
    "buffer_store_dwordx2 v[216:217], v12, s[8:11], s77 offen sc1\n"
    "s_add_u32 s77, s77, 28672\n"
    "v_pk_fma_f32 v[220:221], v[40:41], v[48:49], v[220:221] op_sel_hi:[0,1,1] neg_lo:[1,0,0] neg_hi:[1,0,0]\n"
    "v_pk_fma_f32 v[222:223], v[40:41], v[50:51], v[222:223] op_sel_hi:[0,1,1] neg_lo:[1,0,0] neg_hi:[1,0,0]\n"
    "v_pk_mul_f32 v[220:221], v[220:221], v[40:41] op_sel:[0,1] op_sel_hi:[1,1]\n"
    "v_pk_mul_f32 v[222:223], v[222:223], v[40:41] op_sel:[0,1] op_sel_hi:[1,1]\n"
    "v_pk_mul_f32 v[52:53], v[220:221], v[220:221]\n"
    "v_pk_mul_f32 v[54:55], v[222:223], v[222:223]\n"
    "v_pk_fma_f32 v[52:53], v[52:53], v[24:25], v[24:25] op_sel:[0,1,0] op_sel_hi:[1,1,0]\n"
    "v_pk_fma_f32 v[54:55], v[54:55], v[24:25], v[24:25] op_sel:[0,1,0] op_sel_hi:[1,1,0]\n"
    "v_pk_mul_f32 v[52:53], v[220:221], v[52:53]\n"
    "v_pk_mul_f32 v[54:55], v[222:223], v[54:55]\n"
    "v_exp_f32_e64 v52, v52\n"
    "buffer_load_dwordx4 v[116:119], v1, s[8:11], s85 offen offset:0 sc1\n"
    "v_exp_f32 v53, v53\n"
    "v_exp_f32 v54, v54\n"
    "v_exp_f32_e64 v55, v55\n"
    "v_pk_add_f32 v[52:53], v[52:53], 1.0 op_sel_hi:[1,0]\n"
    "v_pk_add_f32 v[54:55], v[54:55], 1.0 op_sel_hi:[1,0]\n"
    "v_rcp_f32 v52, v52\n"
    "v_rcp_f32 v53, v53\n"
    "buffer_load_dwordx4 v[120:123], v1, s[8:11], s85 offen offset:1024 sc1\n"
    "v_rcp_f32 v54, v54\n"
    "v_rcp_f32 v55, v55\n"
    "v_pk_mul_f32 v[220:221], v[220:221], v[52:53]\n"
    "v_pk_mul_f32 v[222:223], v[222:223], v[54:55]\n"
    "v_cvt_pk_bf16_f32 v220, v220, v221\n"
    "v_cvt_pk_bf16_f32 v221, v222, v223\n"
    "buffer_store_dwordx2 v[220:221], v12, s[8:11], s77 offen sc1\n"
    "s_add_u32 s77, s77, 28672\n"
    "v_pk_fma_f32 v[224:225], v[42:43], v[48:49], v[224:225] op_sel_hi:[0,1,1] neg_lo:[1,0,0] neg_hi:[1,0,0]\n"
    "v_pk_fma_f32 v[226:227], v[42:43], v[50:51], v[226:227] op_sel_hi:[0,1,1] neg_lo:[1,0,0] neg_hi:[1,0,0]\n"
    "v_pk_mul_f32 v[224:225], v[224:225], v[42:43] op_sel:[0,1] op_sel_hi:[1,1]\n"
    "buffer_load_dwordx4 v[124:127], v1, s[8:11], s85 offen offset:2048 sc1\n"
    "v_pk_mul_f32 v[226:227], v[226:227], v[42:43] op_sel:[0,1] op_sel_hi:[1,1]\n"
    "v_pk_mul_f32 v[56:57], v[224:225], v[224:225]\n"
    "v_pk_mul_f32 v[58:59], v[226:227], v[226:227]\n"
    "v_pk_fma_f32 v[56:57], v[56:57], v[24:25], v[24:25] op_sel:[0,1,0] op_sel_hi:[1,1,0]\n"
    "v_pk_fma_f32 v[58:59], v[58:59], v[24:25], v[24:25] op_sel:[0,1,0] op_sel_hi:[1,1,0]\n"
    "v_pk_mul_f32 v[56:57], v[224:225], v[56:57]\n"
    "v_pk_mul_f32 v[58:59], v[226:227], v[58:59]\n"
    "v_exp_f32 v56, v56\n"
    "v_exp_f32 v57, v57\n"
    "v_exp_f32_e64 v58, v58\n"
    "buffer_load_dwordx4 v[128:131], v1, s[8:11], s85 offen offset:3072 sc1\n"
    "v_exp_f32_e64 v59, v59\n"
    "v_pk_add_f32 v[56:57], v[56:57], 1.0 op_sel_hi:[1,0]\n"
    "v_pk_add_f32 v[58:59], v[58:59], 1.0 op_sel_hi:[1,0]\n"
    "v_rcp_f32 v56, v56\n"
    "v_rcp_f32 v57, v57\n"
    "v_rcp_f32 v58, v58\n"
    "v_rcp_f32 v59, v59\n"
    "v_pk_mul_f32 v[224:225], v[224:225], v[56:57]\n"
    "buffer_load_dwordx4 v[132:135], v16, s[8:11], s85 offen offset:0 sc1\n"
    "v_pk_mul_f32 v[226:227], v[226:227], v[58:59]\n"
    "v_cvt_pk_bf16_f32 v224, v224, v225\n"
    "v_cvt_pk_bf16_f32 v225, v226, v227\n"
    "buffer_store_dwordx2 v[224:225], v12, s[8:11], s77 offen sc1\n"
    "s_add_u32 s77, s77, 28672\n"
    "v_pk_fma_f32 v[228:229], v[44:45], v[48:49], v[228:229] op_sel_hi:[0,1,1] neg_lo:[1,0,0] neg_hi:[1,0,0]\n"
    "v_pk_fma_f32 v[230:231], v[44:45], v[50:51], v[230:231] op_sel_hi:[0,1,1] neg_lo:[1,0,0] neg_hi:[1,0,0]\n"
    "v_pk_mul_f32 v[228:229], v[228:229], v[44:45] op_sel:[0,1] op_sel_hi:[1,1]\n"
    "v_pk_mul_f32 v[230:231], v[230:231], v[44:45] op_sel:[0,1] op_sel_hi:[1,1]\n"
    "v_pk_mul_f32 v[52:53], v[228:229], v[228:229]\n"
    "v_pk_mul_f32 v[54:55], v[230:231], v[230:231]\n"
    "v_pk_fma_f32 v[52:53], v[52:53], v[24:25], v[24:25] op_sel:[0,1,0] op_sel_hi:[1,1,0]\n"
    "v_pk_fma_f32 v[54:55], v[54:55], v[24:25], v[24:25] op_sel:[0,1,0] op_sel_hi:[1,1,0]\n"
    "buffer_load_dwordx4 v[136:139], v16, s[8:11], s85 offen offset:1024 sc1\n"
    "v_pk_mul_f32 v[52:53], v[228:229], v[52:53]\n"
    "v_pk_mul_f32 v[54:55], v[230:231], v[54:55]\n"
    "v_exp_f32 v52, v52\n"
    "v_exp_f32 v53, v53\n"
    "v_exp_f32 v54, v54\n"
    "v_exp_f32 v55, v55\n"
    "v_pk_add_f32 v[52:53], v[52:53], 1.0 op_sel_hi:[1,0]\n"
    "v_pk_add_f32 v[54:55], v[54:55], 1.0 op_sel_hi:[1,0]\n"
    "v_rcp_f32_e64 v52, v52\n"
    "buffer_load_dwordx4 v[140:143], v16, s[8:11], s85 offen offset:2048 sc1\n"
    "s_add_u32 s85, s85, 7168\n"
    "v_rcp_f32 v53, v53\n"
    "v_rcp_f32 v54, v54\n"
    "v_rcp_f32_e64 v55, v55\n"
    "v_pk_mul_f32 v[228:229], v[228:229], v[52:53]\n"
    "v_pk_mul_f32 v[230:231], v[230:231], v[54:55]\n"
    "v_cvt_pk_bf16_f32 v228, v228, v229\n"
    "v_cvt_pk_bf16_f32 v229, v230, v231\n"
    "buffer_store_dwordx2 v[228:229], v12, s[8:11], s77 offen sc1\n"
    "s_add_u32 s77, s77, 28672\n"
    "v_pk_fma_f32 v[232:233], v[46:47], v[48:49], v[232:233] op_sel_hi:[0,1,1] neg_lo:[1,0,0] neg_hi:[1,0,0]\n"
    "v_pk_fma_f32 v[234:235], v[46:47], v[50:51], v[234:235] op_sel_hi:[0,1,1] neg_lo:[1,0,0] neg_hi:[1,0,0]\n"
    "v_pk_mul_f32 v[232:233], v[232:233], v[46:47] op_sel:[0,1] op_sel_hi:[1,1]\n"
    "v_pk_mul_f32 v[234:235], v[234:235], v[46:47] op_sel:[0,1] op_sel_hi:[1,1]\n"
    "v_pk_mul_f32 v[56:57], v[232:233], v[232:233]\n"
    "v_pk_mul_f32 v[58:59], v[234:235], v[234:235]\n"
    "v_pk_fma_f32 v[56:57], v[56:57], v[24:25], v[24:25] op_sel:[0,1,0] op_sel_hi:[1,1,0]\n"
    "v_pk_fma_f32 v[58:59], v[58:59], v[24:25], v[24:25] op_sel:[0,1,0] op_sel_hi:[1,1,0]\n"
    "v_pk_mul_f32 v[56:57], v[232:233], v[56:57]\n"
    "v_pk_mul_f32 v[58:59], v[234:235], v[58:59]\n"
    "v_exp_f32 v56, v56\n"
    "v_exp_f32 v57, v57\n"
    "v_exp_f32 v58, v58\n"
    "v_exp_f32 v59, v59\n"
    "v_pk_add_f32 v[56:57], v[56:57], 1.0 op_sel_hi:[1,0]\n"
    "v_pk_add_f32 v[58:59], v[58:59], 1.0 op_sel_hi:[1,0]\n"
    "v_rcp_f32 v56, v56\n"
    "v_rcp_f32 v57, v57\n"
    "v_rcp_f32 v58, v58\n"
    "v_rcp_f32 v59, v59\n"
    "v_pk_mul_f32 v[232:233], v[232:233], v[56:57]\n"
    "v_pk_mul_f32 v[234:235], v[234:235], v[58:59]\n"
    "v_cvt_pk_bf16_f32 v232, v232, v233\n"
    "v_cvt_pk_bf16_f32 v233, v234, v235\n"
    "buffer_store_dwordx2 v[232:233], v12, s[8:11], s77 offen sc1\n"
    "s_add_u32 s77, s77, 28672\n"
    "s_branch LG_4_%=\n"
    "Lslow_13_%=:\n"
    "s_mov_b32 s66, 0x10000\n"
    "Lloop_15_%=:\n"
    "buffer_load_dwordx4 v[24:27], v80, s[8:11], 0 offen sc1\n"
    "buffer_load_dwordx2 v[28:29], v82, s[8:11], 0 offen sc1\n"
    "s_waitcnt vmcnt(0)\n"
    "v_cmp_ne_u32 vcc, s46, v25\n"
    "v_cmp_ne_u32 s[64:65], s46, v27\n"
    "s_or_b64 vcc, vcc, s[64:65]\n"
    "v_cmp_ne_u32 s[64:65], s46, v29\n"
    "s_or_b64 vcc, vcc, s[64:65]\n"
    "s_cbranch_vccz Lback_14_%=\n"
    "s_sub_u32 s66, s66, 1\n"
    "s_cbranch_scc0 Lloop_15_%=\n"
    "s_branch Lback_14_%=\n"
    "LZ_2_%=:\n"
    "s_add_u32 s53, s53, 229376\n"
    "s_lshr_b32 s73, s45, 1\n"
    "s_cmp_eq_u32 s3, 1\n"
    "s_cselect_b32 s63, 1024, 0\n"
    "s_sub_u32 s51, s51, s63\n"
    "s_mul_i32 s66, s63, 1792\n"
    "s_sub_u32 s53, s53, s66\n"
    "s_and_b32 s74, s45, 1\n"
    "s_lshl_b32 s70, s73, 13\n"
    "s_add_u32 m0, s49, 38784\n"
    "v_add_u32 v208, s70, v11\n"
    "buffer_load_dwordx4 v252, s[4:7], s51 offen offset:128 lds\n"
    "s_lshl_b32 s70, s74, 13\n"
    "s_add_u32 m0, s49, 39808\n"
    "v_add_u32 v212, s70, v10\n"
    "buffer_load_dwordx4 v253, s[4:7], s51 offen offset:128 lds\n"
    "v_xor_b32_e64 v209, 64, v208\n"
    "s_add_u32 m0, s49, 39808\n"
    "v_xor_b32_e64 v213, 32, v212\n"
    "buffer_load_dwordx4 v252, s[4:7], s51 offen offset:1152 lds\n"
    "v_xor_b32_e64 v214, 64, v212\n"
    "s_add_u32 m0, s49, 40832\n"
    "v_xor_b32 v215, 0x60, v212\n"
    "buffer_load_dwordx4 v253, s[4:7], s51 offen offset:1152 lds\n"
    "v_add_u32 v210, 0x10000, v208\n"
    "s_add_u32 m0, s48, 55296\n"
    "v_add_u32 v211, 0x10000, v209\n"
    "buffer_load_dwordx4 v250, s[16:19], s53 offen offset:0 lds\n"
    "v_add_u32 v216, 0x10000, v212\n"
    "s_add_u32 m0, s48, 56320\n"
    "v_add_u32 v217, 0x10000, v213\n"
    "buffer_load_dwordx4 v251, s[16:19], s53 offen offset:0 lds\n"
    "v_add_u32 v218, 0x10000, v214\n"
    "s_add_u32 m0, s48, 63360\n"
    "v_add_u32 v219, 0x10000, v215\n"
    "buffer_load_dwordx4 v250, s[16:19], s53 offen offset:128 lds\n"
    "s_add_u32 m0, s48, 64384\n"
    "s_nop 0\n"
    "buffer_load_dwordx4 v251, s[16:19], s53 offen offset:128 lds\n"
    "s_add_u32 s53, s53, 229376\n"
    "s_cmp_eq_u32 s3, 2\n"
    "s_cselect_b32 s63, 1024, 0\n"
    "s_sub_u32 s51, s51, s63\n"
    "s_mul_i32 s66, s63, 1792\n"
    "s_sub_u32 s53, s53, s66\n"
    "s_waitcnt vmcnt(8)\n"
    ST(61)
    "s_barrier\n"
    "ds_read_b64_tr_b16 v[128:129], v212 offset:22528\n"
    "ds_read_b64_tr_b16 v[130:131], v214 offset:23040\n"
    "ds_read_b64_tr_b16 v[132:133], v213 offset:22528\n"
    "ds_read_b64_tr_b16 v[134:135], v215 offset:23040\n"
    "ds_read_b64_tr_b16 v[136:137], v214 offset:22528\n"
    "ds_read_b64_tr_b16 v[138:139], v212 offset:23040\n"
    "ds_read_b64_tr_b16 v[140:141], v215 offset:22528\n"
    "ds_read_b64_tr_b16 v[142:143], v213 offset:23040\n"
    "ds_read_b128 a[0:3], v208 offset:6144\n"
    "ds_read_b128 a[4:7], v208 offset:8192\n"
    "ds_read_b128 a[8:11], v208 offset:10240\n"
    "ds_read_b128 a[12:15], v208 offset:12288\n"
    "s_waitcnt lgkmcnt(0)\n"
    "v_mfma_f32_16x16x32_bf16 v[64:67], a[0:3], v[128:131], 0\n"
    "ds_read_b64_tr_b16 v[144:145], v212 offset:26624\n"
    "s_lshl_b32 s70, s74, 6\n"
    "s_add_u32 m0, s49, 71424\n"
    "v_mfma_f32_16x16x32_bf16 v[68:71], a[0:3], v[132:135], 0\n"
    "buffer_load_dwordx4 v252, s[4:7], s51 offen offset:256 lds\n"
    "ds_read_b64_tr_b16 v[146:147], v214 offset:27136\n"
    "v_add_u32_e64 v24, s70, v0\n"
    "v_mfma_f32_16x16x32_bf16 v[72:75], a[0:3], v[136:139], 0\n"
    "ds_read_b64_tr_b16 v[148:149], v213 offset:26624\n"
    "v_lshlrev_b32_e64 v221, 2, v24\n"
    "s_add_u32 m0, s49, 72448\n"
    "v_mfma_f32_16x16x32_bf16 v[76:79], a[0:3], v[140:143], 0\n"
    "buffer_load_dwordx4 v253, s[4:7], s51 offen offset:256 lds\n"
    "ds_read_b64_tr_b16 v[150:151], v215 offset:27136\n"
    "v_lshlrev_b32_e64 v225, 4, v24\n"
    "v_mfma_f32_16x16x32_bf16 v[80:83], a[4:7], v[128:131], 0\n"
    "ds_read_b64_tr_b16 v[152:153], v214 offset:26624\n"
    "v_and_b32 v25, 0x60, v24\n"
    "s_add_u32 m0, s49, 72448\n"
    "v_mfma_f32_16x16x32_bf16 v[84:87], a[4:7], v[132:135], 0\n"
    "buffer_load_dwordx4 v252, s[4:7], s51 offen offset:1280 lds\n"
    "ds_read_b64_tr_b16 v[154:155], v212 offset:27136\n"
    "v_and_b32_e64 v26, 15, v24\n"
    "v_mfma_f32_16x16x32_bf16 v[88:91], a[4:7], v[136:139], 0\n"
    "ds_read_b64_tr_b16 v[156:157], v215 offset:26624\n"
    "v_lshl_add_u32 v25, v26, 1, v25\n"
    "s_add_u32 m0, s49, 73472\n"
    "v_mfma_f32_16x16x32_bf16 v[92:95], a[4:7], v[140:143], 0\n"
    "buffer_load_dwordx4 v253, s[4:7], s51 offen offset:1280 lds\n"
    "ds_read_b64_tr_b16 v[158:159], v213 offset:27136\n"
    "v_bfe_u32 v26, v24, 4, 1\n"
    "v_mfma_f32_16x16x32_bf16 v[96:99], a[8:11], v[128:131], 0\n"
    "ds_read_b128 a[16:19], v209 offset:6144\n"
    "v_add_u32_e64 v25, v25, v26\n"
    "s_add_u32 m0, s48, 88064\n"
    "v_mfma_f32_16x16x32_bf16 v[100:103], a[8:11], v[132:135], 0\n"
    "buffer_load_dwordx4 v250, s[16:19], s53 offen offset:0 lds\n"
    "ds_read_b128 a[20:23], v209 offset:8192\n"
    "s_lshl_b32 s67, s59, 7\n"
    "v_mfma_f32_16x16x32_bf16 v[104:107], a[8:11], v[136:139], 0\n"
    "ds_read_b128 a[24:27], v209 offset:10240\n"
    "v_add_u32 v25, s67, v25\n"
    "s_add_u32 m0, s48, 89088\n"
    "v_mfma_f32_16x16x32_bf16 v[108:111], a[8:11], v[140:143], 0\n"
    "buffer_load_dwordx4 v251, s[16:19], s53 offen offset:0 lds\n"
    "ds_read_b128 a[28:31], v209 offset:12288\n"
    "v_lshlrev_b32_e64 v25, 4, v25\n"
    "v_mfma_f32_16x16x32_bf16 v[112:115], a[12:15], v[128:131], 0\n"
    "v_add_u32 v220, 331776, v25\n"
    "s_lshl1_add_u32 s71, s60, s74\n"
    "s_add_u32 m0, s48, 96128\n"
    "v_mfma_f32_16x16x32_bf16 v[116:119], a[12:15], v[132:135], 0\n"
    "buffer_load_dwordx4 v250, s[16:19], s53 offen offset:128 lds\n"
    "s_lshl_b32 s71, s71, 9\n"
    "s_add_u32 s71, s71, 384000\n"
    "v_mfma_f32_16x16x32_bf16 v[120:123], a[12:15], v[136:139], 0\n"
    "v_add_u32 v222, s71, v12\n"
    "s_lshl_b32 s71, s60, 10\n"
    "s_add_u32 m0, s48, 97152\n"
    "v_mfma_f32_16x16x32_bf16 v[124:127], a[12:15], v[140:143], 0\n"
    "buffer_load_dwordx4 v251, s[16:19], s53 offen offset:128 lds\n"
    "s_add_u32 s53, s53, 229376\n"
    "s_add_u32 s71, s71, 376832\n"
    "s_waitcnt vmcnt(8) lgkmcnt(0)\n"
    ST(16)
    "s_barrier\n"
    "v_mfma_f32_16x16x32_bf16 v[64:67], a[16:19], v[144:147], v[64:67]\n"
    "ds_read_b64_tr_b16 v[128:129], v212 offset:55296\n"
    "v_add_u32 v231, s71, v12\n"
    "v_xor_b32 v226, s70, v10\n"
    "v_mfma_f32_16x16x32_bf16 v[68:71], a[16:19], v[148:151], v[68:71]\n"
    "ds_read_b64_tr_b16 v[130:131], v214 offset:55808\n"
    "s_cmp_eq_u32 s3, 3\n"
    "s_cselect_b32 s63, 1024, 0\n"
    "s_sub_u32 s51, s51, s63\n"
    "s_mul_i32 s66, s63, 1792\n"
    "s_sub_u32 s53, s53, s66\n"
    "v_mfma_f32_16x16x32_bf16 v[72:75], a[16:19], v[152:155], v[72:75]\n"
    "ds_read_b64_tr_b16 v[132:133], v213 offset:55296\n"
    "v_add_u32 v226, 104448, v226\n"
    "v_xor_b32 v227, 32, v226\n"
    "v_mfma_f32_16x16x32_bf16 v[76:79], a[16:19], v[156:159], v[76:79]\n"
    "ds_read_b64_tr_b16 v[134:135], v215 offset:55808\n"
    "v_xor_b32_e64 v228, 64, v226\n"
    "s_add_u32 m0, s49, 5760\n"
    "v_mfma_f32_16x16x32_bf16 v[80:83], a[20:23], v[144:147], v[80:83]\n"
    "buffer_load_dwordx4 v252, s[4:7], s51 offen offset:384 lds\n"
    "ds_read_b64_tr_b16 v[136:137], v214 offset:55296\n"
    "v_xor_b32 v229, 0x60, v226\n"
    "v_mfma_f32_16x16x32_bf16 v[84:87], a[20:23], v[148:151], v[84:87]\n"
    "ds_read_b64_tr_b16 v[138:139], v212 offset:55808\n"
    "s_lshl_b32 s70, s73, 8\n"
    "s_add_u32 m0, s49, 6784\n"
    "v_mfma_f32_16x16x32_bf16 v[88:91], a[20:23], v[152:155], v[88:91]\n"
    "buffer_load_dwordx4 v253, s[4:7], s51 offen offset:384 lds\n"
    "ds_read_b64_tr_b16 v[140:141], v215 offset:55296\n"
    "v_add_u32 v223, s70, v247\n"
    "v_mfma_f32_16x16x32_bf16 v[92:95], a[20:23], v[156:159], v[92:95]\n"
    "ds_read_b64_tr_b16 v[142:143], v213 offset:55808\n"
    "v_add_u32_e64 v232, s70, v14\n"
    "v_bfe_u32 v24, v0, 3, 1\n"
    "v_mfma_f32_16x16x32_bf16 v[96:99], a[24:27], v[144:147], v[96:99]\n"
    "ds_read_b128 a[32:35], v208 offset:38912\n"
    "v_bfe_u32 v25, v0, 2, 1\n"
    "s_add_u32 m0, s49, 6784\n"
    "v_mfma_f32_16x16x32_bf16 v[100:103], a[24:27], v[148:151], v[100:103]\n"
    "buffer_load_dwordx4 v252, s[4:7], s51 offen offset:1408 lds\n"
    "ds_read_b128 a[36:39], v208 offset:40960\n"
    "v_lshl_add_u32 v24, v24, 9, v223\n"
    "v_mfma_f32_16x16x32_bf16 v[104:107], a[24:27], v[152:155], v[104:107]\n"
    "ds_read_b128 a[40:43], v208 offset:43008\n"
    "v_lshl_add_u32 v24, v25, 7, v24\n"
    "s_lshl_b32 s70, s74, 10\n"
    "v_mfma_f32_16x16x32_bf16 v[108:111], a[24:27], v[156:159], v[108:111]\n"
    "ds_read_b128 a[44:47], v208 offset:45056\n"
    "v_add_u32 v224, s70, v24\n"
    "s_add_u32 m0, s49, 7808\n"
    "v_mfma_f32_16x16x32_bf16 v[112:115], a[28:31], v[144:147], v[112:115]\n"
    "buffer_load_dwordx4 v253, s[4:7], s51 offen offset:1408 lds\n"
    "s_lshl_b32 s70, s73, 10\n"
    "v_add_u32 v230, s70, v1\n"
    "v_mfma_f32_16x16x32_bf16 v[116:119], a[28:31], v[148:151], v[116:119]\n"
    "v_lshlrev_b32 v238, 2, v0\n"
    "s_lshl_b32 s70, s45, 9\n"
    "s_add_u32 m0, s48, 22528\n"
    "v_mfma_f32_16x16x32_bf16 v[120:123], a[28:31], v[152:155], v[120:123]\n"
    "buffer_load_dwordx4 v250, s[16:19], s53 offen offset:0 lds\n"
    "s_add_u32 s70, s70, 4096\n"
    "v_add_u32_e64 v233, s70, v238\n"
    "v_mfma_f32_16x16x32_bf16 v[124:127], a[28:31], v[156:159], v[124:127]\n"
    "v_add_u32 v234, s70, v247\n"
    "s_lshl_b32 s70, s74, 8\n"
    "v_add_u32 v237, s70, v14\n"
    "s_waitcnt lgkmcnt(0)\n"
    "v_mfma_f32_16x16x32_bf16 v[64:67], a[32:35], v[128:131], v[64:67]\n"
    "ds_read_b64_tr_b16 v[144:145], v212 offset:59392\n"
    "s_lshl_b32 s70, s74, 7\n"
    "v_add_u32 v236, s70, v247\n"
    "v_mfma_f32_16x16x32_bf16 v[68:71], a[32:35], v[132:135], v[68:71]\n"
    "ds_read_b64_tr_b16 v[146:147], v214 offset:59904\n"
    "s_mul_i32 s70, s59, 14336\n"
    "s_add_u32 m0, s48, 23552\n"
    "v_mfma_f32_16x16x32_bf16 v[72:75], a[32:35], v[136:139], v[72:75]\n"
    "buffer_load_dwordx4 v251, s[16:19], s53 offen offset:0 lds\n"
    "ds_read_b64_tr_b16 v[148:149], v213 offset:59392\n"
    "s_add_u32 s78, s70, 397312\n"
    "v_mfma_f32_16x16x32_bf16 v[76:79], a[32:35], v[140:143], v[76:79]\n"
    "ds_read_b64_tr_b16 v[150:151], v215 offset:59904\n"
    "s_lshl_b32 s71, s60, 11\n"
    "s_add_u32 s71, s71, s78\n"
    "v_mfma_f32_16x16x32_bf16 v[80:83], a[36:39], v[128:131], v[80:83]\n"
    "ds_read_b64_tr_b16 v[152:153], v214 offset:59392\n"
    "s_cmp_eq_u32 s73, 0\n"
    "s_cselect_b32 s79, s71, 2147483648\n"
    "v_mfma_f32_16x16x32_bf16 v[84:87], a[36:39], v[132:135], v[84:87]\n"
    "ds_read_b64_tr_b16 v[154:155], v212 offset:59904\n"
    "s_lshl3_add_u32 s70, s59, s60\n"
    "s_lshl2_add_u32 s70, s70, s45\n"
    "v_mfma_f32_16x16x32_bf16 v[88:91], a[36:39], v[136:139], v[88:91]\n"
    "ds_read_b64_tr_b16 v[156:157], v215 offset:59392\n"
    "s_lshl_b32 s70, s70, 7\n"
    "s_add_u32 s70, s70, 4096\n"
    "v_mfma_f32_16x16x32_bf16 v[92:95], a[36:39], v[140:143], v[92:95]\n"
    "ds_read_b64_tr_b16 v[158:159], v213 offset:59904\n"
    "v_mov_b32_e64 v235, s70\n"
    "s_add_u32 m0, s48, 30592\n"
    "v_mfma_f32_16x16x32_bf16 v[96:99], a[40:43], v[128:131], v[96:99]\n"
    "buffer_load_dwordx4 v250, s[16:19], s53 offen offset:128 lds\n"
    "ds_read_b128 a[48:51], v209 offset:38912\n"
    "s_lshl1_add_u32 s70, s59, s73\n"
    "v_mfma_f32_16x16x32_bf16 v[100:103], a[40:43], v[132:135], v[100:103]\n"
    "ds_read_b128 a[52:55], v209 offset:40960\n"
    "s_mul_i32 s71, s70, 224\n"
    "s_lshl2_add_u32 s75, s60, s74\n"
    "v_mfma_f32_16x16x32_bf16 v[104:107], a[40:43], v[136:139], v[104:107]\n"
    "ds_read_b128 a[56:59], v209 offset:43008\n"
    "s_lshl1_add_u32 s71, s75, s71\n"
    "s_lshl_b32 s71, s71, 9\n"
    "v_mfma_f32_16x16x32_bf16 v[108:111], a[40:43], v[140:143], v[108:111]\n"
    "ds_read_b128 a[60:63], v209 offset:45056\n"
    "s_add_u32 s77, s71, 4296704\n"
    "s_lshl1_add_u32 s71, s60, s74\n"
    "v_mfma_f32_16x16x32_bf16 v[112:115], a[44:47], v[128:131], v[112:115]\n"
    "s_lshl2_add_u32 s71, s71, 0\n"
    "s_mul_i32 s75, s71, 37\n"
    "s_add_u32 m0, s48, 31616\n"
    "v_mfma_f32_16x16x32_bf16 v[116:119], a[44:47], v[132:135], v[116:119]\n"
    "buffer_load_dwordx4 v251, s[16:19], s53 offen offset:128 lds\n"
    "s_add_u32 s53, s53, 229376\n"
    "s_lshr_b32 s75, s75, 8\n"
    "v_mfma_f32_16x16x32_bf16 v[120:123], a[44:47], v[136:139], v[120:123]\n"
    "s_mul_i32 s76, s75, 7\n"
    "s_sub_u32 s71, s71, s76\n"
    "v_mfma_f32_16x16x32_bf16 v[124:127], a[44:47], v[140:143], v[124:127]\n"
    "s_sub_u32 s76, s71, 1\n"
    "s_cselect_b32 s76, 6, s76\n"
    "s_waitcnt vmcnt(8) lgkmcnt(0)\n"
    ST(17)
    "s_barrier\n"
    "v_mfma_f32_16x16x32_bf16 v[64:67], a[48:51], v[144:147], v[64:67]\n"
    "ds_read_b64_tr_b16 v[128:129], v216 offset:22528\n"
    "s_bitcmp1_b32 s75, 0\n"
    "s_cselect_b32 s71, s76, s71\n"
    "v_mfma_f32_16x16x32_bf16 v[68:71], a[48:51], v[148:151], v[68:71]\n"
    "ds_read_b64_tr_b16 v[130:131], v218 offset:23040\n"
    "s_cmp_eq_u32 s3, 4\n"
    "s_cselect_b32 s63, 1024, 0\n"
    "s_sub_u32 s51, s51, s63\n"
    "s_mul_i32 s66, s63, 1792\n"
    "s_sub_u32 s53, s53, s66\n"
    "v_mfma_f32_16x16x32_bf16 v[72:75], a[48:51], v[152:155], v[72:75]\n"
    "ds_read_b64_tr_b16 v[132:133], v217 offset:22528\n"
    "s_lshl_b32 s75, s75, 5\n"
    "s_add_u32 s75, s75, s70\n"
    "v_mfma_f32_16x16x32_bf16 v[76:79], a[48:51], v[156:159], v[76:79]\n"
    "ds_read_b64_tr_b16 v[134:135], v219 offset:23040\n"
    "s_mul_i32 s75, s75, 14\n"
    "s_add_u32 m0, s49, 38400\n"
    "v_mfma_f32_16x16x32_bf16 v[80:83], a[52:55], v[144:147], v[80:83]\n"
    "buffer_load_dwordx4 v252, s[4:7], s51 offen offset:512 lds\n"
    "ds_read_b64_tr_b16 v[136:137], v218 offset:22528\n"
    "s_add_u32 s71, s71, s75\n"
    "v_mfma_f32_16x16x32_bf16 v[84:87], a[52:55], v[148:151], v[84:87]\n"
    "ds_read_b64_tr_b16 v[138:139], v216 offset:23040\n"
    "s_lshl_b32 s71, s71, 10\n"
    "s_add_u32 m0, s49, 39424\n"
    "v_mfma_f32_16x16x32_bf16 v[88:91], a[52:55], v[152:155], v[88:91]\n"
    "buffer_load_dwordx4 v253, s[4:7], s51 offen offset:512 lds\n"
    "ds_read_b64_tr_b16 v[140:141], v219 offset:22528\n"
    "s_add_u32 s80, s71, 626688\n"
    "v_mfma_f32_16x16x32_bf16 v[92:95], a[52:55], v[156:159], v[92:95]\n"
    "ds_read_b64_tr_b16 v[142:143], v217 offset:23040\n"
    "s_lshl1_add_u32 s71, s60, s74\n"
    "s_lshl2_add_u32 s71, s71, 1\n"
    "v_mfma_f32_16x16x32_bf16 v[96:99], a[56:59], v[144:147], v[96:99]\n"
    "ds_read_b128 a[64:67], v210 offset:6144\n"
    "s_mul_i32 s75, s71, 37\n"
    "s_add_u32 m0, s49, 39424\n"
    "v_mfma_f32_16x16x32_bf16 v[100:103], a[56:59], v[148:151], v[100:103]\n"
    "buffer_load_dwordx4 v252, s[4:7], s51 offen offset:1536 lds\n"
    "ds_read_b128 a[68:71], v210 offset:8192\n"
    "s_lshr_b32 s75, s75, 8\n"
    "v_mfma_f32_16x16x32_bf16 v[104:107], a[56:59], v[152:155], v[104:107]\n"
    "ds_read_b128 a[72:75], v210 offset:10240\n"
    "s_mul_i32 s76, s75, 7\n"
    "s_sub_u32 s71, s71, s76\n"
    "v_mfma_f32_16x16x32_bf16 v[108:111], a[56:59], v[156:159], v[108:111]\n"
    "ds_read_b128 a[76:79], v210 offset:12288\n"
    "s_add_u32 m0, s49, 40448\n"
    "v_mfma_f32_16x16x32_bf16 v[112:115], a[60:63], v[144:147], v[112:115]\n"
    "buffer_load_dwordx4 v253, s[4:7], s51 offen offset:1536 lds\n"
    "s_sub_u32 s76, s71, 1\n"
    "s_cselect_b32 s76, 6, s76\n"
    "v_mfma_f32_16x16x32_bf16 v[116:119], a[60:63], v[148:151], v[116:119]\n"
    "s_bitcmp1_b32 s75, 0\n"
    "s_cselect_b32 s71, s76, s71\n"
    "s_add_u32 m0, s48, 55296\n"
    "v_mfma_f32_16x16x32_bf16 v[120:123], a[60:63], v[152:155], v[120:123]\n"
    "buffer_load_dwordx4 v250, s[16:19], s53 offen offset:0 lds\n"
    "s_lshl_b32 s75, s75, 5\n"
    "s_add_u32 s75, s75, s70\n"
    "v_mfma_f32_16x16x32_bf16 v[124:127], a[60:63], v[156:159], v[124:127]\n"
    "s_mul_i32 s75, s75, 14\n"
    "s_add_u32 s71, s71, s75\n"
    "s_lshl_b32 s71, s71, 10\n"
    "s_waitcnt lgkmcnt(0)\n"
    "v_mfma_f32_16x16x32_bf16 v[64:67], a[64:67], v[128:131], v[64:67]\n"
    "ds_read_b64_tr_b16 v[144:145], v216 offset:26624\n"
    "s_add_u32 s81, s71, 626688\n"
    "s_lshl1_add_u32 s71, s60, s74\n"
    "v_mfma_f32_16x16x32_bf16 v[68:71], a[64:67], v[132:135], v[68:71]\n"
    "ds_read_b64_tr_b16 v[146:147], v218 offset:27136\n"
    "s_lshl2_add_u32 s71, s71, 2\n"
    "s_add_u32 m0, s48, 56320\n"
    "v_mfma_f32_16x16x32_bf16 v[72:75], a[64:67], v[136:139], v[72:75]\n"
    "buffer_load_dwordx4 v251, s[16:19], s53 offen offset:0 lds\n"
    "ds_read_b64_tr_b16 v[148:149], v217 offset:26624\n"
    "s_mul_i32 s75, s71, 37\n"
    "v_mfma_f32_16x16x32_bf16 v[76:79], a[64:67], v[140:143], v[76:79]\n"
    "ds_read_b64_tr_b16 v[150:151], v219 offset:27136\n"
    "s_lshr_b32 s75, s75, 8\n"
    "s_mul_i32 s76, s75, 7\n"
    "v_mfma_f32_16x16x32_bf16 v[80:83], a[68:71], v[128:131], v[80:83]\n"
    "ds_read_b64_tr_b16 v[152:153], v218 offset:26624\n"
    "s_sub_u32 s71, s71, s76\n"
    "v_mfma_f32_16x16x32_bf16 v[84:87], a[68:71], v[132:135], v[84:87]\n"
    "ds_read_b64_tr_b16 v[154:155], v216 offset:27136\n"
    "s_sub_u32 s76, s71, 1\n"
    "s_cselect_b32 s76, 6, s76\n"
    "v_mfma_f32_16x16x32_bf16 v[88:91], a[68:71], v[136:139], v[88:91]\n"
    "ds_read_b64_tr_b16 v[156:157], v219 offset:26624\n"
    "s_bitcmp1_b32 s75, 0\n"
    "s_cselect_b32 s71, s76, s71\n"
    "v_mfma_f32_16x16x32_bf16 v[92:95], a[68:71], v[140:143], v[92:95]\n"
    "ds_read_b64_tr_b16 v[158:159], v217 offset:27136\n"
    "s_lshl_b32 s75, s75, 5\n"
    "s_add_u32 m0, s48, 63360\n"
    "v_mfma_f32_16x16x32_bf16 v[96:99], a[72:75], v[128:131], v[96:99]\n"
    "buffer_load_dwordx4 v250, s[16:19], s53 offen offset:128 lds\n"
    "ds_read_b128 a[80:83], v211 offset:6144\n"
    "s_add_u32 s75, s75, s70\n"
    "v_mfma_f32_16x16x32_bf16 v[100:103], a[72:75], v[132:135], v[100:103]\n"
    "ds_read_b128 a[84:87], v211 offset:8192\n"
    "s_mul_i32 s75, s75, 14\n"
    "s_add_u32 s71, s71, s75\n"
    "v_mfma_f32_16x16x32_bf16 v[104:107], a[72:75], v[136:139], v[104:107]\n"
    "ds_read_b128 a[88:91], v211 offset:10240\n"
    "s_lshl_b32 s71, s71, 10\n"
    "s_add_u32 s82, s71, 626688\n"
    "v_mfma_f32_16x16x32_bf16 v[108:111], a[72:75], v[140:143], v[108:111]\n"
    "ds_read_b128 a[92:95], v211 offset:12288\n"
    "s_lshl1_add_u32 s71, s60, s74\n"
    "s_lshl2_add_u32 s71, s71, 3\n"
    "v_mfma_f32_16x16x32_bf16 v[112:115], a[76:79], v[128:131], v[112:115]\n"
    "s_mul_i32 s75, s71, 37\n"
    "s_lshr_b32 s75, s75, 8\n"
    "s_add_u32 m0, s48, 64384\n"
    "v_mfma_f32_16x16x32_bf16 v[116:119], a[76:79], v[132:135], v[116:119]\n"
    "buffer_load_dwordx4 v251, s[16:19], s53 offen offset:128 lds\n"
    "s_add_u32 s53, s53, 229376\n"
    "s_mul_i32 s76, s75, 7\n"
    "v_mfma_f32_16x16x32_bf16 v[120:123], a[76:79], v[136:139], v[120:123]\n"
    "s_sub_u32 s71, s71, s76\n"
    "s_sub_u32 s76, s71, 1\n"
    "s_cselect_b32 s76, 6, s76\n"
    "v_mfma_f32_16x16x32_bf16 v[124:127], a[76:79], v[140:143], v[124:127]\n"
    "s_bitcmp1_b32 s75, 0\n"
    "s_cselect_b32 s71, s76, s71\n"
    "s_lshl_b32 s75, s75, 5\n"
    "s_waitcnt vmcnt(8) lgkmcnt(0)\n"
    ST(18)
    "s_barrier\n"
    "v_mfma_f32_16x16x32_bf16 v[64:67], a[80:83], v[144:147], v[64:67]\n"
    "ds_read_b64_tr_b16 v[128:129], v212 offset:22528\n"
    "s_add_u32 s75, s75, s70\n"
    "s_mul_i32 s75, s75, 14\n"
    "v_mfma_f32_16x16x32_bf16 v[68:71], a[80:83], v[148:151], v[68:71]\n"
    "ds_read_b64_tr_b16 v[130:131], v214 offset:23040\n"
    "s_cmp_eq_u32 s3, 5\n"
    "s_cselect_b32 s63, 1024, 0\n"
    "s_sub_u32 s51, s51, s63\n"
    "s_mul_i32 s66, s63, 1792\n"
    "s_sub_u32 s53, s53, s66\n"
    "v_mfma_f32_16x16x32_bf16 v[72:75], a[80:83], v[152:155], v[72:75]\n"
    "ds_read_b64_tr_b16 v[132:133], v213 offset:22528\n"
    "s_add_u32 s71, s71, s75\n"
    "s_lshl_b32 s71, s71, 10\n"
    "v_mfma_f32_16x16x32_bf16 v[76:79], a[80:83], v[156:159], v[76:79]\n"
    "ds_read_b64_tr_b16 v[134:135], v215 offset:23040\n"
    "s_add_u32 s83, s71, 626688\n"
    "s_add_u32 m0, s49, 71040\n"
    "v_mfma_f32_16x16x32_bf16 v[80:83], a[84:87], v[144:147], v[80:83]\n"
    "buffer_load_dwordx4 v252, s[4:7], s51 offen offset:640 lds\n"
    "ds_read_b64_tr_b16 v[136:137], v214 offset:22528\n"
    "v_add_u32 v13, 1, v13\n"
    "v_mfma_f32_16x16x32_bf16 v[84:87], a[84:87], v[148:151], v[84:87]\n"
    "ds_read_b64_tr_b16 v[138:139], v212 offset:23040\n"
    "s_nop 0\n"
    "s_add_u32 m0, s49, 72064\n"
    "v_mfma_f32_16x16x32_bf16 v[88:91], a[84:87], v[152:155], v[88:91]\n"
    "buffer_load_dwordx4 v253, s[4:7], s51 offen offset:640 lds\n"
    "ds_read_b64_tr_b16 v[140:141], v215 offset:22528\n"
    "v_readfirstlane_b32 s46, v13\n"
    "v_mfma_f32_16x16x32_bf16 v[92:95], a[84:87], v[156:159], v[92:95]\n"
    "ds_read_b64_tr_b16 v[142:143], v213 offset:23040\n"
    "s_mul_i32 s69, s62, 7\n"
    "v_and_b32 v24, 3, v0\n"
    "v_mfma_f32_16x16x32_bf16 v[96:99], a[88:91], v[144:147], v[96:99]\n"
    "ds_read_b128 a[96:99], v208 offset:6144\n"
    "v_min_u32_e64 v24, 2, v24\n"
    "s_add_u32 m0, s49, 72064\n"
    "v_mfma_f32_16x16x32_bf16 v[100:103], a[88:91], v[148:151], v[100:103]\n"
    "buffer_load_dwordx4 v252, s[4:7], s51 offen offset:1664 lds\n"
    "ds_read_b128 a[100:103], v208 offset:8192\n"
    "v_mad_u32_u24 v24, v24, 3, s69\n"
    "v_mfma_f32_16x16x32_bf16 v[104:107], a[88:91], v[152:155], v[104:107]\n"
    "ds_read_b128 a[104:107], v208 offset:10240\n"
    "v_bfe_u32 v25, v0, 3, 2\n"
    "s_lshl_b32 s70, s45, 2\n"
    "v_mfma_f32_16x16x32_bf16 v[108:111], a[88:91], v[156:159], v[108:111]\n"
    "ds_read_b128 a[108:111], v208 offset:12288\n"
    "v_add_u32 v25, s70, v25\n"
    "s_add_u32 m0, s49, 73088\n"
    "v_mfma_f32_16x16x32_bf16 v[112:115], a[92:95], v[144:147], v[112:115]\n"
    "buffer_load_dwordx4 v253, s[4:7], s51 offen offset:1664 lds\n"
    "v_lshrrev_b32_e64 v26, 3, v24\n"
    "v_lshl_add_u32 v25, v25, 3, v26\n"
    "v_mfma_f32_16x16x32_bf16 v[116:119], a[92:95], v[148:151], v[116:119]\n"
    "v_bfe_u32 v26, v0, 2, 1\n"
    "v_lshl_add_u32 v25, v25, 1, v26\n"
    "s_add_u32 m0, s48, 88064\n"
    "v_mfma_f32_16x16x32_bf16 v[120:123], a[92:95], v[152:155], v[120:123]\n"
    "buffer_load_dwordx4 v250, s[16:19], s53 offen offset:0 lds\n"
    "v_bfe_u32 v26, v24, 2, 1\n"
    "v_lshl_add_u32 v25, v25, 1, v26\n"
    "v_mfma_f32_16x16x32_bf16 v[124:127], a[92:95], v[156:159], v[124:127]\n"
    "v_lshlrev_b32_e64 v25, 7, v25\n"
    "v_add_u32 v246, 4096, v25\n"
    "s_lshl_b32 s70, s62, 2\n"
    "s_waitcnt lgkmcnt(0)\n"
    "v_mfma_f32_16x16x32_bf16 v[64:67], a[96:99], v[128:131], v[64:67]\n"
    "ds_read_b64_tr_b16 v[144:145], v212 offset:26624\n"
    "s_add_u32 s70, s70, s45\n"
    "s_mul_i32 s70, s70, 114688\n"
    "v_mfma_f32_16x16x32_bf16 v[68:71], a[96:99], v[132:135], v[68:71]\n"
    "ds_read_b64_tr_b16 v[146:147], v214 offset:27136\n"
    "s_add_u32 s85, s70, 626688\n"
    "s_add_u32 m0, s48, 89088\n"
    "v_mfma_f32_16x16x32_bf16 v[72:75], a[96:99], v[136:139], v[72:75]\n"
    "buffer_load_dwordx4 v251, s[16:19], s53 offen offset:0 lds\n"
    "ds_read_b64_tr_b16 v[148:149], v213 offset:26624\n"
    "s_and_b32 s70, s62, 0\n"
    "v_mfma_f32_16x16x32_bf16 v[76:79], a[96:99], v[140:143], v[76:79]\n"
    "ds_read_b64_tr_b16 v[150:151], v215 offset:27136\n"
    "s_mul_i32 s70, s70, 14336\n"
    "s_add_u32 s85, s85, s70\n"
    "v_mfma_f32_16x16x32_bf16 v[80:83], a[100:103], v[128:131], v[80:83]\n"
    "ds_read_b64_tr_b16 v[152:153], v214 offset:26624\n"
    "s_lshl_b32 s70, s45, 14\n"
    "s_lshl_b32 s69, s62, 7\n"
    "v_mfma_f32_16x16x32_bf16 v[84:87], a[100:103], v[132:135], v[84:87]\n"
    "ds_read_b64_tr_b16 v[154:155], v212 offset:27136\n"
    "s_add_u32 s87, s70, s69\n"
    "s_mov_b32 s26, 524288\n"
    "v_mfma_f32_16x16x32_bf16 v[88:91], a[100:103], v[136:139], v[88:91]\n"
    "ds_read_b64_tr_b16 v[156:157], v215 offset:26624\n"
    "s_mov_b32 s27, 0x20000\n"
    "s_lshr_b32 s70, s2, 4\n"
    "v_mfma_f32_16x16x32_bf16 v[92:95], a[100:103], v[140:143], v[92:95]\n"
    "ds_read_b64_tr_b16 v[158:159], v213 offset:27136\n"
    "s_and_b32 s71, s2, 7\n"
    "s_add_u32 m0, s48, 96128\n"
    "v_mfma_f32_16x16x32_bf16 v[96:99], a[104:107], v[128:131], v[96:99]\n"
    "buffer_load_dwordx4 v250, s[16:19], s53 offen offset:128 lds\n"
    "ds_read_b128 a[112:115], v209 offset:6144\n"
    "s_lshl4_add_u32 s70, s71, s70\n"
    "v_mfma_f32_16x16x32_bf16 v[100:103], a[104:107], v[132:135], v[100:103]\n"
    "ds_read_b128 a[116:119], v209 offset:8192\n"
    "s_lshl2_add_u32 s70, s70, s45\n"
    "s_lshl_b32 s90, s70, 14\n"
    "v_mfma_f32_16x16x32_bf16 v[104:107], a[104:107], v[136:139], v[104:107]\n"
    "ds_read_b128 a[120:123], v209 offset:10240\n"
    "s_add_u32 s90, s90, 0\n"
    "v_and_b32_e64 v244, 3, v0\n"
    "v_mfma_f32_16x16x32_bf16 v[108:111], a[104:107], v[140:143], v[108:111]\n"
    "ds_read_b128 a[124:127], v209 offset:12288\n"
    "v_lshlrev_b32 v244, 7, v244\n"
    "v_lshrrev_b32 v24, 2, v0\n"
    "v_mfma_f32_16x16x32_bf16 v[112:115], a[108:111], v[128:131], v[112:115]\n"
    "v_lshl_add_u32 v244, v24, 10, v244\n"
    "s_lshl_b32 s93, s45, 8\n"
    "s_add_u32 m0, s48, 97152\n"
    "v_mfma_f32_16x16x32_bf16 v[116:119], a[108:111], v[132:135], v[116:119]\n"
    "buffer_load_dwordx4 v251, s[16:19], s53 offen offset:128 lds\n"
    "s_add_u32 s53, s53, 229376\n"
    "v_mfma_f32_16x16x32_bf16 v[120:123], a[108:111], v[136:139], v[120:123]\n"
    "v_mfma_f32_16x16x32_bf16 v[124:127], a[108:111], v[140:143], v[124:127]\n"
    "s_waitcnt vmcnt(8) lgkmcnt(0)\n"
    ST(19)
    "s_barrier\n"
    "v_mfma_f32_16x16x32_bf16 v[64:67], a[112:115], v[144:147], v[64:67]\n"
    "ds_read_b64_tr_b16 v[128:129], v212 offset:55296\n"
    "v_mfma_f32_16x16x32_bf16 v[68:71], a[112:115], v[148:151], v[68:71]\n"
    "ds_read_b64_tr_b16 v[130:131], v214 offset:55808\n"
    "s_cmp_eq_u32 s3, 6\n"
    "s_cselect_b32 s63, 1024, 0\n"
    "s_sub_u32 s51, s51, s63\n"
    "s_mul_i32 s66, s63, 1792\n"
    "s_sub_u32 s53, s53, s66\n"
    "v_mfma_f32_16x16x32_bf16 v[72:75], a[112:115], v[152:155], v[72:75]\n"
    "ds_read_b64_tr_b16 v[132:133], v213 offset:55296\n"
    "v_mfma_f32_16x16x32_bf16 v[76:79], a[112:115], v[156:159], v[76:79]\n"
    "ds_read_b64_tr_b16 v[134:135], v215 offset:55808\n"
    "s_add_u32 m0, s49, 5376\n"
    "v_mfma_f32_16x16x32_bf16 v[80:83], a[116:119], v[144:147], v[80:83]\n"
    "buffer_load_dwordx4 v252, s[4:7], s51 offen offset:768 lds\n"
    "ds_read_b64_tr_b16 v[136:137], v214 offset:55296\n"
    "v_mfma_f32_16x16x32_bf16 v[84:87], a[116:119], v[148:151], v[84:87]\n"
    "ds_read_b64_tr_b16 v[138:139], v212 offset:55808\n"
    "s_add_u32 m0, s49, 6400\n"
    "v_mfma_f32_16x16x32_bf16 v[88:91], a[116:119], v[152:155], v[88:91]\n"
    "buffer_load_dwordx4 v253, s[4:7], s51 offen offset:768 lds\n"
    "ds_read_b64_tr_b16 v[140:141], v215 offset:55296\n"
    "v_mfma_f32_16x16x32_bf16 v[92:95], a[116:119], v[156:159], v[92:95]\n"
    "ds_read_b64_tr_b16 v[142:143], v213 offset:55808\n"
    "v_mfma_f32_16x16x32_bf16 v[96:99], a[120:123], v[144:147], v[96:99]\n"
    "ds_read_b128 a[128:131], v208 offset:38912\n"
    "s_add_u32 m0, s49, 6400\n"
    "v_mfma_f32_16x16x32_bf16 v[100:103], a[120:123], v[148:151], v[100:103]\n"
    "buffer_load_dwordx4 v252, s[4:7], s51 offen offset:1792 lds\n"
    "ds_read_b128 a[132:135], v208 offset:40960\n"
    "v_mfma_f32_16x16x32_bf16 v[104:107], a[120:123], v[152:155], v[104:107]\n"
    "ds_read_b128 a[136:139], v208 offset:43008\n"
    "v_mfma_f32_16x16x32_bf16 v[108:111], a[120:123], v[156:159], v[108:111]\n"
    "ds_read_b128 a[140:143], v208 offset:45056\n"
    "s_add_u32 m0, s49, 7424\n"
    "v_mfma_f32_16x16x32_bf16 v[112:115], a[124:127], v[144:147], v[112:115]\n"
    "buffer_load_dwordx4 v253, s[4:7], s51 offen offset:1792 lds\n"
    "v_mfma_f32_16x16x32_bf16 v[116:119], a[124:127], v[148:151], v[116:119]\n"
    "s_add_u32 m0, s48, 22528\n"
    "v_mfma_f32_16x16x32_bf16 v[120:123], a[124:127], v[152:155], v[120:123]\n"
    "buffer_load_dwordx4 v250, s[16:19], s53 offen offset:0 lds\n"
    "v_mfma_f32_16x16x32_bf16 v[124:127], a[124:127], v[156:159], v[124:127]\n"
    "s_waitcnt lgkmcnt(0)\n"
    "v_mfma_f32_16x16x32_bf16 v[64:67], a[128:131], v[128:131], v[64:67]\n"
    "ds_read_b64_tr_b16 v[144:145], v212 offset:59392\n"
    "v_mfma_f32_16x16x32_bf16 v[68:71], a[128:131], v[132:135], v[68:71]\n"
    "ds_read_b64_tr_b16 v[146:147], v214 offset:59904\n"
    "s_add_u32 m0, s48, 23552\n"
    "v_mfma_f32_16x16x32_bf16 v[72:75], a[128:131], v[136:139], v[72:75]\n"
    "buffer_load_dwordx4 v251, s[16:19], s53 offen offset:0 lds\n"
    "ds_read_b64_tr_b16 v[148:149], v213 offset:59392\n"
    "v_mfma_f32_16x16x32_bf16 v[76:79], a[128:131], v[140:143], v[76:79]\n"
    "ds_read_b64_tr_b16 v[150:151], v215 offset:59904\n"
    "v_mfma_f32_16x16x32_bf16 v[80:83], a[132:135], v[128:131], v[80:83]\n"
    "ds_read_b64_tr_b16 v[152:153], v214 offset:59392\n"
    "v_mfma_f32_16x16x32_bf16 v[84:87], a[132:135], v[132:135], v[84:87]\n"
    "ds_read_b64_tr_b16 v[154:155], v212 offset:59904\n"
    "v_mfma_f32_16x16x32_bf16 v[88:91], a[132:135], v[136:139], v[88:91]\n"
    "ds_read_b64_tr_b16 v[156:157], v215 offset:59392\n"
    "v_mfma_f32_16x16x32_bf16 v[92:95], a[132:135], v[140:143], v[92:95]\n"
    "ds_read_b64_tr_b16 v[158:159], v213 offset:59904\n"
    "s_add_u32 m0, s48, 30592\n"
    "v_mfma_f32_16x16x32_bf16 v[96:99], a[136:139], v[128:131], v[96:99]\n"
    "buffer_load_dwordx4 v250, s[16:19], s53 offen offset:128 lds\n"
    "ds_read_b128 a[144:147], v209 offset:38912\n"
    "v_mfma_f32_16x16x32_bf16 v[100:103], a[136:139], v[132:135], v[100:103]\n"
    "ds_read_b128 a[148:151], v209 offset:40960\n"
    "v_mfma_f32_16x16x32_bf16 v[104:107], a[136:139], v[136:139], v[104:107]\n"
    "ds_read_b128 a[152:155], v209 offset:43008\n"
    "v_mfma_f32_16x16x32_bf16 v[108:111], a[136:139], v[140:143], v[108:111]\n"
    "ds_read_b128 a[156:159], v209 offset:45056\n"
    "v_mfma_f32_16x16x32_bf16 v[112:115], a[140:143], v[128:131], v[112:115]\n"
    "s_add_u32 m0, s48, 31616\n"
    "v_mfma_f32_16x16x32_bf16 v[116:119], a[140:143], v[132:135], v[116:119]\n"
    "buffer_load_dwordx4 v251, s[16:19], s53 offen offset:128 lds\n"
    "s_add_u32 s53, s53, 229376\n"
    "v_mfma_f32_16x16x32_bf16 v[120:123], a[140:143], v[136:139], v[120:123]\n"
    "v_mfma_f32_16x16x32_bf16 v[124:127], a[140:143], v[140:143], v[124:127]\n"
    "s_waitcnt vmcnt(8) lgkmcnt(0)\n"
    ST(20)
    "s_barrier\n"
    "v_mfma_f32_16x16x32_bf16 v[64:67], a[144:147], v[144:147], v[64:67]\n"
    "ds_read_b64_tr_b16 v[128:129], v216 offset:22528\n"
    "v_mfma_f32_16x16x32_bf16 v[68:71], a[144:147], v[148:151], v[68:71]\n"
    "ds_read_b64_tr_b16 v[130:131], v218 offset:23040\n"
    "s_cmp_eq_u32 s3, 7\n"
    "s_cselect_b32 s63, 1024, 0\n"
    "s_sub_u32 s51, s51, s63\n"
    "s_mul_i32 s66, s63, 1792\n"
    "s_sub_u32 s53, s53, s66\n"
    "v_mfma_f32_16x16x32_bf16 v[72:75], a[144:147], v[152:155], v[72:75]\n"
    "ds_read_b64_tr_b16 v[132:133], v217 offset:22528\n"
    "v_mfma_f32_16x16x32_bf16 v[76:79], a[144:147], v[156:159], v[76:79]\n"
    "ds_read_b64_tr_b16 v[134:135], v219 offset:23040\n"
    "s_add_u32 m0, s49, 38016\n"
    "v_mfma_f32_16x16x32_bf16 v[80:83], a[148:151], v[144:147], v[80:83]\n"
    "buffer_load_dwordx4 v252, s[4:7], s51 offen offset:896 lds\n"
    "ds_read_b64_tr_b16 v[136:137], v218 offset:22528\n"
    "v_mfma_f32_16x16x32_bf16 v[84:87], a[148:151], v[148:151], v[84:87]\n"
    "ds_read_b64_tr_b16 v[138:139], v216 offset:23040\n"
    "s_add_u32 m0, s49, 39040\n"
    "v_mfma_f32_16x16x32_bf16 v[88:91], a[148:151], v[152:155], v[88:91]\n"
    "buffer_load_dwordx4 v253, s[4:7], s51 offen offset:896 lds\n"
    "ds_read_b64_tr_b16 v[140:141], v219 offset:22528\n"
    "v_mfma_f32_16x16x32_bf16 v[92:95], a[148:151], v[156:159], v[92:95]\n"
    "ds_read_b64_tr_b16 v[142:143], v217 offset:23040\n"
    "v_mfma_f32_16x16x32_bf16 v[96:99], a[152:155], v[144:147], v[96:99]\n"
    "ds_read_b128 a[160:163], v210 offset:6144\n"
    "s_add_u32 m0, s49, 39040\n"
    "v_mfma_f32_16x16x32_bf16 v[100:103], a[152:155], v[148:151], v[100:103]\n"
    "buffer_load_dwordx4 v252, s[4:7], s51 offen offset:1920 lds\n"
    "ds_read_b128 a[164:167], v210 offset:8192\n"
    "v_mfma_f32_16x16x32_bf16 v[104:107], a[152:155], v[152:155], v[104:107]\n"
    "ds_read_b128 a[168:171], v210 offset:10240\n"
    "v_mfma_f32_16x16x32_bf16 v[108:111], a[152:155], v[156:159], v[108:111]\n"
    "ds_read_b128 a[172:175], v210 offset:12288\n"
    "s_add_u32 m0, s49, 40064\n"
    "v_mfma_f32_16x16x32_bf16 v[112:115], a[156:159], v[144:147], v[112:115]\n"
    "buffer_load_dwordx4 v253, s[4:7], s51 offen offset:1920 lds\n"
    "v_mfma_f32_16x16x32_bf16 v[116:119], a[156:159], v[148:151], v[116:119]\n"
    "s_add_u32 m0, s48, 55296\n"
    "v_mfma_f32_16x16x32_bf16 v[120:123], a[156:159], v[152:155], v[120:123]\n"
    "buffer_load_dwordx4 v250, s[16:19], s53 offen offset:0 lds\n"
    "v_mfma_f32_16x16x32_bf16 v[124:127], a[156:159], v[156:159], v[124:127]\n"
    "s_waitcnt lgkmcnt(0)\n"
    "v_mfma_f32_16x16x32_bf16 v[64:67], a[160:163], v[128:131], v[64:67]\n"
    "ds_read_b64_tr_b16 v[144:145], v216 offset:26624\n"
    "v_mfma_f32_16x16x32_bf16 v[68:71], a[160:163], v[132:135], v[68:71]\n"
    "ds_read_b64_tr_b16 v[146:147], v218 offset:27136\n"
    "buffer_load_dwordx4 v[160:163], v220, s[8:11], 0 offen sc1\n"
    "v_mfma_f32_16x16x32_bf16 v[72:75], a[160:163], v[136:139], v[72:75]\n"
    "ds_read_b64_tr_b16 v[148:149], v217 offset:26624\n"
    "v_mfma_f32_16x16x32_bf16 v[76:79], a[160:163], v[140:143], v[76:79]\n"
    "ds_read_b64_tr_b16 v[150:151], v219 offset:27136\n"
    "buffer_load_dwordx2 v[164:165], v222, s[8:11], 0 offen sc1\n"
    "v_mfma_f32_16x16x32_bf16 v[80:83], a[164:167], v[128:131], v[80:83]\n"
    "ds_read_b64_tr_b16 v[152:153], v218 offset:26624\n"
    "s_add_u32 m0, s48, 56320\n"
    "v_mfma_f32_16x16x32_bf16 v[84:87], a[164:167], v[132:135], v[84:87]\n"
    "buffer_load_dwordx4 v251, s[16:19], s53 offen offset:0 lds\n"
    "ds_read_b64_tr_b16 v[154:155], v216 offset:27136\n"
    "v_mfma_f32_16x16x32_bf16 v[88:91], a[164:167], v[136:139], v[88:91]\n"
    "ds_read_b64_tr_b16 v[156:157], v219 offset:26624\n"
    "v_mfma_f32_16x16x32_bf16 v[92:95], a[164:167], v[140:143], v[92:95]\n"
    "ds_read_b64_tr_b16 v[158:159], v217 offset:27136\n"
    "s_add_u32 m0, s48, 63360\n"
    "v_mfma_f32_16x16x32_bf16 v[96:99], a[168:171], v[128:131], v[96:99]\n"
    "buffer_load_dwordx4 v250, s[16:19], s53 offen offset:128 lds\n"
    "ds_read_b128 a[176:179], v211 offset:6144\n"
    "v_mfma_f32_16x16x32_bf16 v[100:103], a[168:171], v[132:135], v[100:103]\n"
    "ds_read_b128 a[180:183], v211 offset:8192\n"
    "s_add_u32 m0, s48, 64384\n"
    "v_mfma_f32_16x16x32_bf16 v[104:107], a[168:171], v[136:139], v[104:107]\n"
    "buffer_load_dwordx4 v251, s[16:19], s53 offen offset:128 lds\n"
    "s_add_u32 s53, s53, 229376\n"
    "ds_read_b128 a[184:187], v211 offset:10240\n"
    "v_mfma_f32_16x16x32_bf16 v[108:111], a[168:171], v[140:143], v[108:111]\n"
    "ds_read_b128 a[188:191], v211 offset:12288\n"
    "s_add_u32 m0, s48, 104448\n"
    "v_mfma_f32_16x16x32_bf16 v[112:115], a[172:175], v[128:131], v[112:115]\n"
    "buffer_load_dwordx4 v250, s[16:19], s54 offen offset:0 lds\n"
    "v_mfma_f32_16x16x32_bf16 v[116:119], a[172:175], v[132:135], v[116:119]\n"
    "s_add_u32 m0, s48, 105472\n"
    "v_mfma_f32_16x16x32_bf16 v[120:123], a[172:175], v[136:139], v[120:123]\n"
    "buffer_load_dwordx4 v251, s[16:19], s54 offen offset:0 lds\n"
    "s_add_u32 s54, s54, 229376\n"
    "s_cmp_eq_u32 s3, 1\n"
    "s_cselect_b32 s63, 1024, 0\n"
    "s_mul_i32 s66, s63, 1792\n"
    "s_sub_u32 s54, s54, s66\n"
    "v_mfma_f32_16x16x32_bf16 v[124:127], a[172:175], v[140:143], v[124:127]\n"
    "s_waitcnt vmcnt(12) lgkmcnt(0)\n"
    ST(21)
    "s_barrier\n"
    "v_mfma_f32_16x16x32_bf16 v[64:67], a[176:179], v[144:147], v[64:67]\n"
    "ds_read_b64_tr_b16 v[128:129], v212 offset:22528\n"
    "s_add_u32 m0, s48, 112640\n"
    "v_mfma_f32_16x16x32_bf16 v[68:71], a[176:179], v[148:151], v[68:71]\n"
    "buffer_load_dwordx4 v250, s[16:19], s54 offen offset:0 lds\n"
    "ds_read_b64_tr_b16 v[130:131], v214 offset:23040\n"
    "v_mfma_f32_16x16x32_bf16 v[72:75], a[176:179], v[152:155], v[72:75]\n"
    "ds_read_b64_tr_b16 v[132:133], v213 offset:22528\n"
    "v_mfma_f32_16x16x32_bf16 v[76:79], a[176:179], v[156:159], v[76:79]\n"
    "ds_read_b64_tr_b16 v[134:135], v215 offset:23040\n"
    "s_add_u32 m0, s48, 113664\n"
    "v_mfma_f32_16x16x32_bf16 v[80:83], a[180:183], v[144:147], v[80:83]\n"
    "buffer_load_dwordx4 v251, s[16:19], s54 offen offset:0 lds\n"
    "s_add_u32 s54, s54, 229376\n"
    "s_cmp_eq_u32 s3, 2\n"
    "s_cselect_b32 s63, 1024, 0\n"
    "s_mul_i32 s66, s63, 1792\n"
    "s_sub_u32 s54, s54, s66\n"
    "ds_read_b64_tr_b16 v[136:137], v214 offset:22528\n"
    "v_mfma_f32_16x16x32_bf16 v[84:87], a[180:183], v[148:151], v[84:87]\n"
    "ds_read_b64_tr_b16 v[138:139], v212 offset:23040\n"
    "v_mfma_f32_16x16x32_bf16 v[88:91], a[180:183], v[152:155], v[88:91]\n"
    "ds_read_b64_tr_b16 v[140:141], v215 offset:22528\n"
    "v_mfma_f32_16x16x32_bf16 v[92:95], a[180:183], v[156:159], v[92:95]\n"
    "ds_read_b64_tr_b16 v[142:143], v213 offset:23040\n"
    "s_add_u32 m0, s48, 120832\n"
    "v_mfma_f32_16x16x32_bf16 v[96:99], a[184:187], v[144:147], v[96:99]\n"
    "buffer_load_dwordx4 v250, s[16:19], s54 offen offset:0 lds\n"
    "ds_read_b128 a[192:195], v208 offset:6144\n"
    "v_mfma_f32_16x16x32_bf16 v[100:103], a[184:187], v[148:151], v[100:103]\n"
    "ds_read_b128 a[196:199], v208 offset:8192\n"
    "v_mfma_f32_16x16x32_bf16 v[104:107], a[184:187], v[152:155], v[104:107]\n"
    "ds_read_b128 a[200:203], v208 offset:10240\n"
    "s_add_u32 m0, s48, 121856\n"
    "v_mfma_f32_16x16x32_bf16 v[108:111], a[184:187], v[156:159], v[108:111]\n"
    "buffer_load_dwordx4 v251, s[16:19], s54 offen offset:0 lds\n"
    "s_add_u32 s54, s54, 229376\n"
    "s_cmp_eq_u32 s3, 3\n"
    "s_cselect_b32 s63, 1024, 0\n"
    "s_mul_i32 s66, s63, 1792\n"
    "s_sub_u32 s54, s54, s66\n"
    "ds_read_b128 a[204:207], v208 offset:12288\n"
    "v_mfma_f32_16x16x32_bf16 v[112:115], a[188:191], v[144:147], v[112:115]\n"
    "v_mfma_f32_16x16x32_bf16 v[116:119], a[188:191], v[148:151], v[116:119]\n"
    "s_add_u32 m0, s48, 129024\n"
    "v_mfma_f32_16x16x32_bf16 v[120:123], a[188:191], v[152:155], v[120:123]\n"
    "buffer_load_dwordx4 v250, s[16:19], s54 offen offset:0 lds\n"
    "v_mfma_f32_16x16x32_bf16 v[124:127], a[188:191], v[156:159], v[124:127]\n"
    "s_waitcnt lgkmcnt(0)\n"
    "v_mfma_f32_16x16x32_bf16 v[64:67], a[192:195], v[128:131], v[64:67]\n"
    "ds_read_b64_tr_b16 v[144:145], v212 offset:26624\n"
    "s_add_u32 m0, s48, 130048\n"
    "v_mfma_f32_16x16x32_bf16 v[68:71], a[192:195], v[132:135], v[68:71]\n"
    "buffer_load_dwordx4 v251, s[16:19], s54 offen offset:0 lds\n"
    "s_add_u32 s54, s54, 229376\n"
    "s_cmp_eq_u32 s3, 4\n"
    "s_cselect_b32 s63, 1024, 0\n"
    "s_mul_i32 s66, s63, 1792\n"
    "s_sub_u32 s54, s54, s66\n"
    "ds_read_b64_tr_b16 v[146:147], v214 offset:27136\n"
    "v_mfma_f32_16x16x32_bf16 v[72:75], a[192:195], v[136:139], v[72:75]\n"
    "ds_read_b64_tr_b16 v[148:149], v213 offset:26624\n"
    "v_mfma_f32_16x16x32_bf16 v[76:79], a[192:195], v[140:143], v[76:79]\n"
    "ds_read_b64_tr_b16 v[150:151], v215 offset:27136\n"
    "s_add_u32 m0, s48, 137216\n"
    "v_mfma_f32_16x16x32_bf16 v[80:83], a[196:199], v[128:131], v[80:83]\n"
    "buffer_load_dwordx4 v250, s[16:19], s54 offen offset:0 lds\n"
    "ds_read_b64_tr_b16 v[152:153], v214 offset:26624\n"
    "v_mfma_f32_16x16x32_bf16 v[84:87], a[196:199], v[132:135], v[84:87]\n"
    "ds_read_b64_tr_b16 v[154:155], v212 offset:27136\n"
    "v_mfma_f32_16x16x32_bf16 v[88:91], a[196:199], v[136:139], v[88:91]\n"
    "ds_read_b64_tr_b16 v[156:157], v215 offset:26624\n"
    "v_mfma_f32_16x16x32_bf16 v[92:95], a[196:199], v[140:143], v[92:95]\n"
    "ds_read_b64_tr_b16 v[158:159], v213 offset:27136\n"
    "s_add_u32 m0, s48, 138240\n"
    "v_mfma_f32_16x16x32_bf16 v[96:99], a[200:203], v[128:131], v[96:99]\n"
    "buffer_load_dwordx4 v251, s[16:19], s54 offen offset:0 lds\n"
    "s_add_u32 s54, s54, 229376\n"
    "s_cmp_eq_u32 s3, 5\n"
    "s_cselect_b32 s63, 1024, 0\n"
    "s_mul_i32 s66, s63, 1792\n"
    "s_sub_u32 s54, s54, s66\n"
    "ds_read_b128 a[208:211], v209 offset:6144\n"
    "v_mfma_f32_16x16x32_bf16 v[100:103], a[200:203], v[132:135], v[100:103]\n"
    "ds_read_b128 a[212:215], v209 offset:8192\n"
    "v_mfma_f32_16x16x32_bf16 v[104:107], a[200:203], v[136:139], v[104:107]\n"
    "ds_read_b128 a[216:219], v209 offset:10240\n"
    "s_add_u32 m0, s48, 145408\n"
    "v_mfma_f32_16x16x32_bf16 v[108:111], a[200:203], v[140:143], v[108:111]\n"
    "buffer_load_dwordx4 v250, s[16:19], s54 offen offset:0 lds\n"
    "ds_read_b128 a[220:223], v209 offset:12288\n"
    "v_mfma_f32_16x16x32_bf16 v[112:115], a[204:207], v[128:131], v[112:115]\n"
    "v_mfma_f32_16x16x32_bf16 v[116:119], a[204:207], v[132:135], v[116:119]\n"
    "s_add_u32 m0, s48, 146432\n"
    "v_mfma_f32_16x16x32_bf16 v[120:123], a[204:207], v[136:139], v[120:123]\n"
    "buffer_load_dwordx4 v251, s[16:19], s54 offen offset:0 lds\n"
    "s_add_u32 s54, s54, 229376\n"
    "s_cmp_eq_u32 s3, 6\n"
    "s_cselect_b32 s63, 1024, 0\n"
    "s_mul_i32 s66, s63, 1792\n"
    "s_sub_u32 s54, s54, s66\n"
    "v_mfma_f32_16x16x32_bf16 v[124:127], a[204:207], v[140:143], v[124:127]\n"
    "s_waitcnt vmcnt(15)\n"
    "s_waitcnt vmcnt(12) lgkmcnt(0)\n"
    ST(22)
    "s_barrier\n"
    "v_mfma_f32_16x16x32_bf16 v[64:67], a[208:211], v[144:147], v[64:67]\n"
    "v_cmp_ne_u32 vcc, s46, v161\n"
    "v_cmp_ne_u32 s[64:65], s46, v163\n"
    "v_mfma_f32_16x16x32_bf16 v[68:71], a[208:211], v[148:151], v[68:71]\n"
    "s_or_b64 vcc, vcc, s[64:65]\n"
    "v_cmp_ne_u32 s[64:65], s46, v165\n"
    "s_add_u32 m0, s48, 153600\n"
    "v_mfma_f32_16x16x32_bf16 v[72:75], a[208:211], v[152:155], v[72:75]\n"
    "buffer_load_dwordx4 v250, s[16:19], s54 offen offset:0 lds\n"
    "s_or_b64 vcc, vcc, s[64:65]\n"
    "s_cbranch_vccnz Lslow_18_%=\n"
    "Lback_19_%=:\n"
    "v_mfma_f32_16x16x32_bf16 v[76:79], a[208:211], v[156:159], v[76:79]\n"
    "ds_write_b32 v221, v160\n"
    "v_mfma_f32_16x16x32_bf16 v[80:83], a[212:215], v[144:147], v[80:83]\n"
    "ds_write_b32 v221, v162 offset:1024\n"
    "ds_write_b32 v221, v164 offset:512\n"
    "v_mfma_f32_16x16x32_bf16 v[84:87], a[212:215], v[148:151], v[84:87]\n"
    "ds_read_b64_tr_b16 v[128:129], v212 offset:55296\n"
    "ds_read_b64_tr_b16 v[130:131], v214 offset:55808\n"
    "v_mfma_f32_16x16x32_bf16 v[88:91], a[212:215], v[152:155], v[88:91]\n"
    "ds_read_b64_tr_b16 v[132:133], v213 offset:55296\n"
    "ds_read_b64_tr_b16 v[134:135], v215 offset:55808\n"
    "v_mfma_f32_16x16x32_bf16 v[92:95], a[212:215], v[156:159], v[92:95]\n"
    "ds_read_b64_tr_b16 v[136:137], v214 offset:55296\n"
    "s_add_u32 m0, s48, 154624\n"
    "v_mfma_f32_16x16x32_bf16 v[96:99], a[216:219], v[144:147], v[96:99]\n"
    "buffer_load_dwordx4 v251, s[16:19], s54 offen offset:0 lds\n"
    "s_add_u32 s54, s54, 229376\n"
    "ds_read_b64_tr_b16 v[138:139], v212 offset:55808\n"
    "ds_read_b64_tr_b16 v[140:141], v215 offset:55296\n"
    "v_mfma_f32_16x16x32_bf16 v[100:103], a[216:219], v[148:151], v[100:103]\n"
    "ds_read_b64_tr_b16 v[142:143], v213 offset:55808\n"
    "ds_read_b128 a[224:227], v208 offset:38912\n"
    "v_mfma_f32_16x16x32_bf16 v[104:107], a[216:219], v[152:155], v[104:107]\n"
    "ds_read_b128 a[228:231], v208 offset:40960\n"
    "ds_read_b128 a[232:235], v208 offset:43008\n"
    "v_mfma_f32_16x16x32_bf16 v[108:111], a[216:219], v[156:159], v[108:111]\n"
    "ds_read_b128 a[236:239], v208 offset:45056\n"
    "v_mfma_f32_16x16x32_bf16 v[112:115], a[220:223], v[144:147], v[112:115]\n"
    "s_add_u32 m0, s93, 161792\n"
    "v_mfma_f32_16x16x32_bf16 v[116:119], a[220:223], v[148:151], v[116:119]\n"
    "buffer_load_dword v244, s[20:23], s90 offen lds\n"
    "v_mfma_f32_16x16x32_bf16 v[120:123], a[220:223], v[152:155], v[120:123]\n"
    "v_mfma_f32_16x16x32_bf16 v[124:127], a[220:223], v[156:159], v[124:127]\n"
    "s_waitcnt lgkmcnt(0)\n"
    "v_mfma_f32_16x16x32_bf16 v[64:67], a[224:227], v[128:131], v[64:67]\n"
    "ds_read_b64_tr_b16 v[144:145], v212 offset:59392\n"
    "v_mfma_f32_16x16x32_bf16 v[68:71], a[224:227], v[132:135], v[68:71]\n"
    "ds_read_b64_tr_b16 v[146:147], v214 offset:59904\n"
    "v_mfma_f32_16x16x32_bf16 v[72:75], a[224:227], v[136:139], v[72:75]\n"
    "ds_read_b64_tr_b16 v[148:149], v213 offset:59392\n"
    "v_mfma_f32_16x16x32_bf16 v[76:79], a[224:227], v[140:143], v[76:79]\n"
    "ds_read_b64_tr_b16 v[150:151], v215 offset:59904\n"
    "v_mfma_f32_16x16x32_bf16 v[80:83], a[228:231], v[128:131], v[80:83]\n"
    "ds_read_b64_tr_b16 v[152:153], v214 offset:59392\n"
    "v_mfma_f32_16x16x32_bf16 v[84:87], a[228:231], v[132:135], v[84:87]\n"
    "ds_read_b64_tr_b16 v[154:155], v212 offset:59904\n"
    "v_mfma_f32_16x16x32_bf16 v[88:91], a[228:231], v[136:139], v[88:91]\n"
    "ds_read_b64_tr_b16 v[156:157], v215 offset:59392\n"
    "v_mfma_f32_16x16x32_bf16 v[92:95], a[228:231], v[140:143], v[92:95]\n"
    "ds_read_b64_tr_b16 v[158:159], v213 offset:59904\n"
    "v_mfma_f32_16x16x32_bf16 v[96:99], a[232:235], v[128:131], v[96:99]\n"
    "ds_read_b128 a[240:243], v209 offset:38912\n"
    "v_mfma_f32_16x16x32_bf16 v[100:103], a[232:235], v[132:135], v[100:103]\n"
    "ds_read_b128 a[244:247], v209 offset:40960\n"
    "v_mfma_f32_16x16x32_bf16 v[104:107], a[232:235], v[136:139], v[104:107]\n"
    "ds_read_b128 a[248:251], v209 offset:43008\n"
    "v_mfma_f32_16x16x32_bf16 v[108:111], a[232:235], v[140:143], v[108:111]\n"
    "ds_read_b128 a[252:255], v209 offset:45056\n"
    "v_mfma_f32_16x16x32_bf16 v[112:115], a[236:239], v[128:131], v[112:115]\n"
    "v_mfma_f32_16x16x32_bf16 v[116:119], a[236:239], v[132:135], v[116:119]\n"
    "v_mfma_f32_16x16x32_bf16 v[120:123], a[236:239], v[136:139], v[120:123]\n"
    "v_mfma_f32_16x16x32_bf16 v[124:127], a[236:239], v[140:143], v[124:127]\n"
    "s_waitcnt lgkmcnt(0)\n"
    ST(23)
    "s_barrier\n"
    "v_mfma_f32_16x16x32_bf16 v[64:67], a[240:243], v[144:147], v[64:67]\n"
    "v_mfma_f32_16x16x32_bf16 v[68:71], a[240:243], v[148:151], v[68:71]\n"
    "v_mfma_f32_16x16x32_bf16 v[72:75], a[240:243], v[152:155], v[72:75]\n"
    "v_mfma_f32_16x16x32_bf16 v[76:79], a[240:243], v[156:159], v[76:79]\n"
    "v_mfma_f32_16x16x32_bf16 v[80:83], a[244:247], v[144:147], v[80:83]\n"
    "v_mfma_f32_16x16x32_bf16 v[84:87], a[244:247], v[148:151], v[84:87]\n"
    "v_mfma_f32_16x16x32_bf16 v[88:91], a[244:247], v[152:155], v[88:91]\n"
    "v_mfma_f32_16x16x32_bf16 v[92:95], a[244:247], v[156:159], v[92:95]\n"
    "v_mfma_f32_16x16x32_bf16 v[96:99], a[248:251], v[144:147], v[96:99]\n"
    "v_mfma_f32_16x16x32_bf16 v[100:103], a[248:251], v[148:151], v[100:103]\n"
    "v_mfma_f32_16x16x32_bf16 v[104:107], a[248:251], v[152:155], v[104:107]\n"
    "v_mfma_f32_16x16x32_bf16 v[108:111], a[248:251], v[156:159], v[108:111]\n"
    "v_mfma_f32_16x16x32_bf16 v[112:115], a[252:255], v[144:147], v[112:115]\n"
    "v_mfma_f32_16x16x32_bf16 v[116:119], a[252:255], v[148:151], v[116:119]\n"
    "v_mfma_f32_16x16x32_bf16 v[120:123], a[252:255], v[152:155], v[120:123]\n"
    "v_mfma_f32_16x16x32_bf16 v[124:127], a[252:255], v[156:159], v[124:127]\n"
    "ds_read_b32 v192, v237 offset:512\n"
    "ds_read_b32 v194, v237 offset:576\n"
    "ds_read_b32 v196, v237 offset:640\n"
    "ds_read_b32 v198, v237 offset:704\n"
    "ds_read_b128 v[32:35], v223 offset:0\n"
    "ds_read_b128 v[36:39], v223 offset:1024\n"
    "ds_read_b128 v[40:43], v223 offset:64\n"
    "ds_read_b128 v[44:47], v223 offset:1088\n"
    "ds_read_b128 v[48:51], v223 offset:128\n"
    "ds_read_b128 v[52:55], v223 offset:1152\n"
    "ds_read_b128 v[56:59], v223 offset:192\n"
    "ds_read_b128 v[60:63], v223 offset:1216\n"
    "v_mov_b32 v28, 0xc0135761\n"
    "v_mov_b32 v30, 0xbdd2d3e8\n"
    ST(2)
    "s_waitcnt lgkmcnt(6)\n"
    "s_nop 0\n"
    "v_pk_fma_f32 v[64:65], v[32:33], v[192:193], v[64:65] op_sel_hi:[1,0,1] neg_lo:[1,0,0] neg_hi:[1,0,0]\n"
    "v_pk_fma_f32 v[66:67], v[34:35], v[192:193], v[66:67] op_sel_hi:[1,0,1] neg_lo:[1,0,0] neg_hi:[1,0,0]\n"
    "v_pk_mul_f32 v[64:65], v[64:65], v[36:37]\n"
    "v_pk_mul_f32 v[66:67], v[66:67], v[38:39]\n"
    "v_pk_mul_f32 v[128:129], v[64:65], v[64:65]\n"
    "v_pk_mul_f32 v[130:131], v[66:67], v[66:67]\n"
    "v_pk_fma_f32 v[128:129], v[128:129], v[30:31], v[28:29] op_sel_hi:[1,0,0]\n"
    "v_pk_fma_f32 v[130:131], v[130:131], v[30:31], v[28:29] op_sel_hi:[1,0,0]\n"
    "v_pk_mul_f32 v[128:129], v[64:65], v[128:129]\n"
    "v_pk_mul_f32 v[130:131], v[66:67], v[130:131]\n"
    "v_exp_f32 v128, v128\n"
    "v_exp_f32 v129, v129\n"
    "v_exp_f32 v130, v130\n"
    "v_exp_f32 v131, v131\n"
    "v_pk_add_f32 v[128:129], v[128:129], 1.0 op_sel_hi:[1,0]\n"
    "v_pk_add_f32 v[130:131], v[130:131], 1.0 op_sel_hi:[1,0]\n"
    "v_rcp_f32 v128, v128\n"
    "v_rcp_f32 v129, v129\n"
    "v_rcp_f32 v130, v130\n"
    "v_rcp_f32 v131, v131\n"
    "v_pk_mul_f32 v[64:65], v[64:65], v[128:129]\n"
    "v_pk_mul_f32 v[66:67], v[66:67], v[130:131]\n"
    "v_pk_mul_f32 v[176:177], v[64:65], v[64:65]\n"
    "v_pk_mul_f32 v[178:179], v[66:67], v[66:67]\n"
    "v_pk_fma_f32 v[68:69], v[32:33], v[194:195], v[68:69] op_sel_hi:[1,0,1] neg_lo:[1,0,0] neg_hi:[1,0,0]\n"
    "v_pk_fma_f32 v[70:71], v[34:35], v[194:195], v[70:71] op_sel_hi:[1,0,1] neg_lo:[1,0,0] neg_hi:[1,0,0]\n"
    "v_pk_mul_f32 v[68:69], v[68:69], v[36:37]\n"
    "v_pk_mul_f32 v[70:71], v[70:71], v[38:39]\n"
    "v_pk_mul_f32 v[132:133], v[68:69], v[68:69]\n"
    "v_pk_mul_f32 v[134:135], v[70:71], v[70:71]\n"
    "v_pk_fma_f32 v[132:133], v[132:133], v[30:31], v[28:29] op_sel_hi:[1,0,0]\n"
    "v_pk_fma_f32 v[134:135], v[134:135], v[30:31], v[28:29] op_sel_hi:[1,0,0]\n"
    "v_pk_mul_f32 v[132:133], v[68:69], v[132:133]\n"
    "v_pk_mul_f32 v[134:135], v[70:71], v[134:135]\n"
    "v_exp_f32 v132, v132\n"
    "v_exp_f32 v133, v133\n"
    "v_exp_f32 v134, v134\n"
    "v_exp_f32 v135, v135\n"
    "v_pk_add_f32 v[132:133], v[132:133], 1.0 op_sel_hi:[1,0]\n"
    "v_pk_add_f32 v[134:135], v[134:135], 1.0 op_sel_hi:[1,0]\n"
    "v_rcp_f32 v132, v132\n"
    "v_rcp_f32 v133, v133\n"
    "v_rcp_f32 v134, v134\n"
    "v_rcp_f32 v135, v135\n"
    "v_pk_mul_f32 v[68:69], v[68:69], v[132:133]\n"
    "v_pk_mul_f32 v[70:71], v[70:71], v[134:135]\n"
    "v_pk_add_f32 v[160:161], v[64:65], v[68:69]\n"
    "v_pk_add_f32 v[162:163], v[66:67], v[70:71]\n"
    "v_pk_fma_f32 v[176:177], v[68:69], v[68:69], v[176:177]\n"
    "v_pk_fma_f32 v[178:179], v[70:71], v[70:71], v[178:179]\n"
    "v_pk_fma_f32 v[72:73], v[32:33], v[196:197], v[72:73] op_sel_hi:[1,0,1] neg_lo:[1,0,0] neg_hi:[1,0,0]\n"
    "v_pk_fma_f32 v[74:75], v[34:35], v[196:197], v[74:75] op_sel_hi:[1,0,1] neg_lo:[1,0,0] neg_hi:[1,0,0]\n"
    "v_pk_mul_f32 v[72:73], v[72:73], v[36:37]\n"
    "v_pk_mul_f32 v[74:75], v[74:75], v[38:39]\n"
    "v_pk_mul_f32 v[128:129], v[72:73], v[72:73]\n"
    "v_pk_mul_f32 v[130:131], v[74:75], v[74:75]\n"
    "v_pk_fma_f32 v[128:129], v[128:129], v[30:31], v[28:29] op_sel_hi:[1,0,0]\n"
    "v_pk_fma_f32 v[130:131], v[130:131], v[30:31], v[28:29] op_sel_hi:[1,0,0]\n"
    "v_pk_mul_f32 v[128:129], v[72:73], v[128:129]\n"
    "v_pk_mul_f32 v[130:131], v[74:75], v[130:131]\n"
    "v_exp_f32 v128, v128\n"
    "v_exp_f32 v129, v129\n"
    "v_exp_f32 v130, v130\n"
    "v_exp_f32 v131, v131\n"
    "v_pk_add_f32 v[128:129], v[128:129], 1.0 op_sel_hi:[1,0]\n"
    "v_pk_add_f32 v[130:131], v[130:131], 1.0 op_sel_hi:[1,0]\n"
    "v_rcp_f32 v128, v128\n"
    "v_rcp_f32 v129, v129\n"
    "v_rcp_f32 v130, v130\n"
    "v_rcp_f32 v131, v131\n"
    "v_pk_mul_f32 v[72:73], v[72:73], v[128:129]\n"
    "v_pk_mul_f32 v[74:75], v[74:75], v[130:131]\n"
    "v_pk_add_f32 v[160:161], v[160:161], v[72:73]\n"
    "v_pk_add_f32 v[162:163], v[162:163], v[74:75]\n"
    "v_pk_fma_f32 v[176:177], v[72:73], v[72:73], v[176:177]\n"
    "v_pk_fma_f32 v[178:179], v[74:75], v[74:75], v[178:179]\n"
    "v_pk_fma_f32 v[76:77], v[32:33], v[198:199], v[76:77] op_sel_hi:[1,0,1] neg_lo:[1,0,0] neg_hi:[1,0,0]\n"
    "v_pk_fma_f32 v[78:79], v[34:35], v[198:199], v[78:79] op_sel_hi:[1,0,1] neg_lo:[1,0,0] neg_hi:[1,0,0]\n"
    "v_pk_mul_f32 v[76:77], v[76:77], v[36:37]\n"
    "v_pk_mul_f32 v[78:79], v[78:79], v[38:39]\n"
    "v_pk_mul_f32 v[132:133], v[76:77], v[76:77]\n"
    "v_pk_mul_f32 v[134:135], v[78:79], v[78:79]\n"
    "v_pk_fma_f32 v[132:133], v[132:133], v[30:31], v[28:29] op_sel_hi:[1,0,0]\n"
    "v_pk_fma_f32 v[134:135], v[134:135], v[30:31], v[28:29] op_sel_hi:[1,0,0]\n"
    "v_pk_mul_f32 v[132:133], v[76:77], v[132:133]\n"
    "v_pk_mul_f32 v[134:135], v[78:79], v[134:135]\n"
    "v_exp_f32 v132, v132\n"
    "v_exp_f32 v133, v133\n"
    "v_exp_f32 v134, v134\n"
    "v_exp_f32 v135, v135\n"
    "v_pk_add_f32 v[132:133], v[132:133], 1.0 op_sel_hi:[1,0]\n"
    "v_pk_add_f32 v[134:135], v[134:135], 1.0 op_sel_hi:[1,0]\n"
    "v_rcp_f32 v132, v132\n"
    "v_rcp_f32 v133, v133\n"
    "v_rcp_f32 v134, v134\n"
    "v_rcp_f32 v135, v135\n"
    "v_pk_mul_f32 v[76:77], v[76:77], v[132:133]\n"
    "v_pk_mul_f32 v[78:79], v[78:79], v[134:135]\n"
    "v_pk_add_f32 v[160:161], v[160:161], v[76:77]\n"
    "v_pk_add_f32 v[162:163], v[162:163], v[78:79]\n"
    "v_pk_fma_f32 v[176:177], v[76:77], v[76:77], v[176:177]\n"
    "v_pk_fma_f32 v[178:179], v[78:79], v[78:79], v[178:179]\n"
    "s_waitcnt lgkmcnt(4)\n"
    "s_nop 0\n"
    "v_pk_fma_f32 v[80:81], v[40:41], v[192:193], v[80:81] op_sel_hi:[1,0,1] neg_lo:[1,0,0] neg_hi:[1,0,0]\n"
    "v_pk_fma_f32 v[82:83], v[42:43], v[192:193], v[82:83] op_sel_hi:[1,0,1] neg_lo:[1,0,0] neg_hi:[1,0,0]\n"
    "v_pk_mul_f32 v[80:81], v[80:81], v[44:45]\n"
    "v_pk_mul_f32 v[82:83], v[82:83], v[46:47]\n"
    "v_pk_mul_f32 v[128:129], v[80:81], v[80:81]\n"
    "v_pk_mul_f32 v[130:131], v[82:83], v[82:83]\n"
    "v_pk_fma_f32 v[128:129], v[128:129], v[30:31], v[28:29] op_sel_hi:[1,0,0]\n"
    "v_pk_fma_f32 v[130:131], v[130:131], v[30:31], v[28:29] op_sel_hi:[1,0,0]\n"
    "v_pk_mul_f32 v[128:129], v[80:81], v[128:129]\n"
    "v_pk_mul_f32 v[130:131], v[82:83], v[130:131]\n"
    "v_exp_f32 v128, v128\n"
    "v_exp_f32 v129, v129\n"
    "v_exp_f32 v130, v130\n"
    "v_exp_f32 v131, v131\n"
    "v_pk_add_f32 v[128:129], v[128:129], 1.0 op_sel_hi:[1,0]\n"
    "v_pk_add_f32 v[130:131], v[130:131], 1.0 op_sel_hi:[1,0]\n"
    "v_rcp_f32 v128, v128\n"
    "v_rcp_f32 v129, v129\n"
    "v_rcp_f32 v130, v130\n"
    "v_rcp_f32 v131, v131\n"
    "v_pk_mul_f32 v[80:81], v[80:81], v[128:129]\n"
    "v_pk_mul_f32 v[82:83], v[82:83], v[130:131]\n"
    "v_pk_mul_f32 v[180:181], v[80:81], v[80:81]\n"
    "v_pk_mul_f32 v[182:183], v[82:83], v[82:83]\n"
    "v_pk_fma_f32 v[84:85], v[40:41], v[194:195], v[84:85] op_sel_hi:[1,0,1] neg_lo:[1,0,0] neg_hi:[1,0,0]\n"
    "v_pk_fma_f32 v[86:87], v[42:43], v[194:195], v[86:87] op_sel_hi:[1,0,1] neg_lo:[1,0,0] neg_hi:[1,0,0]\n"
    "v_pk_mul_f32 v[84:85], v[84:85], v[44:45]\n"
    "v_pk_mul_f32 v[86:87], v[86:87], v[46:47]\n"
    "v_pk_mul_f32 v[132:133], v[84:85], v[84:85]\n"
    "v_pk_mul_f32 v[134:135], v[86:87], v[86:87]\n"
    "v_pk_fma_f32 v[132:133], v[132:133], v[30:31], v[28:29] op_sel_hi:[1,0,0]\n"
    "v_pk_fma_f32 v[134:135], v[134:135], v[30:31], v[28:29] op_sel_hi:[1,0,0]\n"
    "v_pk_mul_f32 v[132:133], v[84:85], v[132:133]\n"
    "v_pk_mul_f32 v[134:135], v[86:87], v[134:135]\n"
    "v_exp_f32 v132, v132\n"
    "v_exp_f32 v133, v133\n"
    "v_exp_f32 v134, v134\n"
    "v_exp_f32 v135, v135\n"
    "v_pk_add_f32 v[132:133], v[132:133], 1.0 op_sel_hi:[1,0]\n"
    "v_pk_add_f32 v[134:135], v[134:135], 1.0 op_sel_hi:[1,0]\n"
    "v_rcp_f32_e64 v132, v132\n"
    "s_add_u32 m0, s49, 71680\n"
    "v_rcp_f32_e64 v133, v133\n"
    "buffer_load_dwordx4 v2, s[20:23], s55 offen offset:0 lds\n"
    "v_rcp_f32 v134, v134\n"
    "v_rcp_f32 v135, v135\n"
    "v_pk_mul_f32 v[84:85], v[84:85], v[132:133]\n"
    "v_pk_mul_f32 v[86:87], v[86:87], v[134:135]\n"
    "v_pk_add_f32 v[164:165], v[80:81], v[84:85]\n"
    "v_pk_add_f32 v[166:167], v[82:83], v[86:87]\n"
    "v_pk_fma_f32 v[180:181], v[84:85], v[84:85], v[180:181]\n"
    "v_pk_fma_f32 v[182:183], v[86:87], v[86:87], v[182:183]\n"
    "v_pk_fma_f32 v[88:89], v[40:41], v[196:197], v[88:89] op_sel_hi:[1,0,1] neg_lo:[1,0,0] neg_hi:[1,0,0]\n"
    "s_add_u32 m0, s49, 72704\n"
    "v_pk_fma_f32 v[90:91], v[42:43], v[196:197], v[90:91] op_sel_hi:[1,0,1] neg_lo:[1,0,0] neg_hi:[1,0,0]\n"
    "buffer_load_dwordx4 v3, s[20:23], s55 offen offset:0 lds\n"
    "v_pk_mul_f32 v[88:89], v[88:89], v[44:45]\n"
    "v_pk_mul_f32 v[90:91], v[90:91], v[46:47]\n"
    "v_pk_mul_f32 v[128:129], v[88:89], v[88:89]\n"
    "v_pk_mul_f32 v[130:131], v[90:91], v[90:91]\n"
    "v_pk_fma_f32 v[128:129], v[128:129], v[30:31], v[28:29] op_sel_hi:[1,0,0]\n"
    "v_pk_fma_f32 v[130:131], v[130:131], v[30:31], v[28:29] op_sel_hi:[1,0,0]\n"
    "v_pk_mul_f32 v[128:129], v[88:89], v[128:129]\n"
    "v_pk_mul_f32 v[130:131], v[90:91], v[130:131]\n"
    "v_exp_f32 v128, v128\n"
    "v_exp_f32 v129, v129\n"
    "v_exp_f32_e64 v130, v130\n"
    "s_add_u32 m0, s49, 73728\n"
    "v_exp_f32_e64 v131, v131\n"
    "buffer_load_dwordx4 v2, s[20:23], s56 offen offset:0 lds\n"
    "v_pk_add_f32 v[128:129], v[128:129], 1.0 op_sel_hi:[1,0]\n"
    "v_pk_add_f32 v[130:131], v[130:131], 1.0 op_sel_hi:[1,0]\n"
    "v_rcp_f32 v128, v128\n"
    "v_rcp_f32 v129, v129\n"
    "v_rcp_f32 v130, v130\n"
    "v_rcp_f32 v131, v131\n"
    "v_pk_mul_f32 v[88:89], v[88:89], v[128:129]\n"
    "s_add_u32 m0, s49, 74752\n"
    "v_pk_mul_f32 v[90:91], v[90:91], v[130:131]\n"
    "buffer_load_dwordx4 v3, s[20:23], s56 offen offset:0 lds\n"
    "v_pk_add_f32 v[164:165], v[164:165], v[88:89]\n"
    "v_pk_add_f32 v[166:167], v[166:167], v[90:91]\n"
    "v_pk_fma_f32 v[180:181], v[88:89], v[88:89], v[180:181]\n"
    "v_pk_fma_f32 v[182:183], v[90:91], v[90:91], v[182:183]\n"
    "v_pk_fma_f32 v[92:93], v[40:41], v[198:199], v[92:93] op_sel_hi:[1,0,1] neg_lo:[1,0,0] neg_hi:[1,0,0]\n"
    "v_pk_fma_f32 v[94:95], v[42:43], v[198:199], v[94:95] op_sel_hi:[1,0,1] neg_lo:[1,0,0] neg_hi:[1,0,0]\n"
    "v_pk_mul_f32 v[92:93], v[92:93], v[44:45]\n"
    "v_pk_mul_f32 v[94:95], v[94:95], v[46:47]\n"
    "v_pk_mul_f32 v[132:133], v[92:93], v[92:93]\n"
    "v_pk_mul_f32 v[134:135], v[94:95], v[94:95]\n"
    "v_pk_fma_f32 v[132:133], v[132:133], v[30:31], v[28:29] op_sel_hi:[1,0,0]\n"
    "v_pk_fma_f32 v[134:135], v[134:135], v[30:31], v[28:29] op_sel_hi:[1,0,0]\n"
    "v_pk_mul_f32 v[132:133], v[92:93], v[132:133]\n"
    "s_add_u32 m0, s49, 88064\n"
    "v_pk_mul_f32 v[134:135], v[94:95], v[134:135]\n"
    "buffer_load_dwordx4 v2, s[20:23], s57 offen offset:0 lds\n"
    "v_exp_f32 v132, v132\n"
    "v_exp_f32 v133, v133\n"
    "v_exp_f32 v134, v134\n"
    "v_exp_f32 v135, v135\n"
    "v_pk_add_f32 v[132:133], v[132:133], 1.0 op_sel_hi:[1,0]\n"
    "v_pk_add_f32 v[134:135], v[134:135], 1.0 op_sel_hi:[1,0]\n"
    "v_rcp_f32_e64 v132, v132\n"
    "s_add_u32 m0, s49, 89088\n"
    "v_rcp_f32_e64 v133, v133\n"
    "buffer_load_dwordx4 v3, s[20:23], s57 offen offset:0 lds\n"
    "v_rcp_f32 v134, v134\n"
    "v_rcp_f32 v135, v135\n"
    "v_pk_mul_f32 v[92:93], v[92:93], v[132:133]\n"
    "v_pk_mul_f32 v[94:95], v[94:95], v[134:135]\n"
    "v_pk_add_f32 v[164:165], v[164:165], v[92:93]\n"
    "v_pk_add_f32 v[166:167], v[166:167], v[94:95]\n"
    "v_pk_fma_f32 v[180:181], v[92:93], v[92:93], v[180:181]\n"
    "v_pk_fma_f32 v[182:183], v[94:95], v[94:95], v[182:183]\n"
    "s_waitcnt lgkmcnt(2)\n"
    "s_nop 0\n"
    "v_pk_fma_f32 v[96:97], v[48:49], v[192:193], v[96:97] op_sel_hi:[1,0,1] neg_lo:[1,0,0] neg_hi:[1,0,0]\n"
    "s_add_u32 m0, s49, 90112\n"
    "v_pk_fma_f32 v[98:99], v[50:51], v[192:193], v[98:99] op_sel_hi:[1,0,1] neg_lo:[1,0,0] neg_hi:[1,0,0]\n"
    "buffer_load_dwordx4 v2, s[20:23], s58 offen offset:0 lds\n"
    "v_pk_mul_f32 v[96:97], v[96:97], v[52:53]\n"
    "v_pk_mul_f32 v[98:99], v[98:99], v[54:55]\n"
    "v_pk_mul_f32 v[128:129], v[96:97], v[96:97]\n"
    "v_pk_mul_f32 v[130:131], v[98:99], v[98:99]\n"
    "v_pk_fma_f32 v[128:129], v[128:129], v[30:31], v[28:29] op_sel_hi:[1,0,0]\n"
    "v_pk_fma_f32 v[130:131], v[130:131], v[30:31], v[28:29] op_sel_hi:[1,0,0]\n"
    "v_pk_mul_f32 v[128:129], v[96:97], v[128:129]\n"
    "v_pk_mul_f32 v[130:131], v[98:99], v[130:131]\n"
    "v_exp_f32 v128, v128\n"
    "v_exp_f32 v129, v129\n"
    "v_exp_f32_e64 v130, v130\n"
    "s_add_u32 m0, s49, 91136\n"
    "v_exp_f32_e64 v131, v131\n"
    "buffer_load_dwordx4 v3, s[20:23], s58 offen offset:0 lds\n"
    "v_pk_add_f32 v[128:129], v[128:129], 1.0 op_sel_hi:[1,0]\n"
    "v_pk_add_f32 v[130:131], v[130:131], 1.0 op_sel_hi:[1,0]\n"
    "v_rcp_f32 v128, v128\n"
    "v_rcp_f32 v129, v129\n"
    "v_rcp_f32 v130, v130\n"
    "v_rcp_f32 v131, v131\n"
    "v_pk_mul_f32 v[96:97], v[96:97], v[128:129]\n"
    "s_add_u32 m0, s49, 6016\n"
    "v_pk_mul_f32 v[98:99], v[98:99], v[130:131]\n"
    "buffer_load_dwordx4 v2, s[20:23], s55 offen offset:128 lds\n"
    "v_pk_mul_f32 v[184:185], v[96:97], v[96:97]\n"
    "v_pk_mul_f32 v[186:187], v[98:99], v[98:99]\n"
    "v_pk_fma_f32 v[100:101], v[48:49], v[194:195], v[100:101] op_sel_hi:[1,0,1] neg_lo:[1,0,0] neg_hi:[1,0,0]\n"
    "v_pk_fma_f32 v[102:103], v[50:51], v[194:195], v[102:103] op_sel_hi:[1,0,1] neg_lo:[1,0,0] neg_hi:[1,0,0]\n"
    "v_pk_mul_f32 v[100:101], v[100:101], v[52:53]\n"
    "v_pk_mul_f32 v[102:103], v[102:103], v[54:55]\n"
    "v_pk_mul_f32 v[132:133], v[100:101], v[100:101]\n"
    "v_pk_mul_f32 v[134:135], v[102:103], v[102:103]\n"
    "v_pk_fma_f32 v[132:133], v[132:133], v[30:31], v[28:29] op_sel_hi:[1,0,0]\n"
    "v_pk_fma_f32 v[134:135], v[134:135], v[30:31], v[28:29] op_sel_hi:[1,0,0]\n"
    "v_pk_mul_f32 v[132:133], v[100:101], v[132:133]\n"
    "v_pk_mul_f32 v[134:135], v[102:103], v[134:135]\n"
    "v_exp_f32_e64 v132, v132\n"
    "s_add_u32 m0, s49, 7040\n"
    "v_exp_f32_e64 v133, v133\n"
    "buffer_load_dwordx4 v3, s[20:23], s55 offen offset:128 lds\n"
    "v_exp_f32 v134, v134\n"
    "v_exp_f32 v135, v135\n"
    "v_pk_add_f32 v[132:133], v[132:133], 1.0 op_sel_hi:[1,0]\n"
    "v_pk_add_f32 v[134:135], v[134:135], 1.0 op_sel_hi:[1,0]\n"
    "v_rcp_f32 v132, v132\n"
    "v_rcp_f32 v133, v133\n"
    "s_add_u32 m0, s49, 8064\n"
    "v_rcp_f32_e64 v134, v134\n"
    "buffer_load_dwordx4 v2, s[20:23], s56 offen offset:128 lds\n"
    "v_rcp_f32_e64 v135, v135\n"
    "v_pk_mul_f32 v[100:101], v[100:101], v[132:133]\n"
    "v_pk_mul_f32 v[102:103], v[102:103], v[134:135]\n"
    "v_pk_add_f32 v[168:169], v[96:97], v[100:101]\n"
    "v_pk_add_f32 v[170:171], v[98:99], v[102:103]\n"
    "v_pk_fma_f32 v[184:185], v[100:101], v[100:101], v[184:185]\n"
    "v_pk_fma_f32 v[186:187], v[102:103], v[102:103], v[186:187]\n"
    "v_pk_fma_f32 v[104:105], v[48:49], v[196:197], v[104:105] op_sel_hi:[1,0,1] neg_lo:[1,0,0] neg_hi:[1,0,0]\n"
    "v_pk_fma_f32 v[106:107], v[50:51], v[196:197], v[106:107] op_sel_hi:[1,0,1] neg_lo:[1,0,0] neg_hi:[1,0,0]\n"
    "v_pk_mul_f32 v[104:105], v[104:105], v[52:53]\n"
    "v_pk_mul_f32 v[106:107], v[106:107], v[54:55]\n"
    "s_add_u32 m0, s49, 9088\n"
    "v_pk_mul_f32 v[128:129], v[104:105], v[104:105]\n"
    "buffer_load_dwordx4 v3, s[20:23], s56 offen offset:128 lds\n"
    "v_pk_mul_f32 v[130:131], v[106:107], v[106:107]\n"
    "v_pk_fma_f32 v[128:129], v[128:129], v[30:31], v[28:29] op_sel_hi:[1,0,0]\n"
    "v_pk_fma_f32 v[130:131], v[130:131], v[30:31], v[28:29] op_sel_hi:[1,0,0]\n"
    "v_pk_mul_f32 v[128:129], v[104:105], v[128:129]\n"
    "v_pk_mul_f32 v[130:131], v[106:107], v[130:131]\n"
    "v_exp_f32 v128, v128\n"
    "v_exp_f32 v129, v129\n"
    "v_exp_f32 v130, v130\n"
    "v_exp_f32 v131, v131\n"
    "s_add_u32 m0, s49, 22400\n"
    "v_pk_add_f32 v[128:129], v[128:129], 1.0 op_sel_hi:[1,0]\n"
    "buffer_load_dwordx4 v2, s[20:23], s57 offen offset:128 lds\n"
    "v_pk_add_f32 v[130:131], v[130:131], 1.0 op_sel_hi:[1,0]\n"
    "v_rcp_f32 v128, v128\n"
    "v_rcp_f32 v129, v129\n"
    "v_rcp_f32 v130, v130\n"
    "v_rcp_f32 v131, v131\n"
    "v_pk_mul_f32 v[104:105], v[104:105], v[128:129]\n"
    "v_pk_mul_f32 v[106:107], v[106:107], v[130:131]\n"
    "v_pk_add_f32 v[168:169], v[168:169], v[104:105]\n"
    "v_pk_add_f32 v[170:171], v[170:171], v[106:107]\n"
    "s_add_u32 m0, s49, 23424\n"
    "v_pk_fma_f32 v[184:185], v[104:105], v[104:105], v[184:185]\n"
    "buffer_load_dwordx4 v3, s[20:23], s57 offen offset:128 lds\n"
    "v_pk_fma_f32 v[186:187], v[106:107], v[106:107], v[186:187]\n"
    "v_pk_fma_f32 v[108:109], v[48:49], v[198:199], v[108:109] op_sel_hi:[1,0,1] neg_lo:[1,0,0] neg_hi:[1,0,0]\n"
    "v_pk_fma_f32 v[110:111], v[50:51], v[198:199], v[110:111] op_sel_hi:[1,0,1] neg_lo:[1,0,0] neg_hi:[1,0,0]\n"
    "v_pk_mul_f32 v[108:109], v[108:109], v[52:53]\n"
    "v_pk_mul_f32 v[110:111], v[110:111], v[54:55]\n"
    "v_pk_mul_f32 v[132:133], v[108:109], v[108:109]\n"
    "v_pk_mul_f32 v[134:135], v[110:111], v[110:111]\n"
    "v_pk_fma_f32 v[132:133], v[132:133], v[30:31], v[28:29] op_sel_hi:[1,0,0]\n"
    "v_pk_fma_f32 v[134:135], v[134:135], v[30:31], v[28:29] op_sel_hi:[1,0,0]\n"
    "v_pk_mul_f32 v[132:133], v[108:109], v[132:133]\n"
    "v_pk_mul_f32 v[134:135], v[110:111], v[134:135]\n"
    "v_exp_f32_e64 v132, v132\n"
    "s_add_u32 m0, s49, 24448\n"
    "v_exp_f32_e64 v133, v133\n"
    "buffer_load_dwordx4 v2, s[20:23], s58 offen offset:128 lds\n"
    "v_exp_f32 v134, v134\n"
    "v_exp_f32 v135, v135\n"
    "v_pk_add_f32 v[132:133], v[132:133], 1.0 op_sel_hi:[1,0]\n"
    "v_pk_add_f32 v[134:135], v[134:135], 1.0 op_sel_hi:[1,0]\n"
    "v_rcp_f32 v132, v132\n"
    "v_rcp_f32 v133, v133\n"
    "v_rcp_f32_e64 v134, v134\n"
    "s_add_u32 m0, s49, 25472\n"
    "v_rcp_f32_e64 v135, v135\n"
    "buffer_load_dwordx4 v3, s[20:23], s58 offen offset:128 lds\n"
    "v_pk_mul_f32 v[108:109], v[108:109], v[132:133]\n"
    "v_pk_mul_f32 v[110:111], v[110:111], v[134:135]\n"
    "v_pk_add_f32 v[168:169], v[168:169], v[108:109]\n"
    "v_pk_add_f32 v[170:171], v[170:171], v[110:111]\n"
    "v_pk_fma_f32 v[184:185], v[108:109], v[108:109], v[184:185]\n"
    "v_pk_fma_f32 v[186:187], v[110:111], v[110:111], v[186:187]\n"
    "s_waitcnt lgkmcnt(0)\n"
    "s_nop 0\n"
    "v_pk_fma_f32 v[112:113], v[56:57], v[192:193], v[112:113] op_sel_hi:[1,0,1] neg_lo:[1,0,0] neg_hi:[1,0,0]\n"
    "v_pk_fma_f32 v[114:115], v[58:59], v[192:193], v[114:115] op_sel_hi:[1,0,1] neg_lo:[1,0,0] neg_hi:[1,0,0]\n"
    "v_pk_mul_f32 v[112:113], v[112:113], v[60:61]\n"
    "v_pk_mul_f32 v[114:115], v[114:115], v[62:63]\n"
    "s_add_u32 m0, s49, 38656\n"
    "v_pk_mul_f32 v[128:129], v[112:113], v[112:113]\n"
    "buffer_load_dwordx4 v2, s[20:23], s55 offen offset:256 lds\n"
    "v_pk_mul_f32 v[130:131], v[114:115], v[114:115]\n"
    "v_pk_fma_f32 v[128:129], v[128:129], v[30:31], v[28:29] op_sel_hi:[1,0,0]\n"
    "v_pk_fma_f32 v[130:131], v[130:131], v[30:31], v[28:29] op_sel_hi:[1,0,0]\n"
    "v_pk_mul_f32 v[128:129], v[112:113], v[128:129]\n"
    "v_pk_mul_f32 v[130:131], v[114:115], v[130:131]\n"
    "v_exp_f32 v128, v128\n"
    "v_exp_f32 v129, v129\n"
    "v_exp_f32 v130, v130\n"
    "v_exp_f32 v131, v131\n"
    "s_add_u32 m0, s49, 39680\n"
    "v_pk_add_f32 v[128:129], v[128:129], 1.0 op_sel_hi:[1,0]\n"
    "buffer_load_dwordx4 v3, s[20:23], s55 offen offset:256 lds\n"
    "v_pk_add_f32 v[130:131], v[130:131], 1.0 op_sel_hi:[1,0]\n"
    "v_rcp_f32 v128, v128\n"
    "v_rcp_f32 v129, v129\n"
    "v_rcp_f32 v130, v130\n"
    "v_rcp_f32 v131, v131\n"
    "v_pk_mul_f32 v[112:113], v[112:113], v[128:129]\n"
    "v_pk_mul_f32 v[114:115], v[114:115], v[130:131]\n"
    "v_pk_mul_f32 v[188:189], v[112:113], v[112:113]\n"
    "v_pk_mul_f32 v[190:191], v[114:115], v[114:115]\n"
    "s_add_u32 m0, s49, 40704\n"
    "v_pk_fma_f32 v[116:117], v[56:57], v[194:195], v[116:117] op_sel_hi:[1,0,1] neg_lo:[1,0,0] neg_hi:[1,0,0]\n"
    "buffer_load_dwordx4 v2, s[20:23], s56 offen offset:256 lds\n"
    "v_pk_fma_f32 v[118:119], v[58:59], v[194:195], v[118:119] op_sel_hi:[1,0,1] neg_lo:[1,0,0] neg_hi:[1,0,0]\n"
    "v_pk_mul_f32 v[116:117], v[116:117], v[60:61]\n"
    "v_pk_mul_f32 v[118:119], v[118:119], v[62:63]\n"
    "v_pk_mul_f32 v[132:133], v[116:117], v[116:117]\n"
    "v_pk_mul_f32 v[134:135], v[118:119], v[118:119]\n"
    "v_pk_fma_f32 v[132:133], v[132:133], v[30:31], v[28:29] op_sel_hi:[1,0,0]\n"
    "v_pk_fma_f32 v[134:135], v[134:135], v[30:31], v[28:29] op_sel_hi:[1,0,0]\n"
    "v_pk_mul_f32 v[132:133], v[116:117], v[132:133]\n"
    "v_pk_mul_f32 v[134:135], v[118:119], v[134:135]\n"
    "v_exp_f32 v132, v132\n"
    "v_exp_f32 v133, v133\n"
    "s_add_u32 m0, s49, 41728\n"
    "v_exp_f32_e64 v134, v134\n"
    "buffer_load_dwordx4 v3, s[20:23], s56 offen offset:256 lds\n"
    "v_exp_f32_e64 v135, v135\n"
    "v_pk_add_f32 v[132:133], v[132:133], 1.0 op_sel_hi:[1,0]\n"
    "v_pk_add_f32 v[134:135], v[134:135], 1.0 op_sel_hi:[1,0]\n"
    "v_rcp_f32 v132, v132\n"
    "v_rcp_f32 v133, v133\n"
    "v_rcp_f32 v134, v134\n"
    "v_rcp_f32 v135, v135\n"
    "s_add_u32 m0, s49, 55040\n"
    "v_pk_mul_f32 v[116:117], v[116:117], v[132:133]\n"
    "buffer_load_dwordx4 v2, s[20:23], s57 offen offset:256 lds\n"
    "v_pk_mul_f32 v[118:119], v[118:119], v[134:135]\n"
    "v_pk_add_f32 v[172:173], v[112:113], v[116:117]\n"
    "v_pk_add_f32 v[174:175], v[114:115], v[118:119]\n"
    "v_pk_fma_f32 v[188:189], v[116:117], v[116:117], v[188:189]\n"
    "v_pk_fma_f32 v[190:191], v[118:119], v[118:119], v[190:191]\n"
    "v_pk_fma_f32 v[120:121], v[56:57], v[196:197], v[120:121] op_sel_hi:[1,0,1] neg_lo:[1,0,0] neg_hi:[1,0,0]\n"
    "v_pk_fma_f32 v[122:123], v[58:59], v[196:197], v[122:123] op_sel_hi:[1,0,1] neg_lo:[1,0,0] neg_hi:[1,0,0]\n"
    "v_pk_mul_f32 v[120:121], v[120:121], v[60:61]\n"
    "v_pk_mul_f32 v[122:123], v[122:123], v[62:63]\n"
    "v_pk_mul_f32 v[128:129], v[120:121], v[120:121]\n"
    "v_pk_mul_f32 v[130:131], v[122:123], v[122:123]\n"
    "v_pk_fma_f32 v[128:129], v[128:129], v[30:31], v[28:29] op_sel_hi:[1,0,0]\n"
    "v_pk_fma_f32 v[130:131], v[130:131], v[30:31], v[28:29] op_sel_hi:[1,0,0]\n"
    "s_add_u32 m0, s49, 56064\n"
    "v_pk_mul_f32 v[128:129], v[120:121], v[128:129]\n"
    "buffer_load_dwordx4 v3, s[20:23], s57 offen offset:256 lds\n"
    "v_pk_mul_f32 v[130:131], v[122:123], v[130:131]\n"
    "v_exp_f32 v128, v128\n"
    "v_exp_f32 v129, v129\n"
    "v_exp_f32 v130, v130\n"
    "v_exp_f32 v131, v131\n"
    "v_pk_add_f32 v[128:129], v[128:129], 1.0 op_sel_hi:[1,0]\n"
    "v_pk_add_f32 v[130:131], v[130:131], 1.0 op_sel_hi:[1,0]\n"
    "v_rcp_f32_e64 v128, v128\n"
    "s_add_u32 m0, s49, 57088\n"
    "v_rcp_f32_e64 v129, v129\n"
    "buffer_load_dwordx4 v2, s[20:23], s58 offen offset:256 lds\n"
    "v_rcp_f32 v130, v130\n"
    "v_rcp_f32 v131, v131\n"
    "v_pk_mul_f32 v[120:121], v[120:121], v[128:129]\n"
    "v_pk_mul_f32 v[122:123], v[122:123], v[130:131]\n"
    "v_pk_add_f32 v[172:173], v[172:173], v[120:121]\n"
    "v_pk_add_f32 v[174:175], v[174:175], v[122:123]\n"
    "v_pk_fma_f32 v[188:189], v[120:121], v[120:121], v[188:189]\n"
    "v_pk_fma_f32 v[190:191], v[122:123], v[122:123], v[190:191]\n"
    "v_pk_fma_f32 v[124:125], v[56:57], v[198:199], v[124:125] op_sel_hi:[1,0,1] neg_lo:[1,0,0] neg_hi:[1,0,0]\n"
    "s_add_u32 m0, s49, 58112\n"
    "v_pk_fma_f32 v[126:127], v[58:59], v[198:199], v[126:127] op_sel_hi:[1,0,1] neg_lo:[1,0,0] neg_hi:[1,0,0]\n"
    "buffer_load_dwordx4 v3, s[20:23], s58 offen offset:256 lds\n"
    "v_pk_mul_f32 v[124:125], v[124:125], v[60:61]\n"
    "v_pk_mul_f32 v[126:127], v[126:127], v[62:63]\n"
    "v_pk_mul_f32 v[132:133], v[124:125], v[124:125]\n"
    "v_pk_mul_f32 v[134:135], v[126:127], v[126:127]\n"
    "v_pk_fma_f32 v[132:133], v[132:133], v[30:31], v[28:29] op_sel_hi:[1,0,0]\n"
    "v_pk_fma_f32 v[134:135], v[134:135], v[30:31], v[28:29] op_sel_hi:[1,0,0]\n"
    "v_pk_mul_f32 v[132:133], v[124:125], v[132:133]\n"
    "v_pk_mul_f32 v[134:135], v[126:127], v[134:135]\n"
    "v_exp_f32 v132, v132\n"
    "v_exp_f32 v133, v133\n"
    "v_exp_f32 v134, v134\n"
    "v_exp_f32 v135, v135\n"
    "v_pk_add_f32 v[132:133], v[132:133], 1.0 op_sel_hi:[1,0]\n"
    "v_pk_add_f32 v[134:135], v[134:135], 1.0 op_sel_hi:[1,0]\n"
    "v_rcp_f32 v132, v132\n"
    "v_rcp_f32 v133, v133\n"
    "v_rcp_f32 v134, v134\n"
    "v_rcp_f32 v135, v135\n"
    "v_pk_mul_f32 v[124:125], v[124:125], v[132:133]\n"
    "v_pk_mul_f32 v[126:127], v[126:127], v[134:135]\n"
    "v_pk_add_f32 v[172:173], v[172:173], v[124:125]\n"
    "v_pk_add_f32 v[174:175], v[174:175], v[126:127]\n"
    "v_pk_fma_f32 v[188:189], v[124:125], v[124:125], v[188:189]\n"
    "v_pk_fma_f32 v[190:191], v[126:127], v[126:127], v[190:191]\n"
    "v_add_f32_dpp v40, v160, v160 row_ror:8 row_mask:0xf bank_mask:0x3\n"
    "v_add_f32_dpp v41, v161, v161 row_ror:8 row_mask:0xf bank_mask:0x3\n"
    "v_add_f32_dpp v42, v162, v162 row_ror:8 row_mask:0xf bank_mask:0x3\n"
    "v_add_f32_dpp v43, v163, v163 row_ror:8 row_mask:0xf bank_mask:0x3\n"
    "v_add_f32_dpp v44, v164, v164 row_ror:8 row_mask:0xf bank_mask:0x3\n"
    "v_add_f32_dpp v45, v165, v165 row_ror:8 row_mask:0xf bank_mask:0x3\n"
    "v_add_f32_dpp v46, v166, v166 row_ror:8 row_mask:0xf bank_mask:0x3\n"
    "v_add_f32_dpp v47, v167, v167 row_ror:8 row_mask:0xf bank_mask:0x3\n"
    "v_add_f32_dpp v48, v168, v168 row_ror:8 row_mask:0xf bank_mask:0x3\n"
    "v_add_f32_dpp v49, v169, v169 row_ror:8 row_mask:0xf bank_mask:0x3\n"
    "v_add_f32_dpp v50, v170, v170 row_ror:8 row_mask:0xf bank_mask:0x3\n"
    "v_add_f32_dpp v51, v171, v171 row_ror:8 row_mask:0xf bank_mask:0x3\n"
    "v_add_f32_dpp v52, v172, v172 row_ror:8 row_mask:0xf bank_mask:0x3\n"
    "v_add_f32_dpp v53, v173, v173 row_ror:8 row_mask:0xf bank_mask:0x3\n"
    "v_add_f32_dpp v54, v174, v174 row_ror:8 row_mask:0xf bank_mask:0x3\n"
    "v_add_f32_dpp v55, v175, v175 row_ror:8 row_mask:0xf bank_mask:0x3\n"
    "v_add_f32_dpp v40, v176, v176 row_ror:8 row_mask:0xf bank_mask:0xc\n"
    "v_add_f32_dpp v41, v177, v177 row_ror:8 row_mask:0xf bank_mask:0xc\n"
    "v_add_f32_dpp v42, v178, v178 row_ror:8 row_mask:0xf bank_mask:0xc\n"
    "v_add_f32_dpp v43, v179, v179 row_ror:8 row_mask:0xf bank_mask:0xc\n"
    "v_add_f32_dpp v44, v180, v180 row_ror:8 row_mask:0xf bank_mask:0xc\n"
    "v_add_f32_dpp v45, v181, v181 row_ror:8 row_mask:0xf bank_mask:0xc\n"
    "v_add_f32_dpp v46, v182, v182 row_ror:8 row_mask:0xf bank_mask:0xc\n"
    "v_add_f32_dpp v47, v183, v183 row_ror:8 row_mask:0xf bank_mask:0xc\n"
    "v_add_f32_dpp v48, v184, v184 row_ror:8 row_mask:0xf bank_mask:0xc\n"
    "v_add_f32_dpp v49, v185, v185 row_ror:8 row_mask:0xf bank_mask:0xc\n"
    "v_add_f32_dpp v50, v186, v186 row_ror:8 row_mask:0xf bank_mask:0xc\n"
    "v_add_f32_dpp v51, v187, v187 row_ror:8 row_mask:0xf bank_mask:0xc\n"
    "v_add_f32_dpp v52, v188, v188 row_ror:8 row_mask:0xf bank_mask:0xc\n"
    "v_add_f32_dpp v53, v189, v189 row_ror:8 row_mask:0xf bank_mask:0xc\n"
    "v_add_f32_dpp v54, v190, v190 row_ror:8 row_mask:0xf bank_mask:0xc\n"
    "v_add_f32_dpp v55, v191, v191 row_ror:8 row_mask:0xf bank_mask:0xc\n"
    "v_add_f32_dpp v56, v40, v40 row_half_mirror row_mask:0xf bank_mask:0x5\n"
    "v_add_f32_dpp v57, v41, v41 row_half_mirror row_mask:0xf bank_mask:0x5\n"
    "v_add_f32_dpp v58, v42, v42 row_half_mirror row_mask:0xf bank_mask:0x5\n"
    "v_add_f32_dpp v59, v43, v43 row_half_mirror row_mask:0xf bank_mask:0x5\n"
    "v_add_f32_dpp v60, v44, v44 row_half_mirror row_mask:0xf bank_mask:0x5\n"
    "v_add_f32_dpp v61, v45, v45 row_half_mirror row_mask:0xf bank_mask:0x5\n"
    "v_add_f32_dpp v62, v46, v46 row_half_mirror row_mask:0xf bank_mask:0x5\n"
    "v_add_f32_dpp v63, v47, v47 row_half_mirror row_mask:0xf bank_mask:0x5\n"
    "v_add_f32_dpp v56, v48, v48 row_half_mirror row_mask:0xf bank_mask:0xa\n"
    "v_add_f32_dpp v57, v49, v49 row_half_mirror row_mask:0xf bank_mask:0xa\n"
    "v_add_f32_dpp v58, v50, v50 row_half_mirror row_mask:0xf bank_mask:0xa\n"
    "v_add_f32_dpp v59, v51, v51 row_half_mirror row_mask:0xf bank_mask:0xa\n"
    "v_add_f32_dpp v60, v52, v52 row_half_mirror row_mask:0xf bank_mask:0xa\n"
    "v_add_f32_dpp v61, v53, v53 row_half_mirror row_mask:0xf bank_mask:0xa\n"
    "v_add_f32_dpp v62, v54, v54 row_half_mirror row_mask:0xf bank_mask:0xa\n"
    "v_add_f32_dpp v63, v55, v55 row_half_mirror row_mask:0xf bank_mask:0xa\n"
    "v_add_f32_dpp v56, v56, v56 quad_perm:[1,0,3,2] row_mask:0xf bank_mask:0xf\n"
    "v_add_f32_dpp v57, v57, v57 quad_perm:[1,0,3,2] row_mask:0xf bank_mask:0xf\n"
    "v_add_f32_dpp v58, v58, v58 quad_perm:[1,0,3,2] row_mask:0xf bank_mask:0xf\n"
    "v_add_f32_dpp v59, v59, v59 quad_perm:[1,0,3,2] row_mask:0xf bank_mask:0xf\n"
    "v_add_f32_dpp v60, v60, v60 quad_perm:[1,0,3,2] row_mask:0xf bank_mask:0xf\n"
    "v_add_f32_dpp v61, v61, v61 quad_perm:[1,0,3,2] row_mask:0xf bank_mask:0xf\n"
    "v_add_f32_dpp v62, v62, v62 quad_perm:[1,0,3,2] row_mask:0xf bank_mask:0xf\n"
    "v_add_f32_dpp v63, v63, v63 quad_perm:[1,0,3,2] row_mask:0xf bank_mask:0xf\n"
    "v_add_f32_dpp v56, v56, v56 quad_perm:[2,3,0,1] row_mask:0xf bank_mask:0xf\n"
    "v_add_f32_dpp v57, v57, v57 quad_perm:[2,3,0,1] row_mask:0xf bank_mask:0xf\n"
    "v_add_f32_dpp v58, v58, v58 quad_perm:[2,3,0,1] row_mask:0xf bank_mask:0xf\n"
    "v_add_f32_dpp v59, v59, v59 quad_perm:[2,3,0,1] row_mask:0xf bank_mask:0xf\n"
    "v_add_f32_dpp v60, v60, v60 quad_perm:[2,3,0,1] row_mask:0xf bank_mask:0xf\n"
    "v_add_f32_dpp v61, v61, v61 quad_perm:[2,3,0,1] row_mask:0xf bank_mask:0xf\n"
    "v_add_f32_dpp v62, v62, v62 quad_perm:[2,3,0,1] row_mask:0xf bank_mask:0xf\n"
    "v_add_f32_dpp v63, v63, v63 quad_perm:[2,3,0,1] row_mask:0xf bank_mask:0xf\n"
    ST(55)
    "ds_write_b128 v224, v[56:59] offset:2048\n"
    "ds_write_b128 v224, v[60:63] offset:2112\n"
    "s_waitcnt vmcnt(25) lgkmcnt(0)\n"
    "s_barrier\n"
    "ds_read2st64_b32 v[32:33], v221 offset0:8 offset1:12\n"
    "ds_read2st64_b32 v[34:35], v221 offset0:10 offset1:14\n"
    "ds_read_b64_tr_b16 v[128:129], v226 offset:0\n"
    "ds_read_b64_tr_b16 v[130:131], v228 offset:512\n"
    "ds_read_b64_tr_b16 v[132:133], v227 offset:0\n"
    "ds_read_b64_tr_b16 v[134:135], v229 offset:512\n"
    "ds_read_b64_tr_b16 v[136:137], v226 offset:4096\n"
    "ds_read_b64_tr_b16 v[138:139], v228 offset:4608\n"
    "ds_read_b64_tr_b16 v[140:141], v227 offset:4096\n"
    "ds_read_b64_tr_b16 v[142:143], v229 offset:4608\n"
    "v_mov_b32 v37, s46\n"
    "v_mov_b32 v39, s46\n"
    "s_waitcnt lgkmcnt(8)\n"
    "v_add_f32 v36, v32, v33\n"
    "v_add_f32_e64 v38, v34, v35\n"
    "buffer_store_dwordx4 v[36:39], v225, s[8:11], s79 offen sc1\n"
    ST(3)
    "s_add_u32 s70, s78, 0\n"
    "s_add_u32 s71, s78, 4096\n"
    "s_add_u32 s72, s78, 8192\n"
    "s_add_u32 s73, s78, 12288\n"
    "s_waitcnt lgkmcnt(4)\n"
    "v_mfma_f32_16x16x32_bf16 v[160:163], v[128:131], a[0:3], 0\n"
    "ds_read_b64_tr_b16 v[144:145], v226 offset:8192\n"
    "v_mfma_f32_16x16x32_bf16 v[164:167], v[128:131], a[4:7], 0\n"
    "ds_read_b64_tr_b16 v[146:147], v228 offset:8704\n"
    "v_mfma_f32_16x16x32_bf16 v[168:171], v[128:131], a[8:11], 0\n"
    "ds_read_b64_tr_b16 v[148:149], v227 offset:8192\n"
    "v_mfma_f32_16x16x32_bf16 v[172:175], v[128:131], a[12:15], 0\n"
    "ds_read_b64_tr_b16 v[150:151], v229 offset:8704\n"
    "v_mfma_f32_16x16x32_bf16 v[176:179], v[132:135], a[0:3], 0\n"
    "buffer_load_dwordx2 v[60:61], v231, s[8:11], 0 offen sc1\n"
    "v_mfma_f32_16x16x32_bf16 v[180:183], v[132:135], a[4:7], 0\n"
    "v_mfma_f32_16x16x32_bf16 v[184:187], v[132:135], a[8:11], 0\n"
    "v_mfma_f32_16x16x32_bf16 v[188:191], v[132:135], a[12:15], 0\n"
    "s_waitcnt lgkmcnt(4)\n"
    "v_mfma_f32_16x16x32_bf16 v[160:163], v[136:139], a[16:19], v[160:163]\n"
    "ds_read_b64_tr_b16 v[152:153], v226 offset:12288\n"
    "v_mfma_f32_16x16x32_bf16 v[164:167], v[136:139], a[20:23], v[164:167]\n"
    "ds_read_b64_tr_b16 v[154:155], v228 offset:12800\n"
    "v_mfma_f32_16x16x32_bf16 v[168:171], v[136:139], a[24:27], v[168:171]\n"
    "ds_read_b64_tr_b16 v[156:157], v227 offset:12288\n"
    "v_mfma_f32_16x16x32_bf16 v[172:175], v[136:139], a[28:31], v[172:175]\n"
    "ds_read_b64_tr_b16 v[158:159], v229 offset:12800\n"
    "v_mfma_f32_16x16x32_bf16 v[176:179], v[140:143], a[16:19], v[176:179]\n"
    "v_mfma_f32_16x16x32_bf16 v[180:183], v[140:143], a[20:23], v[180:183]\n"
    "v_mfma_f32_16x16x32_bf16 v[184:187], v[140:143], a[24:27], v[184:187]\n"
    "v_mfma_f32_16x16x32_bf16 v[188:191], v[140:143], a[28:31], v[188:191]\n"
    ST(24)
    "s_waitcnt lgkmcnt(4)\n"
    "v_mfma_f32_16x16x32_bf16 v[160:163], v[144:147], a[32:35], v[160:163]\n"
    "ds_read_b64_tr_b16 v[128:129], v226 offset:16384\n"
    "v_mfma_f32_16x16x32_bf16 v[164:167], v[144:147], a[36:39], v[164:167]\n"
    "ds_read_b64_tr_b16 v[130:131], v228 offset:16896\n"
    "v_mfma_f32_16x16x32_bf16 v[168:171], v[144:147], a[40:43], v[168:171]\n"
    "ds_read_b64_tr_b16 v[132:133], v227 offset:16384\n"
    "v_mfma_f32_16x16x32_bf16 v[172:175], v[144:147], a[44:47], v[172:175]\n"
    "ds_read_b64_tr_b16 v[134:135], v229 offset:16896\n"
    "v_mfma_f32_16x16x32_bf16 v[176:179], v[148:151], a[32:35], v[176:179]\n"
    "v_mfma_f32_16x16x32_bf16 v[180:183], v[148:151], a[36:39], v[180:183]\n"
    "v_mfma_f32_16x16x32_bf16 v[184:187], v[148:151], a[40:43], v[184:187]\n"
    "v_mfma_f32_16x16x32_bf16 v[188:191], v[148:151], a[44:47], v[188:191]\n"
    "s_waitcnt lgkmcnt(0)\n"
    "s_barrier\n"
    "v_mfma_f32_16x16x32_bf16 v[160:163], v[152:155], a[48:51], v[160:163]\n"
    "ds_read_b64_tr_b16 v[136:137], v226 offset:20480\n"
    "v_mfma_f32_16x16x32_bf16 v[164:167], v[152:155], a[52:55], v[164:167]\n"
    "ds_read_b64_tr_b16 v[138:139], v228 offset:20992\n"
    "s_cmp_eq_u32 s3, 7\n"
    "s_cselect_b32 s63, 1024, 0\n"
    "s_mul_i32 s66, s63, 1792\n"
    "s_sub_u32 s54, s54, s66\n"
    "v_mfma_f32_16x16x32_bf16 v[168:171], v[152:155], a[56:59], v[168:171]\n"
    "ds_read_b64_tr_b16 v[140:141], v227 offset:20480\n"
    "v_mfma_f32_16x16x32_bf16 v[172:175], v[152:155], a[60:63], v[172:175]\n"
    "ds_read_b64_tr_b16 v[142:143], v229 offset:20992\n"
    "s_add_u32 m0, s48, 104448\n"
    "v_mfma_f32_16x16x32_bf16 v[176:179], v[156:159], a[48:51], v[176:179]\n"
    "buffer_load_dwordx4 v250, s[16:19], s54 offen offset:0 lds\n"
    "v_mfma_f32_16x16x32_bf16 v[180:183], v[156:159], a[52:55], v[180:183]\n"
    "s_add_u32 m0, s48, 105472\n"
    "v_mfma_f32_16x16x32_bf16 v[184:187], v[156:159], a[56:59], v[184:187]\n"
    "buffer_load_dwordx4 v251, s[16:19], s54 offen offset:0 lds\n"
    "s_add_u32 s54, s54, 229376\n"
    "v_mfma_f32_16x16x32_bf16 v[188:191], v[156:159], a[60:63], v[188:191]\n"
    ST(25)
    "s_waitcnt lgkmcnt(4)\n"
    "v_mfma_f32_16x16x32_bf16 v[160:163], v[128:131], a[64:67], v[160:163]\n"
    "ds_read_b64_tr_b16 v[144:145], v226 offset:24576\n"
    "buffer_load_dwordx4 v[32:35], v230, s[8:11], s70 offen offset:0 sc1\n"
    "v_mfma_f32_16x16x32_bf16 v[164:167], v[128:131], a[68:71], v[164:167]\n"
    "ds_read_b64_tr_b16 v[146:147], v228 offset:25088\n"
    "buffer_load_dwordx4 v[36:39], v230, s[8:11], s70 offen offset:2048 sc1\n"
    "v_mfma_f32_16x16x32_bf16 v[168:171], v[128:131], a[72:75], v[168:171]\n"
    "ds_read_b64_tr_b16 v[148:149], v227 offset:24576\n"
    "buffer_load_dwordx4 v[40:43], v230, s[8:11], s71 offen offset:0 sc1\n"
    "v_mfma_f32_16x16x32_bf16 v[172:175], v[128:131], a[76:79], v[172:175]\n"
    "ds_read_b64_tr_b16 v[150:151], v229 offset:25088\n"
    "v_mfma_f32_16x16x32_bf16 v[176:179], v[132:135], a[64:67], v[176:179]\n"
    "buffer_load_dwordx4 v[44:47], v230, s[8:11], s71 offen offset:2048 sc1\n"
    "v_mfma_f32_16x16x32_bf16 v[180:183], v[132:135], a[68:71], v[180:183]\n"
    "buffer_load_dwordx4 v[48:51], v230, s[8:11], s72 offen offset:0 sc1\n"
    "v_mfma_f32_16x16x32_bf16 v[184:187], v[132:135], a[72:75], v[184:187]\n"
    "buffer_load_dwordx4 v[52:55], v230, s[8:11], s72 offen offset:2048 sc1\n"
    "v_mfma_f32_16x16x32_bf16 v[188:191], v[132:135], a[76:79], v[188:191]\n"
    "buffer_load_dwordx4 v[56:59], v230, s[8:11], s73 offen offset:0 sc1\n"
    "s_waitcnt lgkmcnt(4)\n"
    "v_mfma_f32_16x16x32_bf16 v[160:163], v[136:139], a[80:83], v[160:163]\n"
    "ds_read_b64_tr_b16 v[152:153], v226 offset:28672\n"
    "v_mfma_f32_16x16x32_bf16 v[164:167], v[136:139], a[84:87], v[164:167]\n"
    "ds_read_b64_tr_b16 v[154:155], v228 offset:29184\n"
    "v_mfma_f32_16x16x32_bf16 v[168:171], v[136:139], a[88:91], v[168:171]\n"
    "ds_read_b64_tr_b16 v[156:157], v227 offset:28672\n"
    "v_mfma_f32_16x16x32_bf16 v[172:175], v[136:139], a[92:95], v[172:175]\n"
    "ds_read_b64_tr_b16 v[158:159], v229 offset:29184\n"
    "v_mfma_f32_16x16x32_bf16 v[176:179], v[140:143], a[80:83], v[176:179]\n"
    "v_mfma_f32_16x16x32_bf16 v[180:183], v[140:143], a[84:87], v[180:183]\n"
    "v_mfma_f32_16x16x32_bf16 v[184:187], v[140:143], a[88:91], v[184:187]\n"
    "v_mfma_f32_16x16x32_bf16 v[188:191], v[140:143], a[92:95], v[188:191]\n"
    ST(26)
    "s_waitcnt lgkmcnt(4)\n"
    "v_mfma_f32_16x16x32_bf16 v[160:163], v[144:147], a[96:99], v[160:163]\n"
    "ds_read_b64_tr_b16 v[128:129], v226 offset:32768\n"
    "v_mfma_f32_16x16x32_bf16 v[164:167], v[144:147], a[100:103], v[164:167]\n"
    "ds_read_b64_tr_b16 v[130:131], v228 offset:33280\n"
    "v_mfma_f32_16x16x32_bf16 v[168:171], v[144:147], a[104:107], v[168:171]\n"
    "ds_read_b64_tr_b16 v[132:133], v227 offset:32768\n"
    "v_mfma_f32_16x16x32_bf16 v[172:175], v[144:147], a[108:111], v[172:175]\n"
    "ds_read_b64_tr_b16 v[134:135], v229 offset:33280\n"
    "v_mfma_f32_16x16x32_bf16 v[176:179], v[148:151], a[96:99], v[176:179]\n"
    "v_mfma_f32_16x16x32_bf16 v[180:183], v[148:151], a[100:103], v[180:183]\n"
    "v_mfma_f32_16x16x32_bf16 v[184:187], v[148:151], a[104:107], v[184:187]\n"
    "v_mfma_f32_16x16x32_bf16 v[188:191], v[148:151], a[108:111], v[188:191]\n"
    "s_waitcnt lgkmcnt(4)\n"
    "v_mfma_f32_16x16x32_bf16 v[160:163], v[152:155], a[112:115], v[160:163]\n"
    "ds_read_b64_tr_b16 v[136:137], v226 offset:36864\n"
    "v_mfma_f32_16x16x32_bf16 v[164:167], v[152:155], a[116:119], v[164:167]\n"
    "ds_read_b64_tr_b16 v[138:139], v228 offset:37376\n"
    "v_mfma_f32_16x16x32_bf16 v[168:171], v[152:155], a[120:123], v[168:171]\n"
    "ds_read_b64_tr_b16 v[140:141], v227 offset:36864\n"
    "v_mfma_f32_16x16x32_bf16 v[172:175], v[152:155], a[124:127], v[172:175]\n"
    "ds_read_b64_tr_b16 v[142:143], v229 offset:37376\n"
    "v_mfma_f32_16x16x32_bf16 v[176:179], v[156:159], a[112:115], v[176:179]\n"
    "v_mfma_f32_16x16x32_bf16 v[180:183], v[156:159], a[116:119], v[180:183]\n"
    "v_mfma_f32_16x16x32_bf16 v[184:187], v[156:159], a[120:123], v[184:187]\n"
    "v_mfma_f32_16x16x32_bf16 v[188:191], v[156:159], a[124:127], v[188:191]\n"
    ST(27)
    "s_waitcnt vmcnt(0) lgkmcnt(0)\n"
    ST(4)
    "v_xor_b32 v62, v33, v13\n"
    "v_bitop3_b32 v62, v62, v35, v13 bitop3:0xf6\n"
    "v_bitop3_b32 v62, v62, v37, v13 bitop3:0xf6\n"
    "v_bitop3_b32 v62, v62, v39, v13 bitop3:0xf6\n"
    "v_bitop3_b32 v62, v62, v41, v13 bitop3:0xf6\n"
    "v_bitop3_b32 v62, v62, v43, v13 bitop3:0xf6\n"
    "v_bitop3_b32 v62, v62, v45, v13 bitop3:0xf6\n"
    "v_bitop3_b32 v62, v62, v47, v13 bitop3:0xf6\n"
    "v_bitop3_b32 v62, v62, v49, v13 bitop3:0xf6\n"
    "v_bitop3_b32 v62, v62, v51, v13 bitop3:0xf6\n"
    "v_bitop3_b32 v62, v62, v53, v13 bitop3:0xf6\n"
    "v_bitop3_b32 v62, v62, v55, v13 bitop3:0xf6\n"
    "v_bitop3_b32 v62, v62, v57, v13 bitop3:0xf6\n"
    "v_bitop3_b32 v62, v62, v59, v13 bitop3:0xf6\n"
    "v_cmp_ne_u32 vcc, 0, v62\n"
    "s_cbranch_vccnz Lslow_21_%=\n"
    "Lback_22_%=:\n"
    "v_add_f32 v32, v32, v36\n"
    "v_add_f32 v34, v34, v38\n"
    "v_add_f32 v40, v40, v44\n"
    "v_add_f32 v42, v42, v46\n"
    "v_add_f32 v48, v48, v52\n"
    "v_add_f32 v50, v50, v54\n"
    "v_add_f32 v32, v32, v40\n"
    "v_add_f32 v34, v34, v42\n"
    "v_add_f32 v48, v48, v56\n"
    "v_add_f32 v50, v50, v58\n"
    "v_add_f32 v32, v32, v48\n"
    "v_add_f32 v34, v34, v50\n"
    "v_mul_f32 v32, 0x3a924925, v32\n"
    "v_mul_f32 v34, 0x3a924925, v34\n"
    "v_fma_f32 v34, -v32, v32, v34\n"
    "v_max_f32_e64 v34, 0, v34\n"
    "v_add_f32 v34, 0x3727c5ac, v34\n"
    "v_rsq_f32 v34, v34\n"
    "s_nop 0\n"
    "v_mul_f32_e64 v32, v32, v34\n"
    "ds_write2st64_b32 v233, v32, v34 offset1:1\n"
    "ds_read_b128 v[16:19], v234 offset:0\n"
    "ds_read_b128 v[20:23], v234 offset:256\n"
    "ds_read_b128 v[24:27], v234 offset:64\n"
    "ds_read_b128 v[28:31], v234 offset:320\n"
    "ds_read_b128 v[192:195], v234 offset:128\n"
    "ds_read_b128 v[196:199], v234 offset:384\n"
    "ds_read_b128 v[200:203], v234 offset:192\n"
    "ds_read_b128 v[204:207], v234 offset:448\n"
    ST(58)
    "s_waitcnt lgkmcnt(4)\n"
    "s_nop 0\n"
    "v_pk_fma_f32 v[64:65], v[64:65], v[20:21], v[16:17] neg_lo:[0,0,1] neg_hi:[0,0,1]\n"
    "v_pk_fma_f32 v[66:67], v[66:67], v[22:23], v[18:19] neg_lo:[0,0,1] neg_hi:[0,0,1]\n"
    "v_pk_fma_f32 v[80:81], v[80:81], v[28:29], v[24:25] neg_lo:[0,0,1] neg_hi:[0,0,1]\n"
    "v_pk_fma_f32 v[82:83], v[82:83], v[30:31], v[26:27] neg_lo:[0,0,1] neg_hi:[0,0,1]\n"
    "v_cvt_pk_bf16_f32 v64, v64, v80\n"
    "v_cvt_pk_bf16_f32 v65, v65, v81\n"
    "v_cvt_pk_bf16_f32 v66, v66, v82\n"
    "v_cvt_pk_bf16_f32 v67, v67, v83\n"
    "buffer_store_dwordx4 v[64:67], v1, s[8:11], s80 offen sc1\n"
    "s_add_u32 s80, s80, 7168\n"
    "v_pk_fma_f32 v[68:69], v[68:69], v[20:21], v[16:17] neg_lo:[0,0,1] neg_hi:[0,0,1]\n"
    "v_pk_fma_f32 v[70:71], v[70:71], v[22:23], v[18:19] neg_lo:[0,0,1] neg_hi:[0,0,1]\n"
    "v_pk_fma_f32 v[84:85], v[84:85], v[28:29], v[24:25] neg_lo:[0,0,1] neg_hi:[0,0,1]\n"
    "v_pk_fma_f32 v[86:87], v[86:87], v[30:31], v[26:27] neg_lo:[0,0,1] neg_hi:[0,0,1]\n"
    "v_cvt_pk_bf16_f32 v68, v68, v84\n"
    "v_cvt_pk_bf16_f32 v69, v69, v85\n"
    "v_cvt_pk_bf16_f32 v70, v70, v86\n"
    "v_cvt_pk_bf16_f32 v71, v71, v87\n"
    "buffer_store_dwordx4 v[68:71], v1, s[8:11], s81 offen sc1\n"
    "s_add_u32 s81, s81, 7168\n"
    "v_pk_fma_f32 v[72:73], v[72:73], v[20:21], v[16:17] neg_lo:[0,0,1] neg_hi:[0,0,1]\n"
    "v_pk_fma_f32 v[74:75], v[74:75], v[22:23], v[18:19] neg_lo:[0,0,1] neg_hi:[0,0,1]\n"
    "v_pk_fma_f32 v[88:89], v[88:89], v[28:29], v[24:25] neg_lo:[0,0,1] neg_hi:[0,0,1]\n"
    "v_pk_fma_f32 v[90:91], v[90:91], v[30:31], v[26:27] neg_lo:[0,0,1] neg_hi:[0,0,1]\n"
    "v_cvt_pk_bf16_f32 v72, v72, v88\n"
    "v_cvt_pk_bf16_f32 v73, v73, v89\n"
    "v_cvt_pk_bf16_f32 v74, v74, v90\n"
    "v_cvt_pk_bf16_f32 v75, v75, v91\n"
    "buffer_store_dwordx4 v[72:75], v1, s[8:11], s82 offen sc1\n"
    "s_add_u32 s82, s82, 7168\n"
    "v_pk_fma_f32 v[76:77], v[76:77], v[20:21], v[16:17] neg_lo:[0,0,1] neg_hi:[0,0,1]\n"
    "v_pk_fma_f32 v[78:79], v[78:79], v[22:23], v[18:19] neg_lo:[0,0,1] neg_hi:[0,0,1]\n"
    "v_pk_fma_f32 v[92:93], v[92:93], v[28:29], v[24:25] neg_lo:[0,0,1] neg_hi:[0,0,1]\n"
    "v_pk_fma_f32 v[94:95], v[94:95], v[30:31], v[26:27] neg_lo:[0,0,1] neg_hi:[0,0,1]\n"
    "v_cvt_pk_bf16_f32 v76, v76, v92\n"
    "v_cvt_pk_bf16_f32 v77, v77, v93\n"
    "v_cvt_pk_bf16_f32 v78, v78, v94\n"
    "v_cvt_pk_bf16_f32 v79, v79, v95\n"
    "buffer_store_dwordx4 v[76:79], v1, s[8:11], s83 offen sc1\n"
    "s_add_u32 s83, s83, 7168\n"
    "s_waitcnt lgkmcnt(0)\n"
    "v_pk_fma_f32 v[96:97], v[96:97], v[196:197], v[192:193] neg_lo:[0,0,1] neg_hi:[0,0,1]\n"
    "v_pk_fma_f32 v[98:99], v[98:99], v[198:199], v[194:195] neg_lo:[0,0,1] neg_hi:[0,0,1]\n"
    "v_pk_fma_f32 v[112:113], v[112:113], v[204:205], v[200:201] neg_lo:[0,0,1] neg_hi:[0,0,1]\n"
    "v_pk_fma_f32 v[114:115], v[114:115], v[206:207], v[202:203] neg_lo:[0,0,1] neg_hi:[0,0,1]\n"
    "v_cvt_pk_bf16_f32 v96, v96, v112\n"
    "v_cvt_pk_bf16_f32 v97, v97, v113\n"
    "v_cvt_pk_bf16_f32 v98, v98, v114\n"
    "v_cvt_pk_bf16_f32 v99, v99, v115\n"
    "buffer_store_dwordx4 v[96:99], v1, s[8:11], s80 offen sc1\n"
    "v_pk_fma_f32 v[100:101], v[100:101], v[196:197], v[192:193] neg_lo:[0,0,1] neg_hi:[0,0,1]\n"
    "v_pk_fma_f32 v[102:103], v[102:103], v[198:199], v[194:195] neg_lo:[0,0,1] neg_hi:[0,0,1]\n"
    "v_pk_fma_f32 v[116:117], v[116:117], v[204:205], v[200:201] neg_lo:[0,0,1] neg_hi:[0,0,1]\n"
    "v_pk_fma_f32 v[118:119], v[118:119], v[206:207], v[202:203] neg_lo:[0,0,1] neg_hi:[0,0,1]\n"
    "v_cvt_pk_bf16_f32 v100, v100, v116\n"
    "v_cvt_pk_bf16_f32 v101, v101, v117\n"
    "v_cvt_pk_bf16_f32 v102, v102, v118\n"
    "v_cvt_pk_bf16_f32 v103, v103, v119\n"
    "buffer_store_dwordx4 v[100:103], v1, s[8:11], s81 offen sc1\n"
    "v_pk_fma_f32 v[104:105], v[104:105], v[196:197], v[192:193] neg_lo:[0,0,1] neg_hi:[0,0,1]\n"
    "v_pk_fma_f32 v[106:107], v[106:107], v[198:199], v[194:195] neg_lo:[0,0,1] neg_hi:[0,0,1]\n"
    "v_pk_fma_f32 v[120:121], v[120:121], v[204:205], v[200:201] neg_lo:[0,0,1] neg_hi:[0,0,1]\n"
    "v_pk_fma_f32 v[122:123], v[122:123], v[206:207], v[202:203] neg_lo:[0,0,1] neg_hi:[0,0,1]\n"
    "v_cvt_pk_bf16_f32 v104, v104, v120\n"
    "v_cvt_pk_bf16_f32 v105, v105, v121\n"
    "v_cvt_pk_bf16_f32 v106, v106, v122\n"
    "v_cvt_pk_bf16_f32 v107, v107, v123\n"
    "buffer_store_dwordx4 v[104:107], v1, s[8:11], s82 offen sc1\n"
    "v_pk_fma_f32 v[108:109], v[108:109], v[196:197], v[192:193] neg_lo:[0,0,1] neg_hi:[0,0,1]\n"
    "v_pk_fma_f32 v[110:111], v[110:111], v[198:199], v[194:195] neg_lo:[0,0,1] neg_hi:[0,0,1]\n"
    "v_pk_fma_f32 v[124:125], v[124:125], v[204:205], v[200:201] neg_lo:[0,0,1] neg_hi:[0,0,1]\n"
    "v_pk_fma_f32 v[126:127], v[126:127], v[206:207], v[202:203] neg_lo:[0,0,1] neg_hi:[0,0,1]\n"
    "v_cvt_pk_bf16_f32 v108, v108, v124\n"
    "v_cvt_pk_bf16_f32 v109, v109, v125\n"
    "v_cvt_pk_bf16_f32 v110, v110, v126\n"
    "v_cvt_pk_bf16_f32 v111, v111, v127\n"
    "buffer_store_dwordx4 v[108:111], v1, s[8:11], s83 offen sc1\n"
    ST(5)
    "v_mfma_f32_16x16x32_bf16 v[160:163], v[128:131], a[128:131], v[160:163]\n"
    "ds_read_b64_tr_b16 v[144:145], v226 offset:40960\n"
    "ds_read_b64_tr_b16 v[146:147], v228 offset:41472\n"
    "v_mfma_f32_16x16x32_bf16 v[164:167], v[128:131], a[132:135], v[164:167]\n"
    "ds_read_b64_tr_b16 v[148:149], v227 offset:40960\n"
    "v_mfma_f32_16x16x32_bf16 v[168:171], v[128:131], a[136:139], v[168:171]\n"
    "ds_read_b64_tr_b16 v[150:151], v229 offset:41472\n"
    "ds_read_b128 a[0:3], v6 offset:6144\n"
    "v_mfma_f32_16x16x32_bf16 v[172:175], v[128:131], a[140:143], v[172:175]\n"
    "ds_read_b128 a[4:7], v6 offset:8192\n"
    "v_mfma_f32_16x16x32_bf16 v[176:179], v[132:135], a[128:131], v[176:179]\n"
    "ds_read_b128 a[16:19], v7 offset:6144\n"
    "ds_read_b128 a[20:23], v7 offset:8192\n"
    "v_mfma_f32_16x16x32_bf16 v[180:183], v[132:135], a[132:135], v[180:183]\n"
    "ds_read_b128 a[8:11], v6 offset:22528\n"
    "v_mfma_f32_16x16x32_bf16 v[184:187], v[132:135], a[136:139], v[184:187]\n"
    "ds_read_b128 a[12:15], v6 offset:24576\n"
    "ds_read_b128 a[24:27], v7 offset:22528\n"
    "v_mfma_f32_16x16x32_bf16 v[188:191], v[132:135], a[140:143], v[188:191]\n"
    "ds_read_b128 a[28:31], v7 offset:24576\n"
    "s_waitcnt lgkmcnt(0)\n"
    "v_mfma_f32_16x16x32_bf16 v[160:163], v[136:139], a[144:147], v[160:163]\n"
    "ds_read_b64_tr_b16 v[152:153], v226 offset:45056\n"
    "ds_read_b64_tr_b16 v[154:155], v228 offset:45568\n"
    "s_add_u32 m0, s49, 71296\n"
    "v_mfma_f32_16x16x32_bf16 v[164:167], v[136:139], a[148:151], v[164:167]\n"
    "buffer_load_dwordx4 v2, s[20:23], s55 offen offset:384 lds\n"
    "ds_read_b64_tr_b16 v[156:157], v227 offset:45056\n"
    "v_mfma_f32_16x16x32_bf16 v[168:171], v[136:139], a[152:155], v[168:171]\n"
    "ds_read_b64_tr_b16 v[158:159], v229 offset:45568\n"
    "ds_read_b128 a[32:35], v4 offset:6144\n"
    "v_mfma_f32_16x16x32_bf16 v[172:175], v[136:139], a[156:159], v[172:175]\n"
    "ds_read_b128 a[36:39], v4 offset:8192\n"
    "s_add_u32 m0, s49, 72320\n"
    "v_mfma_f32_16x16x32_bf16 v[176:179], v[140:143], a[144:147], v[176:179]\n"
    "buffer_load_dwordx4 v3, s[20:23], s55 offen offset:384 lds\n"
    "ds_read_b128 a[48:51], v5 offset:6144\n"
    "ds_read_b128 a[52:55], v5 offset:8192\n"
    "v_mfma_f32_16x16x32_bf16 v[180:183], v[140:143], a[148:151], v[180:183]\n"
    "ds_read_b128 a[40:43], v4 offset:22528\n"
    "s_add_u32 m0, s49, 73344\n"
    "v_mfma_f32_16x16x32_bf16 v[184:187], v[140:143], a[152:155], v[184:187]\n"
    "buffer_load_dwordx4 v2, s[20:23], s56 offen offset:384 lds\n"
    "ds_read_b128 a[44:47], v4 offset:24576\n"
    "ds_read_b128 a[56:59], v5 offset:22528\n"
    "v_mfma_f32_16x16x32_bf16 v[188:191], v[140:143], a[156:159], v[188:191]\n"
    "ds_read_b128 a[60:63], v5 offset:24576\n"
    ST(28)
    "s_waitcnt lgkmcnt(0)\n"
    "v_mfma_f32_16x16x32_bf16 v[160:163], v[144:147], a[160:163], v[160:163]\n"
    "v_cmp_ne_u32 vcc, s46, v61\n"
    "s_cbranch_vccnz Lslow_24_%=\n"
    "Lback_25_%=:\n"
    "s_add_u32 m0, s49, 74368\n"
    "v_mfma_f32_16x16x32_bf16 v[164:167], v[144:147], a[164:167], v[164:167]\n"
    "buffer_load_dwordx4 v3, s[20:23], s56 offen offset:384 lds\n"
    "ds_write_b32 v238, v60 offset:1536\n"
    "ds_read_b64_tr_b16 v[128:129], v226 offset:49152\n"
    "v_mfma_f32_16x16x32_bf16 v[168:171], v[144:147], a[168:171], v[168:171]\n"
    "ds_read_b64_tr_b16 v[130:131], v228 offset:49664\n"
    "ds_read_b64_tr_b16 v[132:133], v227 offset:49152\n"
    "v_mfma_f32_16x16x32_bf16 v[172:175], v[144:147], a[172:175], v[172:175]\n"
    "ds_read_b64_tr_b16 v[134:135], v229 offset:49664\n"
    "ds_read_b128 a[64:67], v4 offset:38912\n"
    "s_add_u32 m0, s49, 87680\n"
    "v_mfma_f32_16x16x32_bf16 v[176:179], v[148:151], a[160:163], v[176:179]\n"
    "buffer_load_dwordx4 v2, s[20:23], s57 offen offset:384 lds\n"
    "ds_read_b128 a[68:71], v4 offset:40960\n"
    "ds_read_b128 a[80:83], v5 offset:38912\n"
    "v_mfma_f32_16x16x32_bf16 v[180:183], v[148:151], a[164:167], v[180:183]\n"
    "ds_read_b128 a[84:87], v5 offset:40960\n"
    "ds_read_b128 a[72:75], v4 offset:55296\n"
    "s_add_u32 m0, s49, 88704\n"
    "v_mfma_f32_16x16x32_bf16 v[184:187], v[148:151], a[168:171], v[184:187]\n"
    "buffer_load_dwordx4 v3, s[20:23], s57 offen offset:384 lds\n"
    "ds_read_b128 a[76:79], v4 offset:57344\n"
    "ds_read_b128 a[88:91], v5 offset:55296\n"
    "v_mfma_f32_16x16x32_bf16 v[188:191], v[148:151], a[172:175], v[188:191]\n"
    "ds_read_b128 a[92:95], v5 offset:57344\n"
    "s_waitcnt lgkmcnt(0)\n"
    "v_mfma_f32_16x16x32_bf16 v[160:163], v[152:155], a[176:179], v[160:163]\n"
    "ds_read_b64_tr_b16 v[136:137], v226 offset:53248\n"
    "s_add_u32 m0, s49, 89728\n"
    "v_mfma_f32_16x16x32_bf16 v[164:167], v[152:155], a[180:183], v[164:167]\n"
    "buffer_load_dwordx4 v2, s[20:23], s58 offen offset:384 lds\n"
    "ds_read_b64_tr_b16 v[138:139], v228 offset:53760\n"
    "v_mfma_f32_16x16x32_bf16 v[168:171], v[152:155], a[184:187], v[168:171]\n"
    "ds_read_b64_tr_b16 v[140:141], v227 offset:53248\n"
    "v_mfma_f32_16x16x32_bf16 v[172:175], v[152:155], a[188:191], v[172:175]\n"
    "ds_read_b64_tr_b16 v[142:143], v229 offset:53760\n"
    "s_add_u32 m0, s49, 90752\n"
    "v_mfma_f32_16x16x32_bf16 v[176:179], v[156:159], a[176:179], v[176:179]\n"
    "buffer_load_dwordx4 v3, s[20:23], s58 offen offset:384 lds\n"
    "v_mfma_f32_16x16x32_bf16 v[180:183], v[156:159], a[180:183], v[180:183]\n"
    "s_add_u32 m0, s49, 5632\n"
    "v_mfma_f32_16x16x32_bf16 v[184:187], v[156:159], a[184:187], v[184:187]\n"
    "buffer_load_dwordx4 v2, s[20:23], s55 offen offset:512 lds\n"
    "v_mfma_f32_16x16x32_bf16 v[188:191], v[156:159], a[188:191], v[188:191]\n"
    ST(29)
    "s_waitcnt lgkmcnt(0)\n"
    "s_barrier\n"
    "v_mfma_f32_16x16x32_bf16 v[160:163], v[128:131], a[192:195], v[160:163]\n"
    "ds_read_b64_tr_b16 v[144:145], v226 offset:0\n"
    "s_add_u32 m0, s49, 6656\n"
    "v_mfma_f32_16x16x32_bf16 v[164:167], v[128:131], a[196:199], v[164:167]\n"
    "buffer_load_dwordx4 v3, s[20:23], s55 offen offset:512 lds\n"
    "ds_read_b64_tr_b16 v[146:147], v228 offset:512\n"
    "v_mfma_f32_16x16x32_bf16 v[168:171], v[128:131], a[200:203], v[168:171]\n"
    "ds_read_b64_tr_b16 v[148:149], v227 offset:0\n"
    "v_mfma_f32_16x16x32_bf16 v[172:175], v[128:131], a[204:207], v[172:175]\n"
    "ds_read_b64_tr_b16 v[150:151], v229 offset:512\n"
    "s_add_u32 m0, s49, 7680\n"
    "v_mfma_f32_16x16x32_bf16 v[176:179], v[132:135], a[192:195], v[176:179]\n"
    "buffer_load_dwordx4 v2, s[20:23], s56 offen offset:512 lds\n"
    "v_mfma_f32_16x16x32_bf16 v[180:183], v[132:135], a[196:199], v[180:183]\n"
    "s_add_u32 m0, s49, 8704\n"
    "v_mfma_f32_16x16x32_bf16 v[184:187], v[132:135], a[200:203], v[184:187]\n"
    "buffer_load_dwordx4 v3, s[20:23], s56 offen offset:512 lds\n"
    "v_mfma_f32_16x16x32_bf16 v[188:191], v[132:135], a[204:207], v[188:191]\n"
    "s_waitcnt lgkmcnt(4)\n"
    "v_mfma_f32_16x16x32_bf16 v[160:163], v[136:139], a[208:211], v[160:163]\n"
    "ds_read_b64_tr_b16 v[152:153], v226 offset:4096\n"
    "s_add_u32 m0, s49, 22016\n"
    "v_mfma_f32_16x16x32_bf16 v[164:167], v[136:139], a[212:215], v[164:167]\n"
    "buffer_load_dwordx4 v2, s[20:23], s57 offen offset:512 lds\n"
    "ds_read_b64_tr_b16 v[154:155], v228 offset:4608\n"
    "v_mfma_f32_16x16x32_bf16 v[168:171], v[136:139], a[216:219], v[168:171]\n"
    "ds_read_b64_tr_b16 v[156:157], v227 offset:4096\n"
    "v_mfma_f32_16x16x32_bf16 v[172:175], v[136:139], a[220:223], v[172:175]\n"
    "ds_read_b64_tr_b16 v[158:159], v229 offset:4608\n"
    "s_add_u32 m0, s49, 23040\n"
    "v_mfma_f32_16x16x32_bf16 v[176:179], v[140:143], a[208:211], v[176:179]\n"
    "buffer_load_dwordx4 v3, s[20:23], s57 offen offset:512 lds\n"
    "v_mfma_f32_16x16x32_bf16 v[180:183], v[140:143], a[212:215], v[180:183]\n"
    "s_add_u32 m0, s49, 24064\n"
    "v_mfma_f32_16x16x32_bf16 v[184:187], v[140:143], a[216:219], v[184:187]\n"
    "buffer_load_dwordx4 v2, s[20:23], s58 offen offset:512 lds\n"
    "v_mfma_f32_16x16x32_bf16 v[188:191], v[140:143], a[220:223], v[188:191]\n"
    ST(30)
    "s_waitcnt vmcnt(15)\n"
    "buffer_store_dword v13, v235, s[8:11], 0 offen sc1\n"
    "s_waitcnt lgkmcnt(4)\n"
    "v_mfma_f32_16x16x32_bf16 v[160:163], v[144:147], a[224:227], v[160:163]\n"
    "s_add_u32 m0, s49, 25088\n"
    "v_mfma_f32_16x16x32_bf16 v[164:167], v[144:147], a[228:231], v[164:167]\n"
    "buffer_load_dwordx4 v3, s[20:23], s58 offen offset:512 lds\n"
    "v_mfma_f32_16x16x32_bf16 v[168:171], v[144:147], a[232:235], v[168:171]\n"
    "v_mfma_f32_16x16x32_bf16 v[172:175], v[144:147], a[236:239], v[172:175]\n"
    "s_add_u32 m0, s49, 38272\n"
    "v_mfma_f32_16x16x32_bf16 v[176:179], v[148:151], a[224:227], v[176:179]\n"
    "buffer_load_dwordx4 v2, s[20:23], s55 offen offset:640 lds\n"
    "v_mfma_f32_16x16x32_bf16 v[180:183], v[148:151], a[228:231], v[180:183]\n"
    "s_add_u32 m0, s49, 39296\n"
    "v_mfma_f32_16x16x32_bf16 v[184:187], v[148:151], a[232:235], v[184:187]\n"
    "buffer_load_dwordx4 v3, s[20:23], s55 offen offset:640 lds\n"
    "v_mfma_f32_16x16x32_bf16 v[188:191], v[148:151], a[236:239], v[188:191]\n"
    "s_waitcnt lgkmcnt(0)\n"
    "v_mfma_f32_16x16x32_bf16 v[160:163], v[152:155], a[240:243], v[160:163]\n"
    "s_add_u32 m0, s49, 40320\n"
    "v_mfma_f32_16x16x32_bf16 v[164:167], v[152:155], a[244:247], v[164:167]\n"
    "buffer_load_dwordx4 v2, s[20:23], s56 offen offset:640 lds\n"
    "v_mfma_f32_16x16x32_bf16 v[168:171], v[152:155], a[248:251], v[168:171]\n"
    "v_mfma_f32_16x16x32_bf16 v[172:175], v[152:155], a[252:255], v[172:175]\n"
    "s_add_u32 m0, s49, 41344\n"
    "v_mfma_f32_16x16x32_bf16 v[176:179], v[156:159], a[240:243], v[176:179]\n"
    "buffer_load_dwordx4 v3, s[20:23], s56 offen offset:640 lds\n"
    "v_mfma_f32_16x16x32_bf16 v[180:183], v[156:159], a[244:247], v[180:183]\n"
    "s_add_u32 m0, s49, 54656\n"
    "v_mfma_f32_16x16x32_bf16 v[184:187], v[156:159], a[248:251], v[184:187]\n"
    "buffer_load_dwordx4 v2, s[20:23], s57 offen offset:640 lds\n"
    "v_mfma_f32_16x16x32_bf16 v[188:191], v[156:159], a[252:255], v[188:191]\n"
    ST(31)
    "ds_read_b32 v32, v232 offset:0\n"
    "ds_read_b32 v33, v232 offset:1024\n"
    "ds_read_b32 v34, v232 offset:64\n"
    "ds_read_b32 v35, v232 offset:1088\n"
    "ds_read_b32 v36, v232 offset:128\n"
    "ds_read_b32 v37, v232 offset:1152\n"
    "ds_read_b32 v38, v232 offset:192\n"
    "ds_read_b32 v39, v232 offset:1216\n"
    "ds_read_b128 v[40:43], v236 offset:1536\n"
    "ds_read_b128 v[44:47], v236 offset:1600\n"
    "s_add_u32 m0, s49, 55680\n"
    "s_nop 0\n"
    "buffer_load_dwordx4 v3, s[20:23], s57 offen offset:640 lds\n"
    "s_add_u32 m0, s49, 56704\n"
    "s_nop 0\n"
    "buffer_load_dwordx4 v2, s[20:23], s58 offen offset:640 lds\n"
    "s_add_u32 m0, s49, 57728\n"
    "s_nop 0\n"
    "buffer_load_dwordx4 v3, s[20:23], s58 offen offset:640 lds\n"
    "s_waitcnt vmcnt(17)\n"
    "ds_read_b128 a[96:99], v6 offset:6144\n"
    "ds_read_b128 a[100:103], v6 offset:8192\n"
    "ds_read_b128 a[112:115], v7 offset:6144\n"
    "ds_read_b128 a[116:119], v7 offset:8192\n"
    "ds_read_b128 a[104:107], v6 offset:22528\n"
    "ds_read_b128 a[108:111], v6 offset:24576\n"
    "ds_read_b128 a[120:123], v7 offset:22528\n"
    "ds_read_b128 a[124:127], v7 offset:24576\n"
    "s_waitcnt vmcnt(8)\n"
    "s_nop 0\n"
    "ds_read_b128 a[128:131], v4 offset:6144\n"
    "ds_read_b128 a[132:135], v4 offset:8192\n"
    "ds_read_b128 a[144:147], v5 offset:6144\n"
    "ds_read_b128 a[148:151], v5 offset:8192\n"
    "ds_read_b128 a[136:139], v4 offset:22528\n"
    "ds_read_b128 a[140:143], v4 offset:24576\n"
    "ds_read_b128 a[152:155], v5 offset:22528\n"
    "ds_read_b128 a[156:159], v5 offset:24576\n"
    "s_waitcnt lgkmcnt(0)\n"
    "s_add_u32 m0, s49, 70912\n"
    "s_nop 0\n"
    "buffer_load_dwordx4 v2, s[20:23], s55 offen offset:768 lds\n"
    "s_add_u32 m0, s49, 71936\n"
    "s_nop 0\n"
    "buffer_load_dwordx4 v3, s[20:23], s55 offen offset:768 lds\n"
    "s_add_u32 m0, s49, 72960\n"
    "s_nop 0\n"
    "buffer_load_dwordx4 v2, s[20:23], s56 offen offset:768 lds\n"
    "s_add_u32 m0, s49, 73984\n"
    "s_nop 0\n"
    "buffer_load_dwordx4 v3, s[20:23], s56 offen offset:768 lds\n"
    "s_add_u32 m0, s49, 87296\n"
    "s_nop 0\n"
    "buffer_load_dwordx4 v2, s[20:23], s57 offen offset:768 lds\n"
    "s_add_u32 m0, s49, 88320\n"
    "s_nop 0\n"
    "buffer_load_dwordx4 v3, s[20:23], s57 offen offset:768 lds\n"
    "s_add_u32 m0, s49, 89344\n"
    "s_nop 0\n"
    "buffer_load_dwordx4 v2, s[20:23], s58 offen offset:768 lds\n"
    "s_add_u32 m0, s49, 90368\n"
    "s_nop 0\n"
    "buffer_load_dwordx4 v3, s[20:23], s58 offen offset:768 lds\n"
    "s_add_u32 m0, s49, 5248\n"
    "s_nop 0\n"
    "buffer_load_dwordx4 v2, s[20:23], s55 offen offset:896 lds\n"
    "s_add_u32 m0, s49, 6272\n"
    "s_nop 0\n"
    "buffer_load_dwordx4 v3, s[20:23], s55 offen offset:896 lds\n"
    "s_add_u32 m0, s49, 7296\n"
    "s_nop 0\n"
    "buffer_load_dwordx4 v2, s[20:23], s56 offen offset:896 lds\n"
    "s_add_u32 m0, s49, 8320\n"
    "s_nop 0\n"
    "buffer_load_dwordx4 v3, s[20:23], s56 offen offset:896 lds\n"
    "s_add_u32 m0, s49, 21632\n"
    "s_nop 0\n"
    "buffer_load_dwordx4 v2, s[20:23], s57 offen offset:896 lds\n"
    "s_add_u32 m0, s49, 22656\n"
    "s_nop 0\n"
    "buffer_load_dwordx4 v3, s[20:23], s57 offen offset:896 lds\n"
    "s_add_u32 m0, s49, 23680\n"
    "s_nop 0\n"
    "buffer_load_dwordx4 v2, s[20:23], s58 offen offset:896 lds\n"
    "s_add_u32 m0, s49, 24704\n"
    "s_nop 0\n"
    "buffer_load_dwordx4 v3, s[20:23], s58 offen offset:896 lds\n"
    ST(57)
    "s_waitcnt vmcnt(16)\n"
    "ds_read_b128 a[160:163], v4 offset:38912\n"
    "ds_read_b128 a[164:167], v4 offset:40960\n"
    "ds_read_b128 a[176:179], v5 offset:38912\n"
    "ds_read_b128 a[180:183], v5 offset:40960\n"
    "ds_read_b128 a[168:171], v4 offset:55296\n"
    "ds_read_b128 a[172:175], v4 offset:57344\n"
    "ds_read_b128 a[184:187], v5 offset:55296\n"
    "ds_read_b128 a[188:191], v5 offset:57344\n"
    "s_waitcnt vmcnt(8)\n"
    "s_nop 0\n"
    "ds_read_b128 a[192:195], v6 offset:6144\n"
    "ds_read_b128 a[196:199], v6 offset:8192\n"
    "ds_read_b128 a[208:211], v7 offset:6144\n"
    "ds_read_b128 a[212:215], v7 offset:8192\n"
    "ds_read_b128 a[200:203], v6 offset:22528\n"
    "ds_read_b128 a[204:207], v6 offset:24576\n"
    "ds_read_b128 a[216:219], v7 offset:22528\n"
    "ds_read_b128 a[220:223], v7 offset:24576\n"
    "buffer_load_dword v28, v246, s[8:11], 0 offen sc1\n"
    "s_waitcnt lgkmcnt(0)\n"
    "s_barrier\n"
    "s_add_u32 m0, s48, 98304\n"
    "s_nop 0\n"
    "buffer_load_dwordx4 v8, s[24:27], s87 offen offset:0 lds\n"
    "s_add_u32 m0, s48, 99328\n"
    "s_nop 0\n"
    "buffer_load_dwordx4 v9, s[24:27], s87 offen offset:0 lds\n"
    "s_add_u32 s87, s87, 65536\n"
    "s_add_u32 m0, s48, 106496\n"
    "s_nop 0\n"
    "buffer_load_dwordx4 v8, s[24:27], s87 offen offset:0 lds\n"
    "s_add_u32 m0, s48, 107520\n"
    "s_nop 0\n"
    "buffer_load_dwordx4 v9, s[24:27], s87 offen offset:0 lds\n"
    "s_add_u32 s87, s87, 65536\n"
    "s_add_u32 m0, s48, 114688\n"
    "s_nop 0\n"
    "buffer_load_dwordx4 v8, s[24:27], s87 offen offset:0 lds\n"
    "s_add_u32 m0, s48, 115712\n"
    "s_nop 0\n"
    "buffer_load_dwordx4 v9, s[24:27], s87 offen offset:0 lds\n"
    "s_add_u32 s87, s87, 65536\n"
    "s_add_u32 m0, s48, 122880\n"
    "s_nop 0\n"
    "buffer_load_dwordx4 v8, s[24:27], s87 offen offset:0 lds\n"
    "s_add_u32 m0, s48, 123904\n"
    "s_nop 0\n"
    "buffer_load_dwordx4 v9, s[24:27], s87 offen offset:0 lds\n"
    "s_add_u32 s87, s87, 65536\n"
    "s_add_u32 m0, s48, 131072\n"
    "s_nop 0\n"
    "buffer_load_dwordx4 v8, s[24:27], s87 offen offset:0 lds\n"
    "s_add_u32 m0, s48, 132096\n"
    "s_nop 0\n"
    "buffer_load_dwordx4 v9, s[24:27], s87 offen offset:0 lds\n"
    "s_add_u32 s87, s87, 65536\n"
    "s_add_u32 m0, s48, 139264\n"
    "s_nop 0\n"
    "buffer_load_dwordx4 v8, s[24:27], s87 offen offset:0 lds\n"
    "s_add_u32 m0, s48, 140288\n"
    "s_nop 0\n"
    "buffer_load_dwordx4 v9, s[24:27], s87 offen offset:0 lds\n"
    "s_add_u32 s87, s87, 65536\n"
    "s_add_u32 m0, s48, 147456\n"
    "s_nop 0\n"
    "buffer_load_dwordx4 v8, s[24:27], s87 offen offset:0 lds\n"
    "s_add_u32 m0, s48, 148480\n"
    "s_nop 0\n"
    "buffer_load_dwordx4 v9, s[24:27], s87 offen offset:0 lds\n"
    "s_add_u32 s87, s87, 65536\n"
    "s_add_u32 m0, s48, 155648\n"
    "s_nop 0\n"
    "buffer_load_dwordx4 v8, s[24:27], s87 offen offset:0 lds\n"
    "s_add_u32 m0, s48, 156672\n"
    "s_nop 0\n"
    "buffer_load_dwordx4 v9, s[24:27], s87 offen offset:0 lds\n"
    "s_add_u32 s87, s87, 65536\n"
    "s_sub_u32 s87, s87, 524288\n"
    "s_waitcnt vmcnt(17)\n"
    "s_nop 0\n"
    "ds_read_b128 a[224:227], v4 offset:6144\n"
    "ds_read_b128 a[228:231], v4 offset:8192\n"
    "ds_read_b128 a[240:243], v5 offset:6144\n"
    "ds_read_b128 a[244:247], v5 offset:8192\n"
    "ds_read_b128 a[232:235], v4 offset:22528\n"
    "ds_read_b128 a[236:239], v4 offset:24576\n"
    "ds_read_b128 a[248:251], v5 offset:22528\n"
    "ds_read_b128 a[252:255], v5 offset:24576\n"
    "s_waitcnt vmcnt(16)\n"
    ST(7)
    "v_add_u32 v16, 0x1000, v1\n"
    "s_mov_b32 s66, 0x10000\n"
    "Lspin_28_%=:\n"
    "v_cmp_ne_u32 vcc, s46, v28\n"
    "s_cbranch_vccz Lok_27_%=\n"
    "buffer_load_dword v28, v246, s[8:11], 0 offen sc1\n"
    "s_waitcnt vmcnt(0)\n"
    "s_sub_u32 s66, s66, 1\n"
    "s_cbranch_scc0 Lspin_28_%=\n"
    "Lok_27_%=:\n"
    ST(8)
    "buffer_load_dwordx4 v[60:63], v1, s[8:11], s85 offen offset:0 sc1\n"
    "buffer_load_dwordx4 v[64:67], v1, s[8:11], s85 offen offset:1024 sc1\n"
    "buffer_load_dwordx4 v[68:71], v1, s[8:11], s85 offen offset:2048 sc1\n"
    "s_waitcnt lgkmcnt(0)\n"
    "s_nop 0\n"
    "v_mov_b32 v56, 0xc0135761\n"
    "v_mov_b32 v57, 0xbdd2d3e8\n"
    "v_pk_fma_f32 v[160:161], v[32:33], v[40:41], v[160:161] op_sel_hi:[0,1,1] neg_lo:[1,0,0] neg_hi:[1,0,0]\n"
    "v_pk_fma_f32 v[162:163], v[32:33], v[42:43], v[162:163] op_sel_hi:[0,1,1] neg_lo:[1,0,0] neg_hi:[1,0,0]\n"
    "v_pk_mul_f32 v[160:161], v[160:161], v[32:33] op_sel:[0,1] op_sel_hi:[1,1]\n"
    "v_pk_mul_f32 v[162:163], v[162:163], v[32:33] op_sel:[0,1] op_sel_hi:[1,1]\n"
    "v_pk_mul_f32 v[48:49], v[160:161], v[160:161]\n"
    "buffer_load_dwordx4 v[72:75], v1, s[8:11], s85 offen offset:3072 sc1\n"
    "v_pk_mul_f32 v[50:51], v[162:163], v[162:163]\n"
    "v_pk_fma_f32 v[48:49], v[48:49], v[56:57], v[56:57] op_sel:[0,1,0] op_sel_hi:[1,1,0]\n"
    "v_pk_fma_f32 v[50:51], v[50:51], v[56:57], v[56:57] op_sel:[0,1,0] op_sel_hi:[1,1,0]\n"
    "v_pk_mul_f32 v[48:49], v[160:161], v[48:49]\n"
    "v_pk_mul_f32 v[50:51], v[162:163], v[50:51]\n"
    "v_exp_f32 v48, v48\n"
    "v_exp_f32 v49, v49\n"
    "v_exp_f32 v50, v50\n"
    "v_exp_f32 v51, v51\n"
    "buffer_load_dwordx4 v[76:79], v16, s[8:11], s85 offen offset:0 sc1\n"
    "v_pk_add_f32 v[48:49], v[48:49], 1.0 op_sel_hi:[1,0]\n"
    "v_pk_add_f32 v[50:51], v[50:51], 1.0 op_sel_hi:[1,0]\n"
    "v_rcp_f32 v48, v48\n"
    "v_rcp_f32 v49, v49\n"
    "v_rcp_f32 v50, v50\n"
    "v_rcp_f32 v51, v51\n"
    "v_pk_mul_f32 v[160:161], v[160:161], v[48:49]\n"
    "v_pk_mul_f32 v[162:163], v[162:163], v[50:51]\n"
    "buffer_load_dwordx4 v[80:83], v16, s[8:11], s85 offen offset:1024 sc1\n"
    "v_cvt_pk_bf16_f32 v160, v160, v161\n"
    "v_cvt_pk_bf16_f32 v161, v162, v163\n"
    "buffer_store_dwordx2 v[160:161], v12, s[8:11], s77 offen offset:0 sc1\n"
    "v_pk_fma_f32 v[176:177], v[32:33], v[44:45], v[176:177] op_sel_hi:[0,1,1] neg_lo:[1,0,0] neg_hi:[1,0,0]\n"
    "v_pk_fma_f32 v[178:179], v[32:33], v[46:47], v[178:179] op_sel_hi:[0,1,1] neg_lo:[1,0,0] neg_hi:[1,0,0]\n"
    "v_pk_mul_f32 v[176:177], v[176:177], v[32:33] op_sel:[0,1] op_sel_hi:[1,1]\n"
    "v_pk_mul_f32 v[178:179], v[178:179], v[32:33] op_sel:[0,1] op_sel_hi:[1,1]\n"
    "v_pk_mul_f32 v[52:53], v[176:177], v[176:177]\n"
    "v_pk_mul_f32 v[54:55], v[178:179], v[178:179]\n"
    "v_pk_fma_f32 v[52:53], v[52:53], v[56:57], v[56:57] op_sel:[0,1,0] op_sel_hi:[1,1,0]\n"
    "v_pk_fma_f32 v[54:55], v[54:55], v[56:57], v[56:57] op_sel:[0,1,0] op_sel_hi:[1,1,0]\n"
    "v_pk_mul_f32 v[52:53], v[176:177], v[52:53]\n"
    "v_pk_mul_f32 v[54:55], v[178:179], v[54:55]\n"
    "buffer_load_dwordx4 v[84:87], v16, s[8:11], s85 offen offset:2048 sc1\n"
    "s_add_u32 s85, s85, 7168\n"
    "v_exp_f32 v52, v52\n"
    "v_exp_f32 v53, v53\n"
    "v_exp_f32 v54, v54\n"
    "v_exp_f32 v55, v55\n"
    "v_pk_add_f32 v[52:53], v[52:53], 1.0 op_sel_hi:[1,0]\n"
    "v_pk_add_f32 v[54:55], v[54:55], 1.0 op_sel_hi:[1,0]\n"
    "v_rcp_f32 v52, v52\n"
    "v_rcp_f32 v53, v53\n"
    "buffer_load_dwordx4 v[88:91], v1, s[8:11], s85 offen offset:0 sc1\n"
    "v_rcp_f32 v54, v54\n"
    "v_rcp_f32 v55, v55\n"
    "v_pk_mul_f32 v[176:177], v[176:177], v[52:53]\n"
    "v_pk_mul_f32 v[178:179], v[178:179], v[54:55]\n"
    "v_cvt_pk_bf16_f32 v176, v176, v177\n"
    "v_cvt_pk_bf16_f32 v177, v178, v179\n"
    "buffer_store_dwordx2 v[176:177], v12, s[8:11], s77 offen offset:512 sc1\n"
    "s_add_u32 s77, s77, 28672\n"
    "v_pk_fma_f32 v[164:165], v[34:35], v[40:41], v[164:165] op_sel_hi:[0,1,1] neg_lo:[1,0,0] neg_hi:[1,0,0]\n"
    "v_pk_fma_f32 v[166:167], v[34:35], v[42:43], v[166:167] op_sel_hi:[0,1,1] neg_lo:[1,0,0] neg_hi:[1,0,0]\n"
    "buffer_load_dwordx4 v[92:95], v1, s[8:11], s85 offen offset:1024 sc1\n"
    "v_pk_mul_f32 v[164:165], v[164:165], v[34:35] op_sel:[0,1] op_sel_hi:[1,1]\n"
    "v_pk_mul_f32 v[166:167], v[166:167], v[34:35] op_sel:[0,1] op_sel_hi:[1,1]\n"
    "v_pk_mul_f32 v[48:49], v[164:165], v[164:165]\n"
    "v_pk_mul_f32 v[50:51], v[166:167], v[166:167]\n"
    "v_pk_fma_f32 v[48:49], v[48:49], v[56:57], v[56:57] op_sel:[0,1,0] op_sel_hi:[1,1,0]\n"
    "v_pk_fma_f32 v[50:51], v[50:51], v[56:57], v[56:57] op_sel:[0,1,0] op_sel_hi:[1,1,0]\n"
    "v_pk_mul_f32 v[48:49], v[164:165], v[48:49]\n"
    "v_pk_mul_f32 v[50:51], v[166:167], v[50:51]\n"
    "v_exp_f32 v48, v48\n"
    "v_exp_f32 v49, v49\n"
    "buffer_load_dwordx4 v[96:99], v1, s[8:11], s85 offen offset:2048 sc1\n"
    "v_exp_f32 v50, v50\n"
    "v_exp_f32 v51, v51\n"
    "v_pk_add_f32 v[48:49], v[48:49], 1.0 op_sel_hi:[1,0]\n"
    "v_pk_add_f32 v[50:51], v[50:51], 1.0 op_sel_hi:[1,0]\n"
    "v_rcp_f32 v48, v48\n"
    "v_rcp_f32 v49, v49\n"
    "v_rcp_f32 v50, v50\n"
    "v_rcp_f32 v51, v51\n"
    "buffer_load_dwordx4 v[100:103], v1, s[8:11], s85 offen offset:3072 sc1\n"
    "v_pk_mul_f32 v[164:165], v[164:165], v[48:49]\n"
    "v_pk_mul_f32 v[166:167], v[166:167], v[50:51]\n"
    "v_cvt_pk_bf16_f32 v164, v164, v165\n"
    "v_cvt_pk_bf16_f32 v165, v166, v167\n"
    "buffer_store_dwordx2 v[164:165], v12, s[8:11], s77 offen offset:0 sc1\n"
    "v_pk_fma_f32 v[180:181], v[34:35], v[44:45], v[180:181] op_sel_hi:[0,1,1] neg_lo:[1,0,0] neg_hi:[1,0,0]\n"
    "v_pk_fma_f32 v[182:183], v[34:35], v[46:47], v[182:183] op_sel_hi:[0,1,1] neg_lo:[1,0,0] neg_hi:[1,0,0]\n"
    "v_pk_mul_f32 v[180:181], v[180:181], v[34:35] op_sel:[0,1] op_sel_hi:[1,1]\n"
    "v_pk_mul_f32 v[182:183], v[182:183], v[34:35] op_sel:[0,1] op_sel_hi:[1,1]\n"
    "v_pk_mul_f32 v[52:53], v[180:181], v[180:181]\n"
    "v_pk_mul_f32 v[54:55], v[182:183], v[182:183]\n"
    "v_pk_fma_f32 v[52:53], v[52:53], v[56:57], v[56:57] op_sel:[0,1,0] op_sel_hi:[1,1,0]\n"
    "buffer_load_dwordx4 v[104:107], v16, s[8:11], s85 offen offset:0 sc1\n"
    "v_pk_fma_f32 v[54:55], v[54:55], v[56:57], v[56:57] op_sel:[0,1,0] op_sel_hi:[1,1,0]\n"
    "v_pk_mul_f32 v[52:53], v[180:181], v[52:53]\n"
    "v_pk_mul_f32 v[54:55], v[182:183], v[54:55]\n"
    "v_exp_f32 v52, v52\n"
    "v_exp_f32 v53, v53\n"
    "v_exp_f32 v54, v54\n"
    "v_exp_f32 v55, v55\n"
    "v_pk_add_f32 v[52:53], v[52:53], 1.0 op_sel_hi:[1,0]\n"
    "v_pk_add_f32 v[54:55], v[54:55], 1.0 op_sel_hi:[1,0]\n"
    "buffer_load_dwordx4 v[108:111], v16, s[8:11], s85 offen offset:1024 sc1\n"
    "v_rcp_f32 v52, v52\n"
    "v_rcp_f32 v53, v53\n"
    "v_rcp_f32 v54, v54\n"
    "v_rcp_f32 v55, v55\n"
    "v_pk_mul_f32 v[180:181], v[180:181], v[52:53]\n"
    "v_pk_mul_f32 v[182:183], v[182:183], v[54:55]\n"
    "v_cvt_pk_bf16_f32 v180, v180, v181\n"
    "v_cvt_pk_bf16_f32 v181, v182, v183\n"
    "buffer_load_dwordx4 v[112:115], v16, s[8:11], s85 offen offset:2048 sc1\n"
    "s_add_u32 s85, s85, 7168\n"
    "buffer_store_dwordx2 v[180:181], v12, s[8:11], s77 offen offset:512 sc1\n"
    "s_add_u32 s77, s77, 28672\n"
    "v_pk_fma_f32 v[168:169], v[36:37], v[40:41], v[168:169] op_sel_hi:[0,1,1] neg_lo:[1,0,0] neg_hi:[1,0,0]\n"
    "v_pk_fma_f32 v[170:171], v[36:37], v[42:43], v[170:171] op_sel_hi:[0,1,1] neg_lo:[1,0,0] neg_hi:[1,0,0]\n"
    "v_pk_mul_f32 v[168:169], v[168:169], v[36:37] op_sel:[0,1] op_sel_hi:[1,1]\n"
    "v_pk_mul_f32 v[170:171], v[170:171], v[36:37] op_sel:[0,1] op_sel_hi:[1,1]\n"
    "v_pk_mul_f32 v[48:49], v[168:169], v[168:169]\n"
    "v_pk_mul_f32 v[50:51], v[170:171], v[170:171]\n"
    "v_pk_fma_f32 v[48:49], v[48:49], v[56:57], v[56:57] op_sel:[0,1,0] op_sel_hi:[1,1,0]\n"
    "v_pk_fma_f32 v[50:51], v[50:51], v[56:57], v[56:57] op_sel:[0,1,0] op_sel_hi:[1,1,0]\n"
    "v_pk_mul_f32 v[48:49], v[168:169], v[48:49]\n"
    "v_pk_mul_f32 v[50:51], v[170:171], v[50:51]\n"
    "v_exp_f32_e64 v48, v48\n"
    "buffer_load_dwordx4 v[116:119], v1, s[8:11], s85 offen offset:0 sc1\n"
    "v_exp_f32 v49, v49\n"
    "v_exp_f32 v50, v50\n"
    "v_exp_f32_e64 v51, v51\n"
    "v_pk_add_f32 v[48:49], v[48:49], 1.0 op_sel_hi:[1,0]\n"
    "v_pk_add_f32 v[50:51], v[50:51], 1.0 op_sel_hi:[1,0]\n"
    "v_rcp_f32 v48, v48\n"
    "v_rcp_f32 v49, v49\n"
    "buffer_load_dwordx4 v[120:123], v1, s[8:11], s85 offen offset:1024 sc1\n"
    "v_rcp_f32 v50, v50\n"
    "v_rcp_f32 v51, v51\n"
    "v_pk_mul_f32 v[168:169], v[168:169], v[48:49]\n"
    "v_pk_mul_f32 v[170:171], v[170:171], v[50:51]\n"
    "v_cvt_pk_bf16_f32 v168, v168, v169\n"
    "v_cvt_pk_bf16_f32 v169, v170, v171\n"
    "buffer_store_dwordx2 v[168:169], v12, s[8:11], s77 offen offset:0 sc1\n"
    "v_pk_fma_f32 v[184:185], v[36:37], v[44:45], v[184:185] op_sel_hi:[0,1,1] neg_lo:[1,0,0] neg_hi:[1,0,0]\n"
    "v_pk_fma_f32 v[186:187], v[36:37], v[46:47], v[186:187] op_sel_hi:[0,1,1] neg_lo:[1,0,0] neg_hi:[1,0,0]\n"
    "v_pk_mul_f32 v[184:185], v[184:185], v[36:37] op_sel:[0,1] op_sel_hi:[1,1]\n"
    "v_pk_mul_f32 v[186:187], v[186:187], v[36:37] op_sel:[0,1] op_sel_hi:[1,1]\n"
    "buffer_load_dwordx4 v[124:127], v1, s[8:11], s85 offen offset:2048 sc1\n"
    "v_pk_mul_f32 v[52:53], v[184:185], v[184:185]\n"
    "v_pk_mul_f32 v[54:55], v[186:187], v[186:187]\n"
    "v_pk_fma_f32 v[52:53], v[52:53], v[56:57], v[56:57] op_sel:[0,1,0] op_sel_hi:[1,1,0]\n"
    "v_pk_fma_f32 v[54:55], v[54:55], v[56:57], v[56:57] op_sel:[0,1,0] op_sel_hi:[1,1,0]\n"
    "v_pk_mul_f32 v[52:53], v[184:185], v[52:53]\n"
    "v_pk_mul_f32 v[54:55], v[186:187], v[54:55]\n"
    "v_exp_f32 v52, v52\n"
    "v_exp_f32 v53, v53\n"
    "v_exp_f32_e64 v54, v54\n"
    "buffer_load_dwordx4 v[128:131], v1, s[8:11], s85 offen offset:3072 sc1\n"
    "v_exp_f32_e64 v55, v55\n"
    "v_pk_add_f32 v[52:53], v[52:53], 1.0 op_sel_hi:[1,0]\n"
    "v_pk_add_f32 v[54:55], v[54:55], 1.0 op_sel_hi:[1,0]\n"
    "v_rcp_f32 v52, v52\n"
    "v_rcp_f32 v53, v53\n"
    "v_rcp_f32 v54, v54\n"
    "v_rcp_f32 v55, v55\n"
    "v_pk_mul_f32 v[184:185], v[184:185], v[52:53]\n"
    "buffer_load_dwordx4 v[132:135], v16, s[8:11], s85 offen offset:0 sc1\n"
    "v_pk_mul_f32 v[186:187], v[186:187], v[54:55]\n"
    "v_cvt_pk_bf16_f32 v184, v184, v185\n"
    "v_cvt_pk_bf16_f32 v185, v186, v187\n"
    "buffer_store_dwordx2 v[184:185], v12, s[8:11], s77 offen offset:512 sc1\n"
    "s_add_u32 s77, s77, 28672\n"
    "v_pk_fma_f32 v[172:173], v[38:39], v[40:41], v[172:173] op_sel_hi:[0,1,1] neg_lo:[1,0,0] neg_hi:[1,0,0]\n"
    "v_pk_fma_f32 v[174:175], v[38:39], v[42:43], v[174:175] op_sel_hi:[0,1,1] neg_lo:[1,0,0] neg_hi:[1,0,0]\n"
    "v_pk_mul_f32 v[172:173], v[172:173], v[38:39] op_sel:[0,1] op_sel_hi:[1,1]\n"
    "v_pk_mul_f32 v[174:175], v[174:175], v[38:39] op_sel:[0,1] op_sel_hi:[1,1]\n"
    "v_pk_mul_f32 v[48:49], v[172:173], v[172:173]\n"
    "v_pk_mul_f32 v[50:51], v[174:175], v[174:175]\n"
    "v_pk_fma_f32 v[48:49], v[48:49], v[56:57], v[56:57] op_sel:[0,1,0] op_sel_hi:[1,1,0]\n"
    "v_pk_fma_f32 v[50:51], v[50:51], v[56:57], v[56:57] op_sel:[0,1,0] op_sel_hi:[1,1,0]\n"
    "buffer_load_dwordx4 v[136:139], v16, s[8:11], s85 offen offset:1024 sc1\n"
    "v_pk_mul_f32 v[48:49], v[172:173], v[48:49]\n"
    "v_pk_mul_f32 v[50:51], v[174:175], v[50:51]\n"
    "v_exp_f32 v48, v48\n"
    "v_exp_f32 v49, v49\n"
    "v_exp_f32 v50, v50\n"
    "v_exp_f32 v51, v51\n"
    "v_pk_add_f32 v[48:49], v[48:49], 1.0 op_sel_hi:[1,0]\n"
    "v_pk_add_f32 v[50:51], v[50:51], 1.0 op_sel_hi:[1,0]\n"
    "v_rcp_f32_e64 v48, v48\n"
    "buffer_load_dwordx4 v[140:143], v16, s[8:11], s85 offen offset:2048 sc1\n"
    "s_add_u32 s85, s85, 7168\n"
    "v_rcp_f32 v49, v49\n"
    "v_rcp_f32 v50, v50\n"
    "v_rcp_f32_e64 v51, v51\n"
    "v_pk_mul_f32 v[172:173], v[172:173], v[48:49]\n"
    "v_pk_mul_f32 v[174:175], v[174:175], v[50:51]\n"
    "v_cvt_pk_bf16_f32 v172, v172, v173\n"
    "v_cvt_pk_bf16_f32 v173, v174, v175\n"
    "buffer_store_dwordx2 v[172:173], v12, s[8:11], s77 offen offset:0 sc1\n"
    "v_pk_fma_f32 v[188:189], v[38:39], v[44:45], v[188:189] op_sel_hi:[0,1,1] neg_lo:[1,0,0] neg_hi:[1,0,0]\n"
    "v_pk_fma_f32 v[190:191], v[38:39], v[46:47], v[190:191] op_sel_hi:[0,1,1] neg_lo:[1,0,0] neg_hi:[1,0,0]\n"
    "v_pk_mul_f32 v[188:189], v[188:189], v[38:39] op_sel:[0,1] op_sel_hi:[1,1]\n"
    "v_pk_mul_f32 v[190:191], v[190:191], v[38:39] op_sel:[0,1] op_sel_hi:[1,1]\n"
    "v_pk_mul_f32 v[52:53], v[188:189], v[188:189]\n"
    "v_pk_mul_f32 v[54:55], v[190:191], v[190:191]\n"
    "v_pk_fma_f32 v[52:53], v[52:53], v[56:57], v[56:57] op_sel:[0,1,0] op_sel_hi:[1,1,0]\n"
    "v_pk_fma_f32 v[54:55], v[54:55], v[56:57], v[56:57] op_sel:[0,1,0] op_sel_hi:[1,1,0]\n"
    "v_pk_mul_f32 v[52:53], v[188:189], v[52:53]\n"
    "v_pk_mul_f32 v[54:55], v[190:191], v[54:55]\n"
    "v_exp_f32 v52, v52\n"
    "v_exp_f32 v53, v53\n"
    "v_exp_f32 v54, v54\n"
    "v_exp_f32 v55, v55\n"
    "v_pk_add_f32 v[52:53], v[52:53], 1.0 op_sel_hi:[1,0]\n"
    "v_pk_add_f32 v[54:55], v[54:55], 1.0 op_sel_hi:[1,0]\n"
    "v_rcp_f32 v52, v52\n"
    "v_rcp_f32 v53, v53\n"
    "v_rcp_f32 v54, v54\n"
    "v_rcp_f32 v55, v55\n"
    "v_pk_mul_f32 v[188:189], v[188:189], v[52:53]\n"
    "v_pk_mul_f32 v[190:191], v[190:191], v[54:55]\n"
    "v_cvt_pk_bf16_f32 v188, v188, v189\n"
    "v_cvt_pk_bf16_f32 v189, v190, v191\n"
    "buffer_store_dwordx2 v[188:189], v12, s[8:11], s77 offen offset:512 sc1\n"
    "s_add_u32 s77, s77, 28672\n"
    "s_branch LG_4_%=\n"
    "Lslow_18_%=:\n"
    "s_mov_b32 s66, 0x10000\n"
    "Lloop_20_%=:\n"
    "buffer_load_dwordx4 v[160:163], v220, s[8:11], 0 offen sc1\n"
    "buffer_load_dwordx2 v[164:165], v222, s[8:11], 0 offen sc1\n"
    "s_waitcnt vmcnt(0)\n"
    "v_cmp_ne_u32 vcc, s46, v161\n"
    "v_cmp_ne_u32 s[64:65], s46, v163\n"
    "s_or_b64 vcc, vcc, s[64:65]\n"
    "v_cmp_ne_u32 s[64:65], s46, v165\n"
    "s_or_b64 vcc, vcc, s[64:65]\n"
    "s_cbranch_vccz Lback_19_%=\n"
    "s_sub_u32 s66, s66, 1\n"
    "s_cbranch_scc0 Lloop_20_%=\n"
    "s_branch Lback_19_%=\n"
    "Lslow_21_%=:\n"
    "s_nop 0\n"
    "s_mov_b32 s66, 0x10000\n"
    "Lloop_23_%=:\n"
    "buffer_load_dwordx4 v[32:35], v230, s[8:11], s70 offen offset:0 sc1\n"
    "buffer_load_dwordx4 v[36:39], v230, s[8:11], s70 offen offset:2048 sc1\n"
    "buffer_load_dwordx4 v[40:43], v230, s[8:11], s71 offen offset:0 sc1\n"
    "buffer_load_dwordx4 v[44:47], v230, s[8:11], s71 offen offset:2048 sc1\n"
    "buffer_load_dwordx4 v[48:51], v230, s[8:11], s72 offen offset:0 sc1\n"
    "buffer_load_dwordx4 v[52:55], v230, s[8:11], s72 offen offset:2048 sc1\n"
    "buffer_load_dwordx4 v[56:59], v230, s[8:11], s73 offen offset:0 sc1\n"
    "s_waitcnt vmcnt(0)\n"
    "v_xor_b32 v62, v33, v13\n"
    "v_bitop3_b32 v62, v62, v35, v13 bitop3:0xf6\n"
    "v_bitop3_b32 v62, v62, v37, v13 bitop3:0xf6\n"
    "v_bitop3_b32 v62, v62, v39, v13 bitop3:0xf6\n"
    "v_bitop3_b32 v62, v62, v41, v13 bitop3:0xf6\n"
    "v_bitop3_b32 v62, v62, v43, v13 bitop3:0xf6\n"
    "v_bitop3_b32 v62, v62, v45, v13 bitop3:0xf6\n"
    "v_bitop3_b32 v62, v62, v47, v13 bitop3:0xf6\n"
    "v_bitop3_b32 v62, v62, v49, v13 bitop3:0xf6\n"
    "v_bitop3_b32 v62, v62, v51, v13 bitop3:0xf6\n"
    "v_bitop3_b32 v62, v62, v53, v13 bitop3:0xf6\n"
    "v_bitop3_b32 v62, v62, v55, v13 bitop3:0xf6\n"
    "v_bitop3_b32 v62, v62, v57, v13 bitop3:0xf6\n"
    "v_bitop3_b32 v62, v62, v59, v13 bitop3:0xf6\n"
    "v_cmp_ne_u32 vcc, 0, v62\n"
    "s_cbranch_vccz Lback_22_%=\n"
    "s_sub_u32 s66, s66, 1\n"
    "s_cbranch_scc0 Lloop_23_%=\n"
    "s_branch Lback_22_%=\n"
    "Lslow_24_%=:\n"
    "s_mov_b32 s66, 0x10000\n"
    "Lloop_26_%=:\n"
    "buffer_load_dwordx2 v[60:61], v231, s[8:11], 0 offen sc1\n"
    "s_waitcnt vmcnt(0)\n"
    "v_cmp_ne_u32 vcc, s46, v61\n"
    "s_cbranch_vccz Lback_25_%=\n"
    "s_sub_u32 s66, s66, 1\n"
    "s_cbranch_scc0 Lloop_26_%=\n"
    "s_branch Lback_25_%=\n"
    "LS_3_%=:\n"
    "v_lshlrev_b32 v22, 4, v18\n"
    "s_add_u32 s67, s59, 0\n"
    "s_add_u32 s70, s67, 38\n"
    "s_sub_u32 s71, s67, 14\n"
    "s_cselect_b32 s70, s70, s71\n"
    "s_cmp_lt_u32 s70, 24\n"
    "s_cselect_b32 s96, s12, s16\n"
    "s_cselect_b32 s97, s13, s17\n"
    "s_cselect_b32 s98, s14, s18\n"
    "s_cselect_b32 s71, 0, 512\n"
    "s_cselect_b32 s73, 0, 24\n"
    "s_add_u32 s71, s71, 3072\n"
    "s_sub_u32 s73, s70, s73\n"
    "s_lshl_b32 s73, s73, 7\n"
    "s_mul_i32 s74, s45, s71\n"
    "s_lshl_b32 s74, s74, 7\n"
    "s_add_u32 s73, s73, s74\n"
    "v_mad_u32_u24 v21, v16, s71, v22\n"
    "s_lshl_b32 s74, s71, 3\n"
    "s_lshl_b32 s70, s70, 9\n"
    "s_add_u32 s70, s70, 364544\n"
    "s_cmp_lt_u32 s67, 52\n"
    "s_cselect_b32 s80, s70, 2147483648\n"
    "s_cselect_b32 s73, s73, 2147483648\n"
    "buffer_load_dwordx4 v[96:99], v21, s[96:99], s73 offen\n"
    "s_add_u32 s73, s73, s74\n"
    "buffer_load_dwordx4 v[100:103], v21, s[96:99], s73 offen\n"
    "s_add_u32 s73, s73, s74\n"
    "buffer_load_dwordx4 v[104:107], v21, s[96:99], s73 offen\n"
    "s_add_u32 s73, s73, s74\n"
    "buffer_load_dwordx4 v[108:111], v21, s[96:99], s73 offen\n"
    "s_add_u32 s73, s73, s74\n"
    "buffer_load_dwordx4 v[112:115], v21, s[96:99], s73 offen\n"
    "s_add_u32 s73, s73, s74\n"
    "buffer_load_dwordx4 v[116:119], v21, s[96:99], s73 offen\n"
    "s_add_u32 s73, s73, s74\n"
    "buffer_load_dwordx4 v[120:123], v21, s[96:99], s73 offen\n"
    "s_add_u32 s73, s73, s74\n"
    "buffer_load_dwordx4 v[124:127], v21, s[96:99], s73 offen\n"
    "s_add_u32 s73, s73, s74\n"
    "buffer_load_dwordx4 v[128:131], v21, s[96:99], s73 offen\n"
    "s_add_u32 s73, s73, s74\n"
    "buffer_load_dwordx4 v[132:135], v21, s[96:99], s73 offen\n"
    "s_add_u32 s73, s73, s74\n"
    "buffer_load_dwordx4 v[136:139], v21, s[96:99], s73 offen\n"
    "s_add_u32 s73, s73, s74\n"
    "buffer_load_dwordx4 v[140:143], v21, s[96:99], s73 offen\n"
    "s_add_u32 s73, s73, s74\n"
    "buffer_load_dwordx4 v[144:147], v21, s[96:99], s73 offen\n"
    "s_add_u32 s73, s73, s74\n"
    "buffer_load_dwordx4 v[148:151], v21, s[96:99], s73 offen\n"
    "s_add_u32 s73, s73, s74\n"
    "buffer_load_dwordx4 v[152:155], v21, s[96:99], s73 offen\n"
    "s_add_u32 s73, s73, s74\n"
    "buffer_load_dwordx4 v[156:159], v21, s[96:99], s73 offen\n"
    "s_add_u32 s73, s73, s74\n"
    "v_lshl_add_u32 v2, v16, 13, v19\n"
    "v_lshl_add_u32 v8, v16, 10, v20\n"
    "v_and_b32 v23, 15, v0\n"
    "v_xor_b32 v3, 64, v2\n"
    "v_xor_b32 v9, 64, v8\n"
    "v_add_u32 v3, 0x10000, v3\n"
    "v_add_u32 v9, 0x2000, v9\n"
    "v_lshrrev_b32 v24, 1, v23\n"
    "v_xor_b32 v24, v24, v17\n"
    "v_lshlrev_b32_e64 v24, 4, v24\n"
    "v_lshl_add_u32 v11, v23, 7, v24\n"
    "v_add_u32 v4, s49, v11\n"
    "v_xor_b32 v5, 64, v4\n"
    "v_add_u32 v6, 0x10000, v4\n"
    "v_add_u32 v7, 0x10000, v5\n"
    "v_bfe_u32 v24, v0, 2, 2\n"
    "v_lshl_add_u32 v25, v17, 3, v24\n"
    "v_and_b32 v26, 3, v0\n"
    "v_lshlrev_b32 v26, 3, v26\n"
    "v_lshl_add_u32 v25, v25, 7, v26\n"
    "v_lshrrev_b32 v26, 1, v24\n"
    "v_and_b32 v27, 1, v17\n"
    "v_lshl_or_b32 v26, v27, 1, v26\n"
    "v_lshl_add_u32 v10, v26, 5, v25\n"
    "v_lshlrev_b32 v12, 3, v0\n"
    "v_lshlrev_b32 v14, 2, v23\n"
    "v_lshlrev_b32 v247, 4, v17\n"
    "s_lshr_b32 s61, s2, 5\n"
    "s_lshl3_add_u32 s61, s68, s61\n"
    "s_bfe_u32 s62, s2, 0x20003\n"
    "s_lshl2_add_u32 s62, s69, s62\n"
    "s_mov_b32 s22, 8388608\n"
    "s_mov_b32 s23, 0x20000\n"
    "s_add_u32 s70, s45, 0\n"
    "s_and_b32 s71, s70, 1\n"
    "s_and_b32 s70, s70, 2\n"
    "s_lshl4_add_u32 s70, s70, s71\n"
    "s_lshl_b32 s71, s61, 6\n"
    "s_add_u32 s70, s70, s71\n"
    "s_lshl_b32 s70, s70, 12\n"
    "s_add_u32 s55, s70, s47\n"
    "s_add_u32 s70, s45, 1\n"
    "s_and_b32 s71, s70, 1\n"
    "s_and_b32 s70, s70, 2\n"
    "s_lshl4_add_u32 s70, s70, s71\n"
    "s_lshl_b32 s71, s61, 6\n"
    "s_add_u32 s70, s70, s71\n"
    "s_lshl_b32 s70, s70, 12\n"
    "s_add_u32 s56, s70, s47\n"
    "s_add_u32 s70, s45, 2\n"
    "s_and_b32 s71, s70, 1\n"
    "s_and_b32 s70, s70, 2\n"
    "s_lshl4_add_u32 s70, s70, s71\n"
    "s_lshl_b32 s71, s61, 6\n"
    "s_add_u32 s70, s70, s71\n"
    "s_lshl_b32 s70, s70, 12\n"
    "s_add_u32 s57, s70, s47\n"
    "s_add_u32 s70, s45, 3\n"
    "s_and_b32 s71, s70, 1\n"
    "s_and_b32 s70, s70, 2\n"
    "s_lshl4_add_u32 s70, s70, s71\n"
    "s_lshl_b32 s71, s61, 6\n"
    "s_add_u32 s70, s70, s71\n"
    "s_lshl_b32 s70, s70, 12\n"
    "s_add_u32 s58, s70, s47\n"
    "v_lshlrev_b32_e64 v88, 2, v0\n"
    "v_lshl_add_u32 v23, v18, 3, v17\n"
    "v_lshlrev_b32 v23, 2, v23\n"
    "s_lshl_b32 s67, s45, 8\n"
    "s_add_u32 s67, s67, 2048\n"
    "v_add_u32 v23, s67, v23\n"
    "s_waitcnt vmcnt(16)\n"
    "v_add_u32 v13, 1, v13\n"
    "s_nop 0\n"
    "v_readfirstlane_b32 s46, v13\n"
    ST(1)
    "s_add_u32 s67, s59, 16\n"
    "s_add_u32 s70, s67, 38\n"
    "s_sub_u32 s71, s67, 14\n"
    "s_cselect_b32 s70, s70, s71\n"
    "s_cmp_lt_u32 s70, 24\n"
    "s_cselect_b32 s96, s12, s16\n"
    "s_cselect_b32 s97, s13, s17\n"
    "s_cselect_b32 s98, s14, s18\n"
    "s_cselect_b32 s71, 0, 512\n"
    "s_cselect_b32 s73, 0, 24\n"
    "s_add_u32 s71, s71, 3072\n"
    "s_sub_u32 s73, s70, s73\n"
    "s_lshl_b32 s73, s73, 7\n"
    "s_mul_i32 s74, s45, s71\n"
    "s_lshl_b32 s74, s74, 7\n"
    "s_add_u32 s73, s73, s74\n"
    "v_mad_u32_u24 v21, v16, s71, v22\n"
    "s_lshl_b32 s74, s71, 3\n"
    "s_lshl_b32 s70, s70, 9\n"
    "s_add_u32 s70, s70, 364544\n"
    "s_cmp_lt_u32 s67, 52\n"
    "s_cselect_b32 s81, s70, 2147483648\n"
    "s_cselect_b32 s73, s73, 2147483648\n"
    "s_waitcnt vmcnt(15)\n"
    "v_lshlrev_b32 v72, 16, v96\n"
    "v_and_b32 v73, 0xffff0000, v96\n"
    "v_lshlrev_b32_e64 v74, 16, v97\n"
    "v_and_b32 v75, 0xffff0000, v97\n"
    "v_lshlrev_b32_e64 v76, 16, v98\n"
    "v_and_b32 v77, 0xffff0000, v98\n"
    "v_lshlrev_b32_e64 v78, 16, v99\n"
    "v_and_b32 v79, 0xffff0000, v99\n"
    "buffer_load_dwordx4 v[160:163], v21, s[96:99], s73 offen\n"
    "s_add_u32 s73, s73, s74\n"
    "s_waitcnt vmcnt(15)\n"
    "v_lshlrev_b32_e64 v80, 16, v100\n"
    "v_and_b32 v81, 0xffff0000, v100\n"
    "v_lshlrev_b32_e64 v82, 16, v101\n"
    "v_and_b32 v83, 0xffff0000, v101\n"
    "v_lshlrev_b32_e64 v84, 16, v102\n"
    "v_and_b32 v85, 0xffff0000, v102\n"
    "v_lshlrev_b32_e64 v86, 16, v103\n"
    "v_and_b32 v87, 0xffff0000, v103\n"
    "buffer_load_dwordx4 v[164:167], v21, s[96:99], s73 offen\n"
    "s_add_u32 s73, s73, s74\n"
    "v_pk_add_f32 v[72:73], v[72:73], v[80:81]\n"
    "v_pk_add_f32 v[74:75], v[74:75], v[82:83]\n"
    "v_pk_add_f32 v[76:77], v[76:77], v[84:85]\n"
    "v_pk_add_f32 v[78:79], v[78:79], v[86:87]\n"
    "s_waitcnt vmcnt(15)\n"
    "v_lshlrev_b32_e64 v80, 16, v104\n"
    "v_and_b32 v81, 0xffff0000, v104\n"
    "v_lshlrev_b32_e64 v82, 16, v105\n"
    "v_and_b32 v83, 0xffff0000, v105\n"
    "v_lshlrev_b32_e64 v84, 16, v106\n"
    "v_and_b32 v85, 0xffff0000, v106\n"
    "v_lshlrev_b32_e64 v86, 16, v107\n"
    "v_and_b32 v87, 0xffff0000, v107\n"
    "buffer_load_dwordx4 v[168:171], v21, s[96:99], s73 offen\n"
    "s_add_u32 s73, s73, s74\n"
    "v_pk_add_f32 v[72:73], v[72:73], v[80:81]\n"
    "v_pk_add_f32 v[74:75], v[74:75], v[82:83]\n"
    "v_pk_add_f32 v[76:77], v[76:77], v[84:85]\n"
    "v_pk_add_f32 v[78:79], v[78:79], v[86:87]\n"
    "s_waitcnt vmcnt(15)\n"
    "v_lshlrev_b32_e64 v80, 16, v108\n"
    "v_and_b32 v81, 0xffff0000, v108\n"
    "v_lshlrev_b32_e64 v82, 16, v109\n"
    "v_and_b32 v83, 0xffff0000, v109\n"
    "v_lshlrev_b32_e64 v84, 16, v110\n"
    "v_and_b32 v85, 0xffff0000, v110\n"
    "v_lshlrev_b32_e64 v86, 16, v111\n"
    "v_and_b32 v87, 0xffff0000, v111\n"
    "buffer_load_dwordx4 v[172:175], v21, s[96:99], s73 offen\n"
    "s_add_u32 s73, s73, s74\n"
    "v_pk_add_f32 v[72:73], v[72:73], v[80:81]\n"
    "v_pk_add_f32 v[74:75], v[74:75], v[82:83]\n"
    "v_pk_add_f32 v[76:77], v[76:77], v[84:85]\n"
    "v_pk_add_f32 v[78:79], v[78:79], v[86:87]\n"
    "s_waitcnt vmcnt(15)\n"
    "v_lshlrev_b32_e64 v80, 16, v112\n"
    "v_and_b32 v81, 0xffff0000, v112\n"
    "v_lshlrev_b32_e64 v82, 16, v113\n"
    "v_and_b32 v83, 0xffff0000, v113\n"
    "v_lshlrev_b32_e64 v84, 16, v114\n"
    "v_and_b32 v85, 0xffff0000, v114\n"
    "v_lshlrev_b32_e64 v86, 16, v115\n"
    "v_and_b32 v87, 0xffff0000, v115\n"
    "buffer_load_dwordx4 v[176:179], v21, s[96:99], s73 offen\n"
    "s_add_u32 s73, s73, s74\n"
    "v_pk_add_f32 v[72:73], v[72:73], v[80:81]\n"
    "v_pk_add_f32 v[74:75], v[74:75], v[82:83]\n"
    "v_pk_add_f32 v[76:77], v[76:77], v[84:85]\n"
    "v_pk_add_f32 v[78:79], v[78:79], v[86:87]\n"
    "s_waitcnt vmcnt(15)\n"
    "v_lshlrev_b32_e64 v80, 16, v116\n"
    "v_and_b32 v81, 0xffff0000, v116\n"
    "v_lshlrev_b32_e64 v82, 16, v117\n"
    "v_and_b32 v83, 0xffff0000, v117\n"
    "v_lshlrev_b32_e64 v84, 16, v118\n"
    "v_and_b32 v85, 0xffff0000, v118\n"
    "v_lshlrev_b32_e64 v86, 16, v119\n"
    "v_and_b32 v87, 0xffff0000, v119\n"
    "buffer_load_dwordx4 v[180:183], v21, s[96:99], s73 offen\n"
    "s_add_u32 s73, s73, s74\n"
    "v_pk_add_f32 v[72:73], v[72:73], v[80:81]\n"
    "v_pk_add_f32 v[74:75], v[74:75], v[82:83]\n"
    "v_pk_add_f32 v[76:77], v[76:77], v[84:85]\n"
    "v_pk_add_f32 v[78:79], v[78:79], v[86:87]\n"
    "s_waitcnt vmcnt(15)\n"
    "v_lshlrev_b32_e64 v80, 16, v120\n"
    "v_and_b32 v81, 0xffff0000, v120\n"
    "v_lshlrev_b32_e64 v82, 16, v121\n"
    "v_and_b32 v83, 0xffff0000, v121\n"
    "v_lshlrev_b32_e64 v84, 16, v122\n"
    "v_and_b32 v85, 0xffff0000, v122\n"
    "v_lshlrev_b32_e64 v86, 16, v123\n"
    "v_and_b32 v87, 0xffff0000, v123\n"
    "buffer_load_dwordx4 v[184:187], v21, s[96:99], s73 offen\n"
    "s_add_u32 s73, s73, s74\n"
    "v_pk_add_f32 v[72:73], v[72:73], v[80:81]\n"
    "v_pk_add_f32 v[74:75], v[74:75], v[82:83]\n"
    "v_pk_add_f32 v[76:77], v[76:77], v[84:85]\n"
    "v_pk_add_f32 v[78:79], v[78:79], v[86:87]\n"
    "s_waitcnt vmcnt(15)\n"
    "v_lshlrev_b32_e64 v80, 16, v124\n"
    "v_and_b32 v81, 0xffff0000, v124\n"
    "v_lshlrev_b32_e64 v82, 16, v125\n"
    "v_and_b32 v83, 0xffff0000, v125\n"
    "v_lshlrev_b32_e64 v84, 16, v126\n"
    "v_and_b32 v85, 0xffff0000, v126\n"
    "v_lshlrev_b32_e64 v86, 16, v127\n"
    "v_and_b32 v87, 0xffff0000, v127\n"
    "buffer_load_dwordx4 v[188:191], v21, s[96:99], s73 offen\n"
    "s_add_u32 s73, s73, s74\n"
    "v_pk_add_f32 v[72:73], v[72:73], v[80:81]\n"
    "v_pk_add_f32 v[74:75], v[74:75], v[82:83]\n"
    "v_pk_add_f32 v[76:77], v[76:77], v[84:85]\n"
    "v_pk_add_f32 v[78:79], v[78:79], v[86:87]\n"
    "s_waitcnt vmcnt(15)\n"
    "v_lshlrev_b32_e64 v80, 16, v128\n"
    "v_and_b32 v81, 0xffff0000, v128\n"
    "v_lshlrev_b32_e64 v82, 16, v129\n"
    "v_and_b32 v83, 0xffff0000, v129\n"
    "v_lshlrev_b32_e64 v84, 16, v130\n"
    "v_and_b32 v85, 0xffff0000, v130\n"
    "v_lshlrev_b32_e64 v86, 16, v131\n"
    "v_and_b32 v87, 0xffff0000, v131\n"
    "buffer_load_dwordx4 v[192:195], v21, s[96:99], s73 offen\n"
    "s_add_u32 s73, s73, s74\n"
    "v_pk_add_f32 v[72:73], v[72:73], v[80:81]\n"
    "v_pk_add_f32 v[74:75], v[74:75], v[82:83]\n"
    "v_pk_add_f32 v[76:77], v[76:77], v[84:85]\n"
    "v_pk_add_f32 v[78:79], v[78:79], v[86:87]\n"
    "s_waitcnt vmcnt(15)\n"
    "v_lshlrev_b32_e64 v80, 16, v132\n"
    "v_and_b32 v81, 0xffff0000, v132\n"
    "v_lshlrev_b32_e64 v82, 16, v133\n"
    "v_and_b32 v83, 0xffff0000, v133\n"
    "v_lshlrev_b32_e64 v84, 16, v134\n"
    "v_and_b32 v85, 0xffff0000, v134\n"
    "v_lshlrev_b32_e64 v86, 16, v135\n"
    "v_and_b32 v87, 0xffff0000, v135\n"
    "buffer_load_dwordx4 v[196:199], v21, s[96:99], s73 offen\n"
    "s_add_u32 s73, s73, s74\n"
    "v_pk_add_f32 v[72:73], v[72:73], v[80:81]\n"
    "v_pk_add_f32 v[74:75], v[74:75], v[82:83]\n"
    "v_pk_add_f32 v[76:77], v[76:77], v[84:85]\n"
    "v_pk_add_f32 v[78:79], v[78:79], v[86:87]\n"
    "s_waitcnt vmcnt(15)\n"
    "v_lshlrev_b32_e64 v80, 16, v136\n"
    "v_and_b32 v81, 0xffff0000, v136\n"
    "v_lshlrev_b32_e64 v82, 16, v137\n"
    "v_and_b32 v83, 0xffff0000, v137\n"
    "v_lshlrev_b32_e64 v84, 16, v138\n"
    "v_and_b32 v85, 0xffff0000, v138\n"
    "v_lshlrev_b32_e64 v86, 16, v139\n"
    "v_and_b32 v87, 0xffff0000, v139\n"
    "buffer_load_dwordx4 v[200:203], v21, s[96:99], s73 offen\n"
    "s_add_u32 s73, s73, s74\n"
    "v_pk_add_f32 v[72:73], v[72:73], v[80:81]\n"
    "v_pk_add_f32 v[74:75], v[74:75], v[82:83]\n"
    "v_pk_add_f32 v[76:77], v[76:77], v[84:85]\n"
    "v_pk_add_f32 v[78:79], v[78:79], v[86:87]\n"
    "s_waitcnt vmcnt(15)\n"
    "v_lshlrev_b32_e64 v80, 16, v140\n"
    "v_and_b32 v81, 0xffff0000, v140\n"
    "v_lshlrev_b32_e64 v82, 16, v141\n"
    "v_and_b32 v83, 0xffff0000, v141\n"
    "v_lshlrev_b32_e64 v84, 16, v142\n"
    "v_and_b32 v85, 0xffff0000, v142\n"
    "v_lshlrev_b32_e64 v86, 16, v143\n"
    "v_and_b32 v87, 0xffff0000, v143\n"
    "buffer_load_dwordx4 v[204:207], v21, s[96:99], s73 offen\n"
    "s_add_u32 s73, s73, s74\n"
    "v_pk_add_f32 v[72:73], v[72:73], v[80:81]\n"
    "v_pk_add_f32 v[74:75], v[74:75], v[82:83]\n"
    "v_pk_add_f32 v[76:77], v[76:77], v[84:85]\n"
    "v_pk_add_f32 v[78:79], v[78:79], v[86:87]\n"
    "s_waitcnt vmcnt(15)\n"
    "v_lshlrev_b32_e64 v80, 16, v144\n"
    "v_and_b32 v81, 0xffff0000, v144\n"
    "v_lshlrev_b32_e64 v82, 16, v145\n"
    "v_and_b32 v83, 0xffff0000, v145\n"
    "v_lshlrev_b32_e64 v84, 16, v146\n"
    "v_and_b32 v85, 0xffff0000, v146\n"
    "v_lshlrev_b32_e64 v86, 16, v147\n"
    "v_and_b32 v87, 0xffff0000, v147\n"
    "buffer_load_dwordx4 v[208:211], v21, s[96:99], s73 offen\n"
    "s_add_u32 s73, s73, s74\n"
    "v_pk_add_f32 v[72:73], v[72:73], v[80:81]\n"
    "v_pk_add_f32 v[74:75], v[74:75], v[82:83]\n"
    "v_pk_add_f32 v[76:77], v[76:77], v[84:85]\n"
    "v_pk_add_f32 v[78:79], v[78:79], v[86:87]\n"
    "s_waitcnt vmcnt(15)\n"
    "v_lshlrev_b32_e64 v80, 16, v148\n"
    "v_and_b32 v81, 0xffff0000, v148\n"
    "v_lshlrev_b32_e64 v82, 16, v149\n"
    "v_and_b32 v83, 0xffff0000, v149\n"
    "v_lshlrev_b32_e64 v84, 16, v150\n"
    "v_and_b32 v85, 0xffff0000, v150\n"
    "v_lshlrev_b32_e64 v86, 16, v151\n"
    "v_and_b32 v87, 0xffff0000, v151\n"
    "buffer_load_dwordx4 v[212:215], v21, s[96:99], s73 offen\n"
    "s_add_u32 s73, s73, s74\n"
    "v_pk_add_f32 v[72:73], v[72:73], v[80:81]\n"
    "v_pk_add_f32 v[74:75], v[74:75], v[82:83]\n"
    "v_pk_add_f32 v[76:77], v[76:77], v[84:85]\n"
    "v_pk_add_f32 v[78:79], v[78:79], v[86:87]\n"
    "s_waitcnt vmcnt(15)\n"
    "v_lshlrev_b32_e64 v80, 16, v152\n"
    "v_and_b32 v81, 0xffff0000, v152\n"
    "v_lshlrev_b32_e64 v82, 16, v153\n"
    "v_and_b32 v83, 0xffff0000, v153\n"
    "v_lshlrev_b32_e64 v84, 16, v154\n"
    "v_and_b32 v85, 0xffff0000, v154\n"
    "v_lshlrev_b32_e64 v86, 16, v155\n"
    "v_and_b32 v87, 0xffff0000, v155\n"
    "buffer_load_dwordx4 v[216:219], v21, s[96:99], s73 offen\n"
    "s_add_u32 s73, s73, s74\n"
    "v_pk_add_f32 v[72:73], v[72:73], v[80:81]\n"
    "v_pk_add_f32 v[74:75], v[74:75], v[82:83]\n"
    "v_pk_add_f32 v[76:77], v[76:77], v[84:85]\n"
    "v_pk_add_f32 v[78:79], v[78:79], v[86:87]\n"
    "s_waitcnt vmcnt(15)\n"
    "v_lshlrev_b32_e64 v80, 16, v156\n"
    "v_and_b32 v81, 0xffff0000, v156\n"
    "v_lshlrev_b32_e64 v82, 16, v157\n"
    "v_and_b32 v83, 0xffff0000, v157\n"
    "v_lshlrev_b32_e64 v84, 16, v158\n"
    "v_and_b32 v85, 0xffff0000, v158\n"
    "v_lshlrev_b32_e64 v86, 16, v159\n"
    "v_and_b32 v87, 0xffff0000, v159\n"
    "buffer_load_dwordx4 v[220:223], v21, s[96:99], s73 offen\n"
    "s_add_u32 s73, s73, s74\n"
    "s_nop 0\n"
    "v_pk_add_f32 v[72:73], v[72:73], v[80:81]\n"
    "v_pk_add_f32 v[74:75], v[74:75], v[82:83]\n"
    "v_pk_add_f32 v[76:77], v[76:77], v[84:85]\n"
    "v_pk_add_f32 v[78:79], v[78:79], v[86:87]\n"
    "v_add_f32_dpp v72, v72, v72 row_ror:8 row_mask:0xf bank_mask:0xf\n"
    "v_add_f32_dpp v73, v73, v73 row_ror:8 row_mask:0xf bank_mask:0xf\n"
    "v_add_f32_dpp v74, v74, v74 row_ror:8 row_mask:0xf bank_mask:0xf\n"
    "v_add_f32_dpp v75, v75, v75 row_ror:8 row_mask:0xf bank_mask:0xf\n"
    "v_add_f32_dpp v76, v76, v76 row_ror:8 row_mask:0xf bank_mask:0xf\n"
    "v_add_f32_dpp v77, v77, v77 row_ror:8 row_mask:0xf bank_mask:0xf\n"
    "v_add_f32_dpp v78, v78, v78 row_ror:8 row_mask:0xf bank_mask:0xf\n"
    "v_add_f32_dpp v79, v79, v79 row_ror:8 row_mask:0xf bank_mask:0xf\n"
    "v_permlane16_swap_b32 v72, v73\n"
    "v_permlane16_swap_b32 v74, v75\n"
    "v_permlane16_swap_b32 v76, v77\n"
    "v_permlane16_swap_b32 v78, v79\n"
    "v_add_f32 v72, v72, v73\n"
    "v_add_f32 v74, v74, v75\n"
    "v_add_f32 v76, v76, v77\n"
    "v_add_f32 v78, v78, v79\n"
    "v_permlane32_swap_b32 v72, v74\n"
    "s_nop 0\n"
    "v_permlane32_swap_b32 v76, v78\n"
    "v_add_f32 v72, v72, v74\n"
    "v_add_f32_e64 v76, v76, v78\n"
    "ds_write2_b32 v23, v72, v76 offset1:4\n"
    "v_add_u32 v23, 1024, v23\n"
    "s_waitcnt lgkmcnt(0)\n"
    "s_barrier\n"
    "ds_read2st64_b32 v[72:73], v88 offset0:8 offset1:9\n"
    "ds_read2st64_b32 v[74:75], v88 offset0:10 offset1:11\n"
    "s_waitcnt lgkmcnt(0)\n"
    "v_add_f32 v72, v72, v73\n"
    "v_add_f32 v74, v74, v75\n"
    "v_mov_b32 v73, s46\n"
    "v_add_f32_e64 v72, v72, v74\n"
    "buffer_store_dwordx2 v[72:73], v12, s[8:11], s80 offen sc1\n"
    ST(2)
    "s_add_u32 s67, s59, 32\n"
    "s_add_u32 s70, s67, 38\n"
    "s_sub_u32 s71, s67, 14\n"
    "s_cselect_b32 s70, s70, s71\n"
    "s_cmp_lt_u32 s70, 24\n"
    "s_cselect_b32 s96, s12, s16\n"
    "s_cselect_b32 s97, s13, s17\n"
    "s_cselect_b32 s98, s14, s18\n"
    "s_cselect_b32 s71, 0, 512\n"
    "s_cselect_b32 s73, 0, 24\n"
    "s_add_u32 s71, s71, 3072\n"
    "s_sub_u32 s73, s70, s73\n"
    "s_lshl_b32 s73, s73, 7\n"
    "s_mul_i32 s74, s45, s71\n"
    "s_lshl_b32 s74, s74, 7\n"
    "s_add_u32 s73, s73, s74\n"
    "v_mad_u32_u24 v21, v16, s71, v22\n"
    "s_lshl_b32 s74, s71, 3\n"
    "s_lshl_b32 s70, s70, 9\n"
    "s_add_u32 s70, s70, 364544\n"
    "s_cmp_lt_u32 s67, 52\n"
    "s_cselect_b32 s82, s70, 2147483648\n"
    "s_cselect_b32 s73, s73, 2147483648\n"
    "s_waitcnt vmcnt(16)\n"
    "v_lshlrev_b32_e64 v72, 16, v160\n"
    "v_and_b32 v73, 0xffff0000, v160\n"
    "v_lshlrev_b32_e64 v74, 16, v161\n"
    "v_and_b32 v75, 0xffff0000, v161\n"
    "v_lshlrev_b32_e64 v76, 16, v162\n"
    "v_and_b32 v77, 0xffff0000, v162\n"
    "v_lshlrev_b32_e64 v78, 16, v163\n"
    "v_and_b32 v79, 0xffff0000, v163\n"
    "buffer_load_dwordx4 v[96:99], v21, s[96:99], s73 offen\n"
    "s_add_u32 s73, s73, s74\n"
    "s_waitcnt vmcnt(16)\n"
    "v_lshlrev_b32_e64 v80, 16, v164\n"
    "v_and_b32 v81, 0xffff0000, v164\n"
    "v_lshlrev_b32_e64 v82, 16, v165\n"
    "v_and_b32 v83, 0xffff0000, v165\n"
    "v_lshlrev_b32_e64 v84, 16, v166\n"
    "v_and_b32 v85, 0xffff0000, v166\n"
    "v_lshlrev_b32_e64 v86, 16, v167\n"
    "v_and_b32 v87, 0xffff0000, v167\n"
    "buffer_load_dwordx4 v[100:103], v21, s[96:99], s73 offen\n"
    "s_add_u32 s73, s73, s74\n"
    "v_pk_add_f32 v[72:73], v[72:73], v[80:81]\n"
    "v_pk_add_f32 v[74:75], v[74:75], v[82:83]\n"
    "v_pk_add_f32 v[76:77], v[76:77], v[84:85]\n"
    "v_pk_add_f32 v[78:79], v[78:79], v[86:87]\n"
    "s_waitcnt vmcnt(16)\n"
    "v_lshlrev_b32_e64 v80, 16, v168\n"
    "v_and_b32 v81, 0xffff0000, v168\n"
    "v_lshlrev_b32_e64 v82, 16, v169\n"
    "v_and_b32 v83, 0xffff0000, v169\n"
    "v_lshlrev_b32_e64 v84, 16, v170\n"
    "v_and_b32 v85, 0xffff0000, v170\n"
    "v_lshlrev_b32_e64 v86, 16, v171\n"
    "v_and_b32 v87, 0xffff0000, v171\n"
    "buffer_load_dwordx4 v[104:107], v21, s[96:99], s73 offen\n"
    "s_add_u32 s73, s73, s74\n"
    "v_pk_add_f32 v[72:73], v[72:73], v[80:81]\n"
    "v_pk_add_f32 v[74:75], v[74:75], v[82:83]\n"
    "v_pk_add_f32 v[76:77], v[76:77], v[84:85]\n"
    "v_pk_add_f32 v[78:79], v[78:79], v[86:87]\n"
    "s_waitcnt vmcnt(16)\n"
    "v_lshlrev_b32_e64 v80, 16, v172\n"
    "v_and_b32 v81, 0xffff0000, v172\n"
    "v_lshlrev_b32_e64 v82, 16, v173\n"
    "v_and_b32 v83, 0xffff0000, v173\n"
    "v_lshlrev_b32_e64 v84, 16, v174\n"
    "v_and_b32 v85, 0xffff0000, v174\n"
    "v_lshlrev_b32_e64 v86, 16, v175\n"
    "v_and_b32 v87, 0xffff0000, v175\n"
    "buffer_load_dwordx4 v[108:111], v21, s[96:99], s73 offen\n"
    "s_add_u32 s73, s73, s74\n"
    "v_pk_add_f32 v[72:73], v[72:73], v[80:81]\n"
    "v_pk_add_f32 v[74:75], v[74:75], v[82:83]\n"
    "v_pk_add_f32 v[76:77], v[76:77], v[84:85]\n"
    "v_pk_add_f32 v[78:79], v[78:79], v[86:87]\n"
    "s_waitcnt vmcnt(16)\n"
    "v_lshlrev_b32_e64 v80, 16, v176\n"
    "v_and_b32 v81, 0xffff0000, v176\n"
    "v_lshlrev_b32_e64 v82, 16, v177\n"
    "v_and_b32 v83, 0xffff0000, v177\n"
    "v_lshlrev_b32_e64 v84, 16, v178\n"
    "v_and_b32 v85, 0xffff0000, v178\n"
    "v_lshlrev_b32_e64 v86, 16, v179\n"
    "v_and_b32 v87, 0xffff0000, v179\n"
    "buffer_load_dwordx4 v[112:115], v21, s[96:99], s73 offen\n"
    "s_add_u32 s73, s73, s74\n"
    "v_pk_add_f32 v[72:73], v[72:73], v[80:81]\n"
    "v_pk_add_f32 v[74:75], v[74:75], v[82:83]\n"
    "v_pk_add_f32 v[76:77], v[76:77], v[84:85]\n"
    "v_pk_add_f32 v[78:79], v[78:79], v[86:87]\n"
    "s_waitcnt vmcnt(16)\n"
    "v_lshlrev_b32_e64 v80, 16, v180\n"
    "v_and_b32 v81, 0xffff0000, v180\n"
    "v_lshlrev_b32_e64 v82, 16, v181\n"
    "v_and_b32 v83, 0xffff0000, v181\n"
    "v_lshlrev_b32_e64 v84, 16, v182\n"
    "v_and_b32 v85, 0xffff0000, v182\n"
    "v_lshlrev_b32_e64 v86, 16, v183\n"
    "v_and_b32 v87, 0xffff0000, v183\n"
    "buffer_load_dwordx4 v[116:119], v21, s[96:99], s73 offen\n"
    "s_add_u32 s73, s73, s74\n"
    "v_pk_add_f32 v[72:73], v[72:73], v[80:81]\n"
    "v_pk_add_f32 v[74:75], v[74:75], v[82:83]\n"
    "v_pk_add_f32 v[76:77], v[76:77], v[84:85]\n"
    "v_pk_add_f32 v[78:79], v[78:79], v[86:87]\n"
    "s_waitcnt vmcnt(16)\n"
    "v_lshlrev_b32_e64 v80, 16, v184\n"
    "v_and_b32 v81, 0xffff0000, v184\n"
    "v_lshlrev_b32_e64 v82, 16, v185\n"
    "v_and_b32 v83, 0xffff0000, v185\n"
    "v_lshlrev_b32_e64 v84, 16, v186\n"
    "v_and_b32 v85, 0xffff0000, v186\n"
    "v_lshlrev_b32_e64 v86, 16, v187\n"
    "v_and_b32 v87, 0xffff0000, v187\n"
    "buffer_load_dwordx4 v[120:123], v21, s[96:99], s73 offen\n"
    "s_add_u32 s73, s73, s74\n"
    "v_pk_add_f32 v[72:73], v[72:73], v[80:81]\n"
    "v_pk_add_f32 v[74:75], v[74:75], v[82:83]\n"
    "v_pk_add_f32 v[76:77], v[76:77], v[84:85]\n"
    "v_pk_add_f32 v[78:79], v[78:79], v[86:87]\n"
    "s_waitcnt vmcnt(16)\n"
    "v_lshlrev_b32_e64 v80, 16, v188\n"
    "v_and_b32 v81, 0xffff0000, v188\n"
    "v_lshlrev_b32_e64 v82, 16, v189\n"
    "v_and_b32 v83, 0xffff0000, v189\n"
    "v_lshlrev_b32_e64 v84, 16, v190\n"
    "v_and_b32 v85, 0xffff0000, v190\n"
    "v_lshlrev_b32_e64 v86, 16, v191\n"
    "v_and_b32 v87, 0xffff0000, v191\n"
    "buffer_load_dwordx4 v[124:127], v21, s[96:99], s73 offen\n"
    "s_add_u32 s73, s73, s74\n"
    "v_pk_add_f32 v[72:73], v[72:73], v[80:81]\n"
    "v_pk_add_f32 v[74:75], v[74:75], v[82:83]\n"
    "v_pk_add_f32 v[76:77], v[76:77], v[84:85]\n"
    "v_pk_add_f32 v[78:79], v[78:79], v[86:87]\n"
    "s_waitcnt vmcnt(16)\n"
    "v_lshlrev_b32_e64 v80, 16, v192\n"
    "v_and_b32 v81, 0xffff0000, v192\n"
    "v_lshlrev_b32_e64 v82, 16, v193\n"
    "v_and_b32 v83, 0xffff0000, v193\n"
    "v_lshlrev_b32_e64 v84, 16, v194\n"
    "v_and_b32 v85, 0xffff0000, v194\n"
    "v_lshlrev_b32_e64 v86, 16, v195\n"
    "v_and_b32 v87, 0xffff0000, v195\n"
    "buffer_load_dwordx4 v[128:131], v21, s[96:99], s73 offen\n"
    "s_add_u32 s73, s73, s74\n"
    "v_pk_add_f32 v[72:73], v[72:73], v[80:81]\n"
    "v_pk_add_f32 v[74:75], v[74:75], v[82:83]\n"
    "v_pk_add_f32 v[76:77], v[76:77], v[84:85]\n"
    "v_pk_add_f32 v[78:79], v[78:79], v[86:87]\n"
    "s_waitcnt vmcnt(16)\n"
    "v_lshlrev_b32_e64 v80, 16, v196\n"
    "v_and_b32 v81, 0xffff0000, v196\n"
    "v_lshlrev_b32_e64 v82, 16, v197\n"
    "v_and_b32 v83, 0xffff0000, v197\n"
    "v_lshlrev_b32_e64 v84, 16, v198\n"
    "v_and_b32 v85, 0xffff0000, v198\n"
    "v_lshlrev_b32_e64 v86, 16, v199\n"
    "v_and_b32 v87, 0xffff0000, v199\n"
    "buffer_load_dwordx4 v[132:135], v21, s[96:99], s73 offen\n"
    "s_add_u32 s73, s73, s74\n"
    "v_pk_add_f32 v[72:73], v[72:73], v[80:81]\n"
    "v_pk_add_f32 v[74:75], v[74:75], v[82:83]\n"
    "v_pk_add_f32 v[76:77], v[76:77], v[84:85]\n"
    "v_pk_add_f32 v[78:79], v[78:79], v[86:87]\n"
    "s_waitcnt vmcnt(16)\n"
    "v_lshlrev_b32_e64 v80, 16, v200\n"
    "v_and_b32 v81, 0xffff0000, v200\n"
    "v_lshlrev_b32_e64 v82, 16, v201\n"
    "v_and_b32 v83, 0xffff0000, v201\n"
    "v_lshlrev_b32_e64 v84, 16, v202\n"
    "v_and_b32 v85, 0xffff0000, v202\n"
    "v_lshlrev_b32_e64 v86, 16, v203\n"
    "v_and_b32 v87, 0xffff0000, v203\n"
    "buffer_load_dwordx4 v[136:139], v21, s[96:99], s73 offen\n"
    "s_add_u32 s73, s73, s74\n"
    "v_pk_add_f32 v[72:73], v[72:73], v[80:81]\n"
    "v_pk_add_f32 v[74:75], v[74:75], v[82:83]\n"
    "v_pk_add_f32 v[76:77], v[76:77], v[84:85]\n"
    "v_pk_add_f32 v[78:79], v[78:79], v[86:87]\n"
    "s_waitcnt vmcnt(16)\n"
    "v_lshlrev_b32_e64 v80, 16, v204\n"
    "v_and_b32 v81, 0xffff0000, v204\n"
    "v_lshlrev_b32_e64 v82, 16, v205\n"
    "v_and_b32 v83, 0xffff0000, v205\n"
    "v_lshlrev_b32_e64 v84, 16, v206\n"
    "v_and_b32 v85, 0xffff0000, v206\n"
    "v_lshlrev_b32_e64 v86, 16, v207\n"
    "v_and_b32 v87, 0xffff0000, v207\n"
    "buffer_load_dwordx4 v[140:143], v21, s[96:99], s73 offen\n"
    "s_add_u32 s73, s73, s74\n"
    "v_pk_add_f32 v[72:73], v[72:73], v[80:81]\n"
    "v_pk_add_f32 v[74:75], v[74:75], v[82:83]\n"
    "v_pk_add_f32 v[76:77], v[76:77], v[84:85]\n"
    "v_pk_add_f32 v[78:79], v[78:79], v[86:87]\n"
    "s_waitcnt vmcnt(16)\n"
    "v_lshlrev_b32_e64 v80, 16, v208\n"
    "v_and_b32 v81, 0xffff0000, v208\n"
    "v_lshlrev_b32_e64 v82, 16, v209\n"
    "v_and_b32 v83, 0xffff0000, v209\n"
    "v_lshlrev_b32_e64 v84, 16, v210\n"
    "v_and_b32 v85, 0xffff0000, v210\n"
    "v_lshlrev_b32_e64 v86, 16, v211\n"
    "v_and_b32 v87, 0xffff0000, v211\n"
    "buffer_load_dwordx4 v[144:147], v21, s[96:99], s73 offen\n"
    "s_add_u32 s73, s73, s74\n"
    "v_pk_add_f32 v[72:73], v[72:73], v[80:81]\n"
    "v_pk_add_f32 v[74:75], v[74:75], v[82:83]\n"
    "v_pk_add_f32 v[76:77], v[76:77], v[84:85]\n"
    "v_pk_add_f32 v[78:79], v[78:79], v[86:87]\n"
    "s_waitcnt vmcnt(16)\n"
    "v_lshlrev_b32_e64 v80, 16, v212\n"
    "v_and_b32 v81, 0xffff0000, v212\n"
    "v_lshlrev_b32_e64 v82, 16, v213\n"
    "v_and_b32 v83, 0xffff0000, v213\n"
    "v_lshlrev_b32_e64 v84, 16, v214\n"
    "v_and_b32 v85, 0xffff0000, v214\n"
    "v_lshlrev_b32_e64 v86, 16, v215\n"
    "v_and_b32 v87, 0xffff0000, v215\n"
    "buffer_load_dwordx4 v[148:151], v21, s[96:99], s73 offen\n"
    "s_add_u32 s73, s73, s74\n"
    "v_pk_add_f32 v[72:73], v[72:73], v[80:81]\n"
    "v_pk_add_f32 v[74:75], v[74:75], v[82:83]\n"
    "v_pk_add_f32 v[76:77], v[76:77], v[84:85]\n"
    "v_pk_add_f32 v[78:79], v[78:79], v[86:87]\n"
    "s_waitcnt vmcnt(16)\n"
    "v_lshlrev_b32_e64 v80, 16, v216\n"
    "v_and_b32 v81, 0xffff0000, v216\n"
    "v_lshlrev_b32_e64 v82, 16, v217\n"
    "v_and_b32 v83, 0xffff0000, v217\n"
    "v_lshlrev_b32_e64 v84, 16, v218\n"
    "v_and_b32 v85, 0xffff0000, v218\n"
    "v_lshlrev_b32_e64 v86, 16, v219\n"
    "v_and_b32 v87, 0xffff0000, v219\n"
    "buffer_load_dwordx4 v[152:155], v21, s[96:99], s73 offen\n"
    "s_add_u32 s73, s73, s74\n"
    "v_pk_add_f32 v[72:73], v[72:73], v[80:81]\n"
    "v_pk_add_f32 v[74:75], v[74:75], v[82:83]\n"
    "v_pk_add_f32 v[76:77], v[76:77], v[84:85]\n"
    "v_pk_add_f32 v[78:79], v[78:79], v[86:87]\n"
    "s_waitcnt vmcnt(16)\n"
    "v_lshlrev_b32_e64 v80, 16, v220\n"
    "v_and_b32 v81, 0xffff0000, v220\n"
    "v_lshlrev_b32_e64 v82, 16, v221\n"
    "v_and_b32 v83, 0xffff0000, v221\n"
    "v_lshlrev_b32_e64 v84, 16, v222\n"
    "v_and_b32 v85, 0xffff0000, v222\n"
    "v_lshlrev_b32_e64 v86, 16, v223\n"
    "v_and_b32 v87, 0xffff0000, v223\n"
    "buffer_load_dwordx4 v[156:159], v21, s[96:99], s73 offen\n"
    "s_add_u32 s73, s73, s74\n"
    "s_nop 0\n"
    "v_pk_add_f32 v[72:73], v[72:73], v[80:81]\n"
    "v_pk_add_f32 v[74:75], v[74:75], v[82:83]\n"
    "v_pk_add_f32 v[76:77], v[76:77], v[84:85]\n"
    "v_pk_add_f32 v[78:79], v[78:79], v[86:87]\n"
    "v_add_f32_dpp v72, v72, v72 row_ror:8 row_mask:0xf bank_mask:0xf\n"
    "v_add_f32_dpp v73, v73, v73 row_ror:8 row_mask:0xf bank_mask:0xf\n"
    "v_add_f32_dpp v74, v74, v74 row_ror:8 row_mask:0xf bank_mask:0xf\n"
    "v_add_f32_dpp v75, v75, v75 row_ror:8 row_mask:0xf bank_mask:0xf\n"
    "v_add_f32_dpp v76, v76, v76 row_ror:8 row_mask:0xf bank_mask:0xf\n"
    "v_add_f32_dpp v77, v77, v77 row_ror:8 row_mask:0xf bank_mask:0xf\n"
    "v_add_f32_dpp v78, v78, v78 row_ror:8 row_mask:0xf bank_mask:0xf\n"
    "v_add_f32_dpp v79, v79, v79 row_ror:8 row_mask:0xf bank_mask:0xf\n"
    "v_permlane16_swap_b32 v72, v73\n"
    "v_permlane16_swap_b32 v74, v75\n"
    "v_permlane16_swap_b32 v76, v77\n"
    "v_permlane16_swap_b32 v78, v79\n"
    "v_add_f32 v72, v72, v73\n"
    "v_add_f32 v74, v74, v75\n"
    "v_add_f32 v76, v76, v77\n"
    "v_add_f32 v78, v78, v79\n"
    "v_permlane32_swap_b32 v72, v74\n"
    "s_nop 0\n"
    "v_permlane32_swap_b32 v76, v78\n"
    "v_add_f32 v72, v72, v74\n"
    "v_add_f32_e64 v76, v76, v78\n"
    "ds_write2_b32 v23, v72, v76 offset1:4\n"
    "v_add_u32 v23, 1024, v23\n"
    "s_waitcnt lgkmcnt(0)\n"
    "s_barrier\n"
    "ds_read2st64_b32 v[72:73], v88 offset0:12 offset1:13\n"
    "ds_read2st64_b32 v[74:75], v88 offset0:14 offset1:15\n"
    "s_waitcnt lgkmcnt(0)\n"
    "v_add_f32 v72, v72, v73\n"
    "v_add_f32 v74, v74, v75\n"
    "v_mov_b32 v73, s46\n"
    "v_add_f32_e64 v72, v72, v74\n"
    "buffer_store_dwordx2 v[72:73], v12, s[8:11], s81 offen sc1\n"
    ST(3)
    "s_add_u32 s67, s59, 48\n"
    "s_add_u32 s70, s67, 38\n"
    "s_sub_u32 s71, s67, 14\n"
    "s_cselect_b32 s70, s70, s71\n"
    "s_cmp_lt_u32 s70, 24\n"
    "s_cselect_b32 s96, s12, s16\n"
    "s_cselect_b32 s97, s13, s17\n"
    "s_cselect_b32 s98, s14, s18\n"
    "s_cselect_b32 s71, 0, 512\n"
    "s_cselect_b32 s73, 0, 24\n"
    "s_add_u32 s71, s71, 3072\n"
    "s_sub_u32 s73, s70, s73\n"
    "s_lshl_b32 s73, s73, 7\n"
    "s_mul_i32 s74, s45, s71\n"
    "s_lshl_b32 s74, s74, 7\n"
    "s_add_u32 s73, s73, s74\n"
    "v_mad_u32_u24 v21, v16, s71, v22\n"
    "s_lshl_b32 s74, s71, 3\n"
    "s_lshl_b32 s70, s70, 9\n"
    "s_add_u32 s70, s70, 364544\n"
    "s_cmp_lt_u32 s67, 52\n"
    "s_cselect_b32 s83, s70, 2147483648\n"
    "s_cselect_b32 s73, s73, 2147483648\n"
    "s_waitcnt vmcnt(16)\n"
    "v_lshlrev_b32_e64 v72, 16, v96\n"
    "v_and_b32 v73, 0xffff0000, v96\n"
    "v_lshlrev_b32_e64 v74, 16, v97\n"
    "v_and_b32 v75, 0xffff0000, v97\n"
    "v_lshlrev_b32_e64 v76, 16, v98\n"
    "v_and_b32 v77, 0xffff0000, v98\n"
    "v_lshlrev_b32_e64 v78, 16, v99\n"
    "v_and_b32 v79, 0xffff0000, v99\n"
    "buffer_load_dwordx4 v[160:163], v21, s[96:99], s73 offen\n"
    "s_add_u32 s73, s73, s74\n"
    "s_waitcnt vmcnt(16)\n"
    "v_lshlrev_b32_e64 v80, 16, v100\n"
    "v_and_b32 v81, 0xffff0000, v100\n"
    "v_lshlrev_b32_e64 v82, 16, v101\n"
    "v_and_b32 v83, 0xffff0000, v101\n"
    "v_lshlrev_b32_e64 v84, 16, v102\n"
    "v_and_b32 v85, 0xffff0000, v102\n"
    "v_lshlrev_b32_e64 v86, 16, v103\n"
    "v_and_b32 v87, 0xffff0000, v103\n"
    "buffer_load_dwordx4 v[164:167], v21, s[96:99], s73 offen\n"
    "s_add_u32 s73, s73, s74\n"
    "v_pk_add_f32 v[72:73], v[72:73], v[80:81]\n"
    "v_pk_add_f32 v[74:75], v[74:75], v[82:83]\n"
    "v_pk_add_f32 v[76:77], v[76:77], v[84:85]\n"
    "v_pk_add_f32 v[78:79], v[78:79], v[86:87]\n"
    "s_waitcnt vmcnt(16)\n"
    "v_lshlrev_b32_e64 v80, 16, v104\n"
    "v_and_b32 v81, 0xffff0000, v104\n"
    "v_lshlrev_b32_e64 v82, 16, v105\n"
    "v_and_b32 v83, 0xffff0000, v105\n"
    "v_lshlrev_b32_e64 v84, 16, v106\n"
    "v_and_b32 v85, 0xffff0000, v106\n"
    "v_lshlrev_b32_e64 v86, 16, v107\n"
    "v_and_b32 v87, 0xffff0000, v107\n"
    "buffer_load_dwordx4 v[168:171], v21, s[96:99], s73 offen\n"
    "s_add_u32 s73, s73, s74\n"
    "v_pk_add_f32 v[72:73], v[72:73], v[80:81]\n"
    "v_pk_add_f32 v[74:75], v[74:75], v[82:83]\n"
    "v_pk_add_f32 v[76:77], v[76:77], v[84:85]\n"
    "v_pk_add_f32 v[78:79], v[78:79], v[86:87]\n"
    "s_waitcnt vmcnt(16)\n"
    "v_lshlrev_b32_e64 v80, 16, v108\n"
    "v_and_b32 v81, 0xffff0000, v108\n"
    "v_lshlrev_b32_e64 v82, 16, v109\n"
    "v_and_b32 v83, 0xffff0000, v109\n"
    "v_lshlrev_b32_e64 v84, 16, v110\n"
    "v_and_b32 v85, 0xffff0000, v110\n"
    "v_lshlrev_b32_e64 v86, 16, v111\n"
    "v_and_b32 v87, 0xffff0000, v111\n"
    "buffer_load_dwordx4 v[172:175], v21, s[96:99], s73 offen\n"
    "s_add_u32 s73, s73, s74\n"
    "v_pk_add_f32 v[72:73], v[72:73], v[80:81]\n"
    "v_pk_add_f32 v[74:75], v[74:75], v[82:83]\n"
    "v_pk_add_f32 v[76:77], v[76:77], v[84:85]\n"
    "v_pk_add_f32 v[78:79], v[78:79], v[86:87]\n"
    "s_waitcnt vmcnt(16)\n"
    "v_lshlrev_b32_e64 v80, 16, v112\n"
    "v_and_b32 v81, 0xffff0000, v112\n"
    "v_lshlrev_b32_e64 v82, 16, v113\n"
    "v_and_b32 v83, 0xffff0000, v113\n"
    "v_lshlrev_b32_e64 v84, 16, v114\n"
    "v_and_b32 v85, 0xffff0000, v114\n"
    "v_lshlrev_b32_e64 v86, 16, v115\n"
    "v_and_b32 v87, 0xffff0000, v115\n"
    "buffer_load_dwordx4 v[176:179], v21, s[96:99], s73 offen\n"
    "s_add_u32 s73, s73, s74\n"
    "v_pk_add_f32 v[72:73], v[72:73], v[80:81]\n"
    "v_pk_add_f32 v[74:75], v[74:75], v[82:83]\n"
    "v_pk_add_f32 v[76:77], v[76:77], v[84:85]\n"
    "v_pk_add_f32 v[78:79], v[78:79], v[86:87]\n"
    "s_waitcnt vmcnt(16)\n"
    "v_lshlrev_b32_e64 v80, 16, v116\n"
    "v_and_b32 v81, 0xffff0000, v116\n"
    "v_lshlrev_b32_e64 v82, 16, v117\n"
    "v_and_b32 v83, 0xffff0000, v117\n"
    "v_lshlrev_b32_e64 v84, 16, v118\n"
    "v_and_b32 v85, 0xffff0000, v118\n"
    "v_lshlrev_b32_e64 v86, 16, v119\n"
    "v_and_b32 v87, 0xffff0000, v119\n"
    "buffer_load_dwordx4 v[180:183], v21, s[96:99], s73 offen\n"
    "s_add_u32 s73, s73, s74\n"
    "v_pk_add_f32 v[72:73], v[72:73], v[80:81]\n"
    "v_pk_add_f32 v[74:75], v[74:75], v[82:83]\n"
    "v_pk_add_f32 v[76:77], v[76:77], v[84:85]\n"
    "v_pk_add_f32 v[78:79], v[78:79], v[86:87]\n"
    "s_waitcnt vmcnt(16)\n"
    "v_lshlrev_b32_e64 v80, 16, v120\n"
    "v_and_b32 v81, 0xffff0000, v120\n"
    "v_lshlrev_b32_e64 v82, 16, v121\n"
    "v_and_b32 v83, 0xffff0000, v121\n"
    "v_lshlrev_b32_e64 v84, 16, v122\n"
    "v_and_b32 v85, 0xffff0000, v122\n"
    "v_lshlrev_b32_e64 v86, 16, v123\n"
    "v_and_b32 v87, 0xffff0000, v123\n"
    "buffer_load_dwordx4 v[184:187], v21, s[96:99], s73 offen\n"
    "s_add_u32 s73, s73, s74\n"
    "v_pk_add_f32 v[72:73], v[72:73], v[80:81]\n"
    "v_pk_add_f32 v[74:75], v[74:75], v[82:83]\n"
    "v_pk_add_f32 v[76:77], v[76:77], v[84:85]\n"
    "v_pk_add_f32 v[78:79], v[78:79], v[86:87]\n"
    "s_waitcnt vmcnt(16)\n"
    "v_lshlrev_b32_e64 v80, 16, v124\n"
    "v_and_b32 v81, 0xffff0000, v124\n"
    "v_lshlrev_b32_e64 v82, 16, v125\n"
    "v_and_b32 v83, 0xffff0000, v125\n"
    "v_lshlrev_b32_e64 v84, 16, v126\n"
    "v_and_b32 v85, 0xffff0000, v126\n"
    "v_lshlrev_b32_e64 v86, 16, v127\n"
    "v_and_b32 v87, 0xffff0000, v127\n"
    "buffer_load_dwordx4 v[188:191], v21, s[96:99], s73 offen\n"
    "s_add_u32 s73, s73, s74\n"
    "v_pk_add_f32 v[72:73], v[72:73], v[80:81]\n"
    "v_pk_add_f32 v[74:75], v[74:75], v[82:83]\n"
    "v_pk_add_f32 v[76:77], v[76:77], v[84:85]\n"
    "v_pk_add_f32 v[78:79], v[78:79], v[86:87]\n"
    "s_waitcnt vmcnt(16)\n"
    "v_lshlrev_b32_e64 v80, 16, v128\n"
    "v_and_b32 v81, 0xffff0000, v128\n"
    "v_lshlrev_b32_e64 v82, 16, v129\n"
    "v_and_b32 v83, 0xffff0000, v129\n"
    "v_lshlrev_b32_e64 v84, 16, v130\n"
    "v_and_b32 v85, 0xffff0000, v130\n"
    "v_lshlrev_b32_e64 v86, 16, v131\n"
    "v_and_b32 v87, 0xffff0000, v131\n"
    "buffer_load_dwordx4 v[192:195], v21, s[96:99], s73 offen\n"
    "s_add_u32 s73, s73, s74\n"
    "v_pk_add_f32 v[72:73], v[72:73], v[80:81]\n"
    "v_pk_add_f32 v[74:75], v[74:75], v[82:83]\n"
    "v_pk_add_f32 v[76:77], v[76:77], v[84:85]\n"
    "v_pk_add_f32 v[78:79], v[78:79], v[86:87]\n"
    "s_waitcnt vmcnt(16)\n"
    "v_lshlrev_b32_e64 v80, 16, v132\n"
    "v_and_b32 v81, 0xffff0000, v132\n"
    "v_lshlrev_b32_e64 v82, 16, v133\n"
    "v_and_b32 v83, 0xffff0000, v133\n"
    "v_lshlrev_b32_e64 v84, 16, v134\n"
    "v_and_b32 v85, 0xffff0000, v134\n"
    "v_lshlrev_b32_e64 v86, 16, v135\n"
    "v_and_b32 v87, 0xffff0000, v135\n"
    "buffer_load_dwordx4 v[196:199], v21, s[96:99], s73 offen\n"
    "s_add_u32 s73, s73, s74\n"
    "v_pk_add_f32 v[72:73], v[72:73], v[80:81]\n"
    "v_pk_add_f32 v[74:75], v[74:75], v[82:83]\n"
    "v_pk_add_f32 v[76:77], v[76:77], v[84:85]\n"
    "v_pk_add_f32 v[78:79], v[78:79], v[86:87]\n"
    "s_waitcnt vmcnt(16)\n"
    "v_lshlrev_b32_e64 v80, 16, v136\n"
    "v_and_b32 v81, 0xffff0000, v136\n"
    "v_lshlrev_b32_e64 v82, 16, v137\n"
    "v_and_b32 v83, 0xffff0000, v137\n"
    "v_lshlrev_b32_e64 v84, 16, v138\n"
    "v_and_b32 v85, 0xffff0000, v138\n"
    "v_lshlrev_b32_e64 v86, 16, v139\n"
    "v_and_b32 v87, 0xffff0000, v139\n"
    "buffer_load_dwordx4 v[200:203], v21, s[96:99], s73 offen\n"
    "s_add_u32 s73, s73, s74\n"
    "v_pk_add_f32 v[72:73], v[72:73], v[80:81]\n"
    "v_pk_add_f32 v[74:75], v[74:75], v[82:83]\n"
    "v_pk_add_f32 v[76:77], v[76:77], v[84:85]\n"
    "v_pk_add_f32 v[78:79], v[78:79], v[86:87]\n"
    "s_waitcnt vmcnt(16)\n"
    "v_lshlrev_b32_e64 v80, 16, v140\n"
    "v_and_b32 v81, 0xffff0000, v140\n"
    "v_lshlrev_b32_e64 v82, 16, v141\n"
    "v_and_b32 v83, 0xffff0000, v141\n"
    "v_lshlrev_b32_e64 v84, 16, v142\n"
    "v_and_b32 v85, 0xffff0000, v142\n"
    "v_lshlrev_b32_e64 v86, 16, v143\n"
    "v_and_b32 v87, 0xffff0000, v143\n"
    "buffer_load_dwordx4 v[204:207], v21, s[96:99], s73 offen\n"
    "s_add_u32 s73, s73, s74\n"
    "v_pk_add_f32 v[72:73], v[72:73], v[80:81]\n"
    "v_pk_add_f32 v[74:75], v[74:75], v[82:83]\n"
    "v_pk_add_f32 v[76:77], v[76:77], v[84:85]\n"
    "v_pk_add_f32 v[78:79], v[78:79], v[86:87]\n"
    "s_waitcnt vmcnt(16)\n"
    "v_lshlrev_b32_e64 v80, 16, v144\n"
    "v_and_b32 v81, 0xffff0000, v144\n"
    "v_lshlrev_b32_e64 v82, 16, v145\n"
    "v_and_b32 v83, 0xffff0000, v145\n"
    "v_lshlrev_b32_e64 v84, 16, v146\n"
    "v_and_b32 v85, 0xffff0000, v146\n"
    "v_lshlrev_b32_e64 v86, 16, v147\n"
    "v_and_b32 v87, 0xffff0000, v147\n"
    "buffer_load_dwordx4 v[208:211], v21, s[96:99], s73 offen\n"
    "s_add_u32 s73, s73, s74\n"
    "v_pk_add_f32 v[72:73], v[72:73], v[80:81]\n"
    "v_pk_add_f32 v[74:75], v[74:75], v[82:83]\n"
    "v_pk_add_f32 v[76:77], v[76:77], v[84:85]\n"
    "v_pk_add_f32 v[78:79], v[78:79], v[86:87]\n"
    "s_waitcnt vmcnt(16)\n"
    "v_lshlrev_b32_e64 v80, 16, v148\n"
    "v_and_b32 v81, 0xffff0000, v148\n"
    "v_lshlrev_b32_e64 v82, 16, v149\n"
    "v_and_b32 v83, 0xffff0000, v149\n"
    "v_lshlrev_b32_e64 v84, 16, v150\n"
    "v_and_b32 v85, 0xffff0000, v150\n"
    "v_lshlrev_b32_e64 v86, 16, v151\n"
    "v_and_b32 v87, 0xffff0000, v151\n"
    "buffer_load_dwordx4 v[212:215], v21, s[96:99], s73 offen\n"
    "s_add_u32 s73, s73, s74\n"
    "v_pk_add_f32 v[72:73], v[72:73], v[80:81]\n"
    "v_pk_add_f32 v[74:75], v[74:75], v[82:83]\n"
    "v_pk_add_f32 v[76:77], v[76:77], v[84:85]\n"
    "v_pk_add_f32 v[78:79], v[78:79], v[86:87]\n"
    "s_waitcnt vmcnt(16)\n"
    "v_lshlrev_b32_e64 v80, 16, v152\n"
    "v_and_b32 v81, 0xffff0000, v152\n"
    "v_lshlrev_b32_e64 v82, 16, v153\n"
    "v_and_b32 v83, 0xffff0000, v153\n"
    "v_lshlrev_b32_e64 v84, 16, v154\n"
    "v_and_b32 v85, 0xffff0000, v154\n"
    "v_lshlrev_b32_e64 v86, 16, v155\n"
    "v_and_b32 v87, 0xffff0000, v155\n"
    "buffer_load_dwordx4 v[216:219], v21, s[96:99], s73 offen\n"
    "s_add_u32 s73, s73, s74\n"
    "v_pk_add_f32 v[72:73], v[72:73], v[80:81]\n"
    "v_pk_add_f32 v[74:75], v[74:75], v[82:83]\n"
    "v_pk_add_f32 v[76:77], v[76:77], v[84:85]\n"
    "v_pk_add_f32 v[78:79], v[78:79], v[86:87]\n"
    "s_waitcnt vmcnt(16)\n"
    "v_lshlrev_b32_e64 v80, 16, v156\n"
    "v_and_b32 v81, 0xffff0000, v156\n"
    "v_lshlrev_b32_e64 v82, 16, v157\n"
    "v_and_b32 v83, 0xffff0000, v157\n"
    "v_lshlrev_b32_e64 v84, 16, v158\n"
    "v_and_b32 v85, 0xffff0000, v158\n"
    "v_lshlrev_b32_e64 v86, 16, v159\n"
    "v_and_b32 v87, 0xffff0000, v159\n"
    "buffer_load_dwordx4 v[220:223], v21, s[96:99], s73 offen\n"
    "s_add_u32 s73, s73, s74\n"
    "s_nop 0\n"
    "v_pk_add_f32 v[72:73], v[72:73], v[80:81]\n"
    "v_pk_add_f32 v[74:75], v[74:75], v[82:83]\n"
    "v_pk_add_f32 v[76:77], v[76:77], v[84:85]\n"
    "v_pk_add_f32 v[78:79], v[78:79], v[86:87]\n"
    "v_add_f32_dpp v72, v72, v72 row_ror:8 row_mask:0xf bank_mask:0xf\n"
    "v_add_f32_dpp v73, v73, v73 row_ror:8 row_mask:0xf bank_mask:0xf\n"
    "v_add_f32_dpp v74, v74, v74 row_ror:8 row_mask:0xf bank_mask:0xf\n"
    "v_add_f32_dpp v75, v75, v75 row_ror:8 row_mask:0xf bank_mask:0xf\n"
    "v_add_f32_dpp v76, v76, v76 row_ror:8 row_mask:0xf bank_mask:0xf\n"
    "v_add_f32_dpp v77, v77, v77 row_ror:8 row_mask:0xf bank_mask:0xf\n"
    "v_add_f32_dpp v78, v78, v78 row_ror:8 row_mask:0xf bank_mask:0xf\n"
    "v_add_f32_dpp v79, v79, v79 row_ror:8 row_mask:0xf bank_mask:0xf\n"
    "v_permlane16_swap_b32 v72, v73\n"
    "v_permlane16_swap_b32 v74, v75\n"
    "v_permlane16_swap_b32 v76, v77\n"
    "v_permlane16_swap_b32 v78, v79\n"
    "v_add_f32 v72, v72, v73\n"
    "v_add_f32 v74, v74, v75\n"
    "v_add_f32 v76, v76, v77\n"
    "v_add_f32 v78, v78, v79\n"
    "v_permlane32_swap_b32 v72, v74\n"
    "s_nop 0\n"
    "v_permlane32_swap_b32 v76, v78\n"
    "v_add_f32 v72, v72, v74\n"
    "v_add_f32_e64 v76, v76, v78\n"
    "ds_write2_b32 v23, v72, v76 offset1:4\n"
    "v_add_u32 v23, 1024, v23\n"
    "s_waitcnt lgkmcnt(0)\n"
    "s_barrier\n"
    "ds_read2st64_b32 v[72:73], v88 offset0:16 offset1:17\n"
    "ds_read2st64_b32 v[74:75], v88 offset0:18 offset1:19\n"
    "s_waitcnt lgkmcnt(0)\n"
    "v_add_f32 v72, v72, v73\n"
    "v_add_f32 v74, v74, v75\n"
    "v_mov_b32 v73, s46\n"
    "v_add_f32_e64 v72, v72, v74\n"
    "buffer_store_dwordx2 v[72:73], v12, s[8:11], s82 offen sc1\n"
    ST(4)
    "s_waitcnt vmcnt(16)\n"
    "v_lshlrev_b32 v72, 16, v160\n"
    "v_and_b32 v73, 0xffff0000, v160\n"
    "v_lshlrev_b32_e64 v74, 16, v161\n"
    "v_and_b32 v75, 0xffff0000, v161\n"
    "v_lshlrev_b32_e64 v76, 16, v162\n"
    "v_and_b32 v77, 0xffff0000, v162\n"
    "v_lshlrev_b32_e64 v78, 16, v163\n"
    "v_and_b32 v79, 0xffff0000, v163\n"
    "s_waitcnt vmcnt(15)\n"
    "v_lshlrev_b32 v80, 16, v164\n"
    "v_and_b32 v81, 0xffff0000, v164\n"
    "v_lshlrev_b32_e64 v82, 16, v165\n"
    "v_and_b32 v83, 0xffff0000, v165\n"
    "v_lshlrev_b32_e64 v84, 16, v166\n"
    "v_and_b32 v85, 0xffff0000, v166\n"
    "v_lshlrev_b32_e64 v86, 16, v167\n"
    "v_and_b32 v87, 0xffff0000, v167\n"
    "v_pk_add_f32 v[72:73], v[72:73], v[80:81]\n"
    "v_pk_add_f32 v[74:75], v[74:75], v[82:83]\n"
    "v_pk_add_f32 v[76:77], v[76:77], v[84:85]\n"
    "v_pk_add_f32 v[78:79], v[78:79], v[86:87]\n"
    "s_waitcnt vmcnt(14)\n"
    "v_lshlrev_b32 v80, 16, v168\n"
    "v_and_b32 v81, 0xffff0000, v168\n"
    "v_lshlrev_b32_e64 v82, 16, v169\n"
    "v_and_b32 v83, 0xffff0000, v169\n"
    "v_lshlrev_b32_e64 v84, 16, v170\n"
    "v_and_b32 v85, 0xffff0000, v170\n"
    "v_lshlrev_b32_e64 v86, 16, v171\n"
    "v_and_b32 v87, 0xffff0000, v171\n"
    "v_pk_add_f32 v[72:73], v[72:73], v[80:81]\n"
    "v_pk_add_f32 v[74:75], v[74:75], v[82:83]\n"
    "v_pk_add_f32 v[76:77], v[76:77], v[84:85]\n"
    "v_pk_add_f32 v[78:79], v[78:79], v[86:87]\n"
    "s_waitcnt vmcnt(13)\n"
    "v_lshlrev_b32 v80, 16, v172\n"
    "v_and_b32 v81, 0xffff0000, v172\n"
    "v_lshlrev_b32_e64 v82, 16, v173\n"
    "v_and_b32 v83, 0xffff0000, v173\n"
    "v_lshlrev_b32_e64 v84, 16, v174\n"
    "v_and_b32 v85, 0xffff0000, v174\n"
    "v_lshlrev_b32_e64 v86, 16, v175\n"
    "v_and_b32 v87, 0xffff0000, v175\n"
    "v_pk_add_f32 v[72:73], v[72:73], v[80:81]\n"
    "v_pk_add_f32 v[74:75], v[74:75], v[82:83]\n"
    "v_pk_add_f32 v[76:77], v[76:77], v[84:85]\n"
    "v_pk_add_f32 v[78:79], v[78:79], v[86:87]\n"
    "s_waitcnt vmcnt(12)\n"
    "v_lshlrev_b32 v80, 16, v176\n"
    "v_and_b32 v81, 0xffff0000, v176\n"
    "v_lshlrev_b32_e64 v82, 16, v177\n"
    "v_and_b32 v83, 0xffff0000, v177\n"
    "v_lshlrev_b32_e64 v84, 16, v178\n"
    "v_and_b32 v85, 0xffff0000, v178\n"
    "v_lshlrev_b32_e64 v86, 16, v179\n"
    "v_and_b32 v87, 0xffff0000, v179\n"
    "v_pk_add_f32 v[72:73], v[72:73], v[80:81]\n"
    "v_pk_add_f32 v[74:75], v[74:75], v[82:83]\n"
    "v_pk_add_f32 v[76:77], v[76:77], v[84:85]\n"
    "v_pk_add_f32 v[78:79], v[78:79], v[86:87]\n"
    "s_waitcnt vmcnt(11)\n"
    "v_lshlrev_b32 v80, 16, v180\n"
    "v_and_b32 v81, 0xffff0000, v180\n"
    "v_lshlrev_b32_e64 v82, 16, v181\n"
    "v_and_b32 v83, 0xffff0000, v181\n"
    "v_lshlrev_b32_e64 v84, 16, v182\n"
    "v_and_b32 v85, 0xffff0000, v182\n"
    "v_lshlrev_b32_e64 v86, 16, v183\n"
    "v_and_b32 v87, 0xffff0000, v183\n"
    "v_pk_add_f32 v[72:73], v[72:73], v[80:81]\n"
    "v_pk_add_f32 v[74:75], v[74:75], v[82:83]\n"
    "v_pk_add_f32 v[76:77], v[76:77], v[84:85]\n"
    "v_pk_add_f32 v[78:79], v[78:79], v[86:87]\n"
    "s_waitcnt vmcnt(10)\n"
    "v_lshlrev_b32 v80, 16, v184\n"
    "v_and_b32 v81, 0xffff0000, v184\n"
    "v_lshlrev_b32_e64 v82, 16, v185\n"
    "v_and_b32 v83, 0xffff0000, v185\n"
    "v_lshlrev_b32_e64 v84, 16, v186\n"
    "v_and_b32 v85, 0xffff0000, v186\n"
    "v_lshlrev_b32_e64 v86, 16, v187\n"
    "v_and_b32 v87, 0xffff0000, v187\n"
    "v_pk_add_f32 v[72:73], v[72:73], v[80:81]\n"
    "v_pk_add_f32 v[74:75], v[74:75], v[82:83]\n"
    "v_pk_add_f32 v[76:77], v[76:77], v[84:85]\n"
    "v_pk_add_f32 v[78:79], v[78:79], v[86:87]\n"
    "s_waitcnt vmcnt(9)\n"
    "v_lshlrev_b32 v80, 16, v188\n"
    "v_and_b32 v81, 0xffff0000, v188\n"
    "v_lshlrev_b32_e64 v82, 16, v189\n"
    "v_and_b32 v83, 0xffff0000, v189\n"
    "v_lshlrev_b32_e64 v84, 16, v190\n"
    "v_and_b32 v85, 0xffff0000, v190\n"
    "v_lshlrev_b32_e64 v86, 16, v191\n"
    "v_and_b32 v87, 0xffff0000, v191\n"
    "v_pk_add_f32 v[72:73], v[72:73], v[80:81]\n"
    "v_pk_add_f32 v[74:75], v[74:75], v[82:83]\n"
    "v_pk_add_f32 v[76:77], v[76:77], v[84:85]\n"
    "v_pk_add_f32 v[78:79], v[78:79], v[86:87]\n"
    "s_waitcnt vmcnt(8)\n"
    "v_lshlrev_b32 v80, 16, v192\n"
    "v_and_b32 v81, 0xffff0000, v192\n"
    "v_lshlrev_b32_e64 v82, 16, v193\n"
    "v_and_b32 v83, 0xffff0000, v193\n"
    "v_lshlrev_b32_e64 v84, 16, v194\n"
    "v_and_b32 v85, 0xffff0000, v194\n"
    "v_lshlrev_b32_e64 v86, 16, v195\n"
    "v_and_b32 v87, 0xffff0000, v195\n"
    "v_pk_add_f32 v[72:73], v[72:73], v[80:81]\n"
    "v_pk_add_f32 v[74:75], v[74:75], v[82:83]\n"
    "v_pk_add_f32 v[76:77], v[76:77], v[84:85]\n"
    "v_pk_add_f32 v[78:79], v[78:79], v[86:87]\n"
    "s_waitcnt vmcnt(7)\n"
    "v_lshlrev_b32 v80, 16, v196\n"
    "v_and_b32 v81, 0xffff0000, v196\n"
    "v_lshlrev_b32_e64 v82, 16, v197\n"
    "v_and_b32 v83, 0xffff0000, v197\n"
    "v_lshlrev_b32_e64 v84, 16, v198\n"
    "v_and_b32 v85, 0xffff0000, v198\n"
    "v_lshlrev_b32_e64 v86, 16, v199\n"
    "v_and_b32 v87, 0xffff0000, v199\n"
    "v_pk_add_f32 v[72:73], v[72:73], v[80:81]\n"
    "v_pk_add_f32 v[74:75], v[74:75], v[82:83]\n"
    "v_pk_add_f32 v[76:77], v[76:77], v[84:85]\n"
    "v_pk_add_f32 v[78:79], v[78:79], v[86:87]\n"
    "s_waitcnt vmcnt(6)\n"
    "v_lshlrev_b32 v80, 16, v200\n"
    "v_and_b32 v81, 0xffff0000, v200\n"
    "v_lshlrev_b32_e64 v82, 16, v201\n"
    "v_and_b32 v83, 0xffff0000, v201\n"
    "v_lshlrev_b32_e64 v84, 16, v202\n"
    "v_and_b32 v85, 0xffff0000, v202\n"
    "v_lshlrev_b32_e64 v86, 16, v203\n"
    "v_and_b32 v87, 0xffff0000, v203\n"
    "v_pk_add_f32 v[72:73], v[72:73], v[80:81]\n"
    "v_pk_add_f32 v[74:75], v[74:75], v[82:83]\n"
    "v_pk_add_f32 v[76:77], v[76:77], v[84:85]\n"
    "v_pk_add_f32 v[78:79], v[78:79], v[86:87]\n"
    "s_waitcnt vmcnt(5)\n"
    "v_lshlrev_b32 v80, 16, v204\n"
    "v_and_b32 v81, 0xffff0000, v204\n"
    "v_lshlrev_b32_e64 v82, 16, v205\n"
    "v_and_b32 v83, 0xffff0000, v205\n"
    "v_lshlrev_b32_e64 v84, 16, v206\n"
    "v_and_b32 v85, 0xffff0000, v206\n"
    "v_lshlrev_b32_e64 v86, 16, v207\n"
    "v_and_b32 v87, 0xffff0000, v207\n"
    "v_pk_add_f32 v[72:73], v[72:73], v[80:81]\n"
    "v_pk_add_f32 v[74:75], v[74:75], v[82:83]\n"
    "v_pk_add_f32 v[76:77], v[76:77], v[84:85]\n"
    "v_pk_add_f32 v[78:79], v[78:79], v[86:87]\n"
    "s_waitcnt vmcnt(4)\n"
    "v_lshlrev_b32 v80, 16, v208\n"
    "v_and_b32 v81, 0xffff0000, v208\n"
    "v_lshlrev_b32_e64 v82, 16, v209\n"
    "v_and_b32 v83, 0xffff0000, v209\n"
    "v_lshlrev_b32_e64 v84, 16, v210\n"
    "v_and_b32 v85, 0xffff0000, v210\n"
    "v_lshlrev_b32_e64 v86, 16, v211\n"
    "v_and_b32 v87, 0xffff0000, v211\n"
    "v_pk_add_f32 v[72:73], v[72:73], v[80:81]\n"
    "v_pk_add_f32 v[74:75], v[74:75], v[82:83]\n"
    "v_pk_add_f32 v[76:77], v[76:77], v[84:85]\n"
    "v_pk_add_f32 v[78:79], v[78:79], v[86:87]\n"
    "s_waitcnt vmcnt(3)\n"
    "v_lshlrev_b32 v80, 16, v212\n"
    "v_and_b32 v81, 0xffff0000, v212\n"
    "v_lshlrev_b32_e64 v82, 16, v213\n"
    "v_and_b32 v83, 0xffff0000, v213\n"
    "v_lshlrev_b32_e64 v84, 16, v214\n"
    "v_and_b32 v85, 0xffff0000, v214\n"
    "v_lshlrev_b32_e64 v86, 16, v215\n"
    "v_and_b32 v87, 0xffff0000, v215\n"
    "v_pk_add_f32 v[72:73], v[72:73], v[80:81]\n"
    "v_pk_add_f32 v[74:75], v[74:75], v[82:83]\n"
    "v_pk_add_f32 v[76:77], v[76:77], v[84:85]\n"
    "v_pk_add_f32 v[78:79], v[78:79], v[86:87]\n"
    "s_waitcnt vmcnt(2)\n"
    "v_lshlrev_b32 v80, 16, v216\n"
    "v_and_b32 v81, 0xffff0000, v216\n"
    "v_lshlrev_b32_e64 v82, 16, v217\n"
    "v_and_b32 v83, 0xffff0000, v217\n"
    "v_lshlrev_b32_e64 v84, 16, v218\n"
    "v_and_b32 v85, 0xffff0000, v218\n"
    "v_lshlrev_b32_e64 v86, 16, v219\n"
    "v_and_b32 v87, 0xffff0000, v219\n"
    "v_pk_add_f32 v[72:73], v[72:73], v[80:81]\n"
    "v_pk_add_f32 v[74:75], v[74:75], v[82:83]\n"
    "v_pk_add_f32 v[76:77], v[76:77], v[84:85]\n"
    "v_pk_add_f32 v[78:79], v[78:79], v[86:87]\n"
    "s_waitcnt vmcnt(1)\n"
    "v_lshlrev_b32 v80, 16, v220\n"
    "v_and_b32 v81, 0xffff0000, v220\n"
    "v_lshlrev_b32_e64 v82, 16, v221\n"
    "v_and_b32 v83, 0xffff0000, v221\n"
    "v_lshlrev_b32_e64 v84, 16, v222\n"
    "v_and_b32 v85, 0xffff0000, v222\n"
    "v_lshlrev_b32_e64 v86, 16, v223\n"
    "v_and_b32 v87, 0xffff0000, v223\n"
    "v_pk_add_f32 v[72:73], v[72:73], v[80:81]\n"
    "v_pk_add_f32 v[74:75], v[74:75], v[82:83]\n"
    "v_pk_add_f32 v[76:77], v[76:77], v[84:85]\n"
    "v_pk_add_f32 v[78:79], v[78:79], v[86:87]\n"
    "v_add_f32_dpp v72, v72, v72 row_ror:8 row_mask:0xf bank_mask:0xf\n"
    "v_add_f32_dpp v73, v73, v73 row_ror:8 row_mask:0xf bank_mask:0xf\n"
    "v_add_f32_dpp v74, v74, v74 row_ror:8 row_mask:0xf bank_mask:0xf\n"
    "v_add_f32_dpp v75, v75, v75 row_ror:8 row_mask:0xf bank_mask:0xf\n"
    "v_add_f32_dpp v76, v76, v76 row_ror:8 row_mask:0xf bank_mask:0xf\n"
    "v_add_f32_dpp v77, v77, v77 row_ror:8 row_mask:0xf bank_mask:0xf\n"
    "v_add_f32_dpp v78, v78, v78 row_ror:8 row_mask:0xf bank_mask:0xf\n"
    "v_add_f32_dpp v79, v79, v79 row_ror:8 row_mask:0xf bank_mask:0xf\n"
    "v_permlane16_swap_b32 v72, v73\n"
    "v_permlane16_swap_b32 v74, v75\n"
    "v_permlane16_swap_b32 v76, v77\n"
    "v_permlane16_swap_b32 v78, v79\n"
    "v_add_f32 v72, v72, v73\n"
    "v_add_f32 v74, v74, v75\n"
    "v_add_f32 v76, v76, v77\n"
    "v_add_f32 v78, v78, v79\n"
    "v_permlane32_swap_b32 v72, v74\n"
    "s_nop 0\n"
    "v_permlane32_swap_b32 v76, v78\n"
    "v_add_f32 v72, v72, v74\n"
    "v_add_f32_e64 v76, v76, v78\n"
    "ds_write2_b32 v23, v72, v76 offset1:4\n"
    "v_add_u32 v23, 1024, v23\n"
    "s_waitcnt lgkmcnt(0)\n"
    "s_barrier\n"
    "ds_read2st64_b32 v[72:73], v88 offset0:20 offset1:21\n"
    "ds_read2st64_b32 v[74:75], v88 offset0:22 offset1:23\n"
    "s_waitcnt lgkmcnt(0)\n"
    "v_add_f32 v72, v72, v73\n"
    "v_add_f32 v74, v74, v75\n"
    "v_mov_b32 v73, s46\n"
    "v_add_f32_e64 v72, v72, v74\n"
    "buffer_store_dwordx2 v[72:73], v12, s[8:11], s83 offen sc1\n"
    ST(5)
    "s_lshr_b32 s70, s2, 4\n"
    "s_and_b32 s71, s2, 7\n"
    "s_lshl4_add_u32 s70, s71, s70\n"
    "s_lshl2_add_u32 s70, s70, s45\n"
    "s_lshl_b32 s90, s70, 14\n"
    "s_add_u32 s90, s90, 0\n"
    "v_and_b32 v89, 3, v0\n"
    "v_lshlrev_b32 v89, 7, v89\n"
    "v_lshrrev_b32_e64 v90, 2, v0\n"
    "v_lshl_add_u32 v89, v90, 10, v89\n"
    "s_lshl_b32 s93, s45, 8\n"
    "s_add_u32 m0, s93, 159744\n"
    "s_nop 0\n"
    "buffer_load_dword v89, s[20:23], s90 offen lds\n"
    "s_mov_b32 s26, 524288\n"
    "s_mov_b32 s27, 0x20000\n"
    "s_mov_b32 s30, 917504\n"
    "s_mov_b32 s31, 0x20000\n"
    "s_lshl2_add_u32 s67, s59, s45\n"
    "s_lshl_b32 s70, s67, 13\n"
    "s_mul_i32 s71, s67, 14336\n"
    "v_lshlrev_b32 v21, 6, v0\n"
    "s_lshr_b32 s73, s47, 2\n"
    "s_add_u32 m0, s73, 0\n"
    "s_nop 0\n"
    "buffer_load_dword v21, s[24:27], s70 offen lds\n"
    "s_add_u32 s70, s70, 4096\n"
    "buffer_load_dword v21, s[24:27], s70 offen lds\n"
    "s_add_u32 s70, s70, 4096\n"
    "buffer_load_dword v21, s[28:31], s71 offen lds\n"
    "s_add_u32 s71, s71, 4096\n"
    "buffer_load_dword v21, s[28:31], s71 offen lds\n"
    "s_add_u32 s71, s71, 4096\n"
    "buffer_load_dword v21, s[28:31], s71 offen lds\n"
    "s_add_u32 s71, s71, 2048\n"
    "buffer_load_dword v21, s[28:31], s71 offen lds\n"
    "s_add_u32 s71, s71, 2048\n"
    "s_add_u32 m0, s49, 104448\n"
    "s_nop 0\n"
    "buffer_load_dwordx4 v2, s[20:23], s55 offen offset:0 lds\n"
    "s_add_u32 m0, s49, 105472\n"
    "s_nop 0\n"
    "buffer_load_dwordx4 v3, s[20:23], s55 offen offset:0 lds\n"
    "s_add_u32 m0, s49, 106496\n"
    "s_nop 0\n"
    "buffer_load_dwordx4 v2, s[20:23], s56 offen offset:0 lds\n"
    "s_add_u32 m0, s49, 107520\n"
    "s_nop 0\n"
    "buffer_load_dwordx4 v3, s[20:23], s56 offen offset:0 lds\n"
    "s_add_u32 m0, s49, 120832\n"
    "s_nop 0\n"
    "buffer_load_dwordx4 v2, s[20:23], s57 offen offset:0 lds\n"
    "s_add_u32 m0, s49, 121856\n"
    "s_nop 0\n"
    "buffer_load_dwordx4 v3, s[20:23], s57 offen offset:0 lds\n"
    "s_add_u32 m0, s49, 122880\n"
    "s_nop 0\n"
    "buffer_load_dwordx4 v2, s[20:23], s58 offen offset:0 lds\n"
    "s_add_u32 m0, s49, 123904\n"
    "s_nop 0\n"
    "buffer_load_dwordx4 v3, s[20:23], s58 offen offset:0 lds\n"
    "s_add_u32 m0, s49, 71552\n"
    "s_nop 0\n"
    "buffer_load_dwordx4 v2, s[20:23], s55 offen offset:128 lds\n"
    "s_add_u32 m0, s49, 72576\n"
    "s_nop 0\n"
    "buffer_load_dwordx4 v3, s[20:23], s55 offen offset:128 lds\n"
    "s_add_u32 m0, s49, 73600\n"
    "s_nop 0\n"
    "buffer_load_dwordx4 v2, s[20:23], s56 offen offset:128 lds\n"
    "s_add_u32 m0, s49, 74624\n"
    "s_nop 0\n"
    "buffer_load_dwordx4 v3, s[20:23], s56 offen offset:128 lds\n"
    "s_add_u32 m0, s49, 87936\n"
    "s_nop 0\n"
    "buffer_load_dwordx4 v2, s[20:23], s57 offen offset:128 lds\n"
    "s_add_u32 m0, s49, 88960\n"
    "s_nop 0\n"
    "buffer_load_dwordx4 v3, s[20:23], s57 offen offset:128 lds\n"
    "s_add_u32 m0, s49, 89984\n"
    "s_nop 0\n"
    "buffer_load_dwordx4 v2, s[20:23], s58 offen offset:128 lds\n"
    "s_add_u32 m0, s49, 91008\n"
    "s_nop 0\n"
    "buffer_load_dwordx4 v3, s[20:23], s58 offen offset:128 lds\n"
    "s_add_u32 m0, s49, 5888\n"
    "s_nop 0\n"
    "buffer_load_dwordx4 v2, s[20:23], s55 offen offset:256 lds\n"
    "s_add_u32 m0, s49, 6912\n"
    "s_nop 0\n"
    "buffer_load_dwordx4 v3, s[20:23], s55 offen offset:256 lds\n"
    "s_add_u32 m0, s49, 7936\n"
    "s_nop 0\n"
    "buffer_load_dwordx4 v2, s[20:23], s56 offen offset:256 lds\n"
    "s_add_u32 m0, s49, 8960\n"
    "s_nop 0\n"
    "buffer_load_dwordx4 v3, s[20:23], s56 offen offset:256 lds\n"
    "s_add_u32 m0, s49, 22272\n"
    "s_nop 0\n"
    "buffer_load_dwordx4 v2, s[20:23], s57 offen offset:256 lds\n"
    "s_add_u32 m0, s49, 23296\n"
    "s_nop 0\n"
    "buffer_load_dwordx4 v3, s[20:23], s57 offen offset:256 lds\n"
    "s_add_u32 m0, s49, 24320\n"
    "s_nop 0\n"
    "buffer_load_dwordx4 v2, s[20:23], s58 offen offset:256 lds\n"
    "s_add_u32 m0, s49, 25344\n"
    "s_nop 0\n"
    "buffer_load_dwordx4 v3, s[20:23], s58 offen offset:256 lds\n"
    "s_add_u32 m0, s49, 38528\n"
    "s_nop 0\n"
    "buffer_load_dwordx4 v2, s[20:23], s55 offen offset:384 lds\n"
    "s_add_u32 m0, s49, 39552\n"
    "s_nop 0\n"
    "buffer_load_dwordx4 v3, s[20:23], s55 offen offset:384 lds\n"
    "s_add_u32 m0, s49, 40576\n"
    "s_nop 0\n"
    "buffer_load_dwordx4 v2, s[20:23], s56 offen offset:384 lds\n"
    "s_add_u32 m0, s49, 41600\n"
    "s_nop 0\n"
    "buffer_load_dwordx4 v3, s[20:23], s56 offen offset:384 lds\n"
    "s_add_u32 m0, s49, 54912\n"
    "s_nop 0\n"
    "buffer_load_dwordx4 v2, s[20:23], s57 offen offset:384 lds\n"
    "s_add_u32 m0, s49, 55936\n"
    "s_nop 0\n"
    "buffer_load_dwordx4 v3, s[20:23], s57 offen offset:384 lds\n"
    "s_add_u32 m0, s49, 56960\n"
    "s_nop 0\n"
    "buffer_load_dwordx4 v2, s[20:23], s58 offen offset:384 lds\n"
    "s_add_u32 m0, s49, 57984\n"
    "s_nop 0\n"
    "buffer_load_dwordx4 v3, s[20:23], s58 offen offset:384 lds\n"
    "s_waitcnt vmcnt(24)\n"
    "s_nop 0\n"
    "ds_read_b128 a[0:3], v6 offset:38912\n"
    "ds_read_b128 a[4:7], v6 offset:40960\n"
    "ds_read_b128 a[16:19], v7 offset:38912\n"
    "ds_read_b128 a[20:23], v7 offset:40960\n"
    "ds_read_b128 a[8:11], v6 offset:55296\n"
    "ds_read_b128 a[12:15], v6 offset:57344\n"
    "ds_read_b128 a[24:27], v7 offset:55296\n"
    "ds_read_b128 a[28:31], v7 offset:57344\n"
    "s_waitcnt lgkmcnt(0)\n"
    "s_add_u32 m0, s49, 103936\n"
    "s_nop 0\n"
    "buffer_load_dwordx4 v2, s[20:23], s55 offen offset:512 lds\n"
    "s_add_u32 m0, s49, 104960\n"
    "s_nop 0\n"
    "buffer_load_dwordx4 v3, s[20:23], s55 offen offset:512 lds\n"
    "s_add_u32 m0, s49, 105984\n"
    "s_nop 0\n"
    "buffer_load_dwordx4 v2, s[20:23], s56 offen offset:512 lds\n"
    "s_add_u32 m0, s49, 107008\n"
    "s_nop 0\n"
    "buffer_load_dwordx4 v3, s[20:23], s56 offen offset:512 lds\n"
    "s_add_u32 m0, s49, 120320\n"
    "s_nop 0\n"
    "buffer_load_dwordx4 v2, s[20:23], s57 offen offset:512 lds\n"
    "s_add_u32 m0, s49, 121344\n"
    "s_nop 0\n"
    "buffer_load_dwordx4 v3, s[20:23], s57 offen offset:512 lds\n"
    "s_add_u32 m0, s49, 122368\n"
    "s_nop 0\n"
    "buffer_load_dwordx4 v2, s[20:23], s58 offen offset:512 lds\n"
    "s_add_u32 m0, s49, 123392\n"
    "s_nop 0\n"
    "buffer_load_dwordx4 v3, s[20:23], s58 offen offset:512 lds\n"
    "s_waitcnt vmcnt(24)\n"
    "ds_read_b128 a[32:35], v6 offset:6144\n"
    "ds_read_b128 a[36:39], v6 offset:8192\n"
    "ds_read_b128 a[48:51], v7 offset:6144\n"
    "ds_read_b128 a[52:55], v7 offset:8192\n"
    "ds_read_b128 a[40:43], v6 offset:22528\n"
    "ds_read_b128 a[44:47], v6 offset:24576\n"
    "ds_read_b128 a[56:59], v7 offset:22528\n"
    "ds_read_b128 a[60:63], v7 offset:24576\n"
    "s_waitcnt lgkmcnt(0)\n"
    "s_add_u32 m0, s49, 71040\n"
    "s_nop 0\n"
    "buffer_load_dwordx4 v2, s[20:23], s55 offen offset:640 lds\n"
    "s_add_u32 m0, s49, 72064\n"
    "s_nop 0\n"
    "buffer_load_dwordx4 v3, s[20:23], s55 offen offset:640 lds\n"
    "s_add_u32 m0, s49, 73088\n"
    "s_nop 0\n"
    "buffer_load_dwordx4 v2, s[20:23], s56 offen offset:640 lds\n"
    "s_add_u32 m0, s49, 74112\n"
    "s_nop 0\n"
    "buffer_load_dwordx4 v3, s[20:23], s56 offen offset:640 lds\n"
    "s_add_u32 m0, s49, 87424\n"
    "s_nop 0\n"
    "buffer_load_dwordx4 v2, s[20:23], s57 offen offset:640 lds\n"
    "s_add_u32 m0, s49, 88448\n"
    "s_nop 0\n"
    "buffer_load_dwordx4 v3, s[20:23], s57 offen offset:640 lds\n"
    "s_add_u32 m0, s49, 89472\n"
    "s_nop 0\n"
    "buffer_load_dwordx4 v2, s[20:23], s58 offen offset:640 lds\n"
    "s_add_u32 m0, s49, 90496\n"
    "s_nop 0\n"
    "buffer_load_dwordx4 v3, s[20:23], s58 offen offset:640 lds\n"
    "s_waitcnt vmcnt(24)\n"
    "ds_read_b128 a[64:67], v4 offset:6144\n"
    "ds_read_b128 a[68:71], v4 offset:8192\n"
    "ds_read_b128 a[80:83], v5 offset:6144\n"
    "ds_read_b128 a[84:87], v5 offset:8192\n"
    "ds_read_b128 a[72:75], v4 offset:22528\n"
    "ds_read_b128 a[76:79], v4 offset:24576\n"
    "ds_read_b128 a[88:91], v5 offset:22528\n"
    "ds_read_b128 a[92:95], v5 offset:24576\n"
    "s_waitcnt lgkmcnt(0)\n"
    "s_add_u32 m0, s49, 5376\n"
    "s_nop 0\n"
    "buffer_load_dwordx4 v2, s[20:23], s55 offen offset:768 lds\n"
    "s_add_u32 m0, s49, 6400\n"
    "s_nop 0\n"
    "buffer_load_dwordx4 v3, s[20:23], s55 offen offset:768 lds\n"
    "s_add_u32 m0, s49, 7424\n"
    "s_nop 0\n"
    "buffer_load_dwordx4 v2, s[20:23], s56 offen offset:768 lds\n"
    "s_add_u32 m0, s49, 8448\n"
    "s_nop 0\n"
    "buffer_load_dwordx4 v3, s[20:23], s56 offen offset:768 lds\n"
    "s_add_u32 m0, s49, 21760\n"
    "s_nop 0\n"
    "buffer_load_dwordx4 v2, s[20:23], s57 offen offset:768 lds\n"
    "s_add_u32 m0, s49, 22784\n"
    "s_nop 0\n"
    "buffer_load_dwordx4 v3, s[20:23], s57 offen offset:768 lds\n"
    "s_add_u32 m0, s49, 23808\n"
    "s_nop 0\n"
    "buffer_load_dwordx4 v2, s[20:23], s58 offen offset:768 lds\n"
    "s_add_u32 m0, s49, 24832\n"
    "s_nop 0\n"
    "buffer_load_dwordx4 v3, s[20:23], s58 offen offset:768 lds\n"
    "s_waitcnt vmcnt(24)\n"
    "ds_read_b128 a[96:99], v4 offset:38912\n"
    "ds_read_b128 a[100:103], v4 offset:40960\n"
    "ds_read_b128 a[112:115], v5 offset:38912\n"
    "ds_read_b128 a[116:119], v5 offset:40960\n"
    "ds_read_b128 a[104:107], v4 offset:55296\n"
    "ds_read_b128 a[108:111], v4 offset:57344\n"
    "ds_read_b128 a[120:123], v5 offset:55296\n"
    "ds_read_b128 a[124:127], v5 offset:57344\n"
    "s_waitcnt lgkmcnt(0)\n"
    "s_add_u32 m0, s49, 38016\n"
    "s_nop 0\n"
    "buffer_load_dwordx4 v2, s[20:23], s55 offen offset:896 lds\n"
    "s_add_u32 m0, s49, 39040\n"
    "s_nop 0\n"
    "buffer_load_dwordx4 v3, s[20:23], s55 offen offset:896 lds\n"
    "s_add_u32 m0, s49, 40064\n"
    "s_nop 0\n"
    "buffer_load_dwordx4 v2, s[20:23], s56 offen offset:896 lds\n"
    "s_add_u32 m0, s49, 41088\n"
    "s_nop 0\n"
    "buffer_load_dwordx4 v3, s[20:23], s56 offen offset:896 lds\n"
    "s_add_u32 m0, s49, 54400\n"
    "s_nop 0\n"
    "buffer_load_dwordx4 v2, s[20:23], s57 offen offset:896 lds\n"
    "s_add_u32 m0, s49, 55424\n"
    "s_nop 0\n"
    "buffer_load_dwordx4 v3, s[20:23], s57 offen offset:896 lds\n"
    "s_add_u32 m0, s49, 56448\n"
    "s_nop 0\n"
    "buffer_load_dwordx4 v2, s[20:23], s58 offen offset:896 lds\n"
    "s_add_u32 m0, s49, 57472\n"
    "s_nop 0\n"
    "buffer_load_dwordx4 v3, s[20:23], s58 offen offset:896 lds\n"
    "s_waitcnt vmcnt(24)\n"
    "ds_read_b128 a[128:131], v6 offset:38912\n"
    "ds_read_b128 a[132:135], v6 offset:40960\n"
    "ds_read_b128 a[144:147], v7 offset:38912\n"
    "ds_read_b128 a[148:151], v7 offset:40960\n"
    "ds_read_b128 a[136:139], v6 offset:55296\n"
    "ds_read_b128 a[140:143], v6 offset:57344\n"
    "ds_read_b128 a[152:155], v7 offset:55296\n"
    "ds_read_b128 a[156:159], v7 offset:57344\n"
    "s_waitcnt vmcnt(16)\n"
    "s_nop 0\n"
    "ds_read_b128 a[160:163], v6 offset:6144\n"
    "ds_read_b128 a[164:167], v6 offset:8192\n"
    "ds_read_b128 a[176:179], v7 offset:6144\n"
    "ds_read_b128 a[180:183], v7 offset:8192\n"
    "ds_read_b128 a[168:171], v6 offset:22528\n"
    "ds_read_b128 a[172:175], v6 offset:24576\n"
    "ds_read_b128 a[184:187], v7 offset:22528\n"
    "ds_read_b128 a[188:191], v7 offset:24576\n"
    "s_waitcnt vmcnt(8)\n"
    "s_nop 0\n"
    "ds_read_b128 a[192:195], v4 offset:6144\n"
    "ds_read_b128 a[196:199], v4 offset:8192\n"
    "ds_read_b128 a[208:211], v5 offset:6144\n"
    "ds_read_b128 a[212:215], v5 offset:8192\n"
    "ds_read_b128 a[200:203], v4 offset:22528\n"
    "ds_read_b128 a[204:207], v4 offset:24576\n"
    "ds_read_b128 a[216:219], v5 offset:22528\n"
    "ds_read_b128 a[220:223], v5 offset:24576\n"
    "s_waitcnt vmcnt(0)\n"
    "s_nop 0\n"
    "ds_read_b128 a[224:227], v4 offset:38912\n"
    "ds_read_b128 a[228:231], v4 offset:40960\n"
    "ds_read_b128 a[240:243], v5 offset:38912\n"
    "ds_read_b128 a[244:247], v5 offset:40960\n"
    "ds_read_b128 a[232:235], v4 offset:55296\n"
    "ds_read_b128 a[236:239], v4 offset:57344\n"
    "ds_read_b128 a[248:251], v5 offset:55296\n"
    "ds_read_b128 a[252:255], v5 offset:57344\n"
    "s_mul_i32 s69, s62, 7\n"
    "v_and_b32 v24, 3, v0\n"
    "v_min_u32_e64 v24, 2, v24\n"
    "v_mad_u32_u24 v24, v24, 3, s69\n"
    "v_bfe_u32 v25, v0, 3, 2\n"
    "s_lshl_b32 s70, s45, 2\n"
    "v_add_u32 v25, s70, v25\n"
    "v_lshrrev_b32_e64 v26, 3, v24\n"
    "v_lshl_add_u32 v25, v25, 3, v26\n"
    "v_bfe_u32 v26, v0, 2, 1\n"
    "v_lshl_add_u32 v25, v25, 1, v26\n"
    "v_bfe_u32 v26, v24, 2, 1\n"
    "v_lshl_add_u32 v25, v25, 1, v26\n"
    "v_lshlrev_b32_e64 v25, 7, v25\n"
    "v_add_u32 v246, 4096, v25\n"
    "s_lshl_b32 s70, s62, 2\n"
    "s_add_u32 s70, s70, s45\n"
    "s_mul_i32 s70, s70, 114688\n"
    "s_add_u32 s85, s70, 626688\n"
    "s_and_b32 s70, s62, 0\n"
    "s_mul_i32 s70, s70, 14336\n"
    "s_add_u32 s85, s85, s70\n"
    "s_lshl_b32 s70, s45, 14\n"
    "s_lshl_b32 s69, s62, 7\n"
    "s_add_u32 s87, s70, s69\n"
    "s_mov_b32 s26, 524288\n"
    "s_mov_b32 s27, 0x20000\n"
    "buffer_load_dword v28, v246, s[8:11], 0 offen sc1\n"
    "s_waitcnt lgkmcnt(0)\n"
    "s_barrier\n"
    "s_add_u32 m0, s48, 98304\n"
    "s_nop 0\n"
    "buffer_load_dwordx4 v8, s[24:27], s87 offen offset:0 lds\n"
    "s_add_u32 m0, s48, 99328\n"
    "s_nop 0\n"
    "buffer_load_dwordx4 v9, s[24:27], s87 offen offset:0 lds\n"
    "s_add_u32 s87, s87, 65536\n"
    "s_add_u32 m0, s48, 106496\n"
    "s_nop 0\n"
    "buffer_load_dwordx4 v8, s[24:27], s87 offen offset:0 lds\n"
    "s_add_u32 m0, s48, 107520\n"
    "s_nop 0\n"
    "buffer_load_dwordx4 v9, s[24:27], s87 offen offset:0 lds\n"
    "s_add_u32 s87, s87, 65536\n"
    "s_add_u32 m0, s48, 114688\n"
    "s_nop 0\n"
    "buffer_load_dwordx4 v8, s[24:27], s87 offen offset:0 lds\n"
    "s_add_u32 m0, s48, 115712\n"
    "s_nop 0\n"
    "buffer_load_dwordx4 v9, s[24:27], s87 offen offset:0 lds\n"
    "s_add_u32 s87, s87, 65536\n"
    "s_add_u32 m0, s48, 122880\n"
    "s_nop 0\n"
    "buffer_load_dwordx4 v8, s[24:27], s87 offen offset:0 lds\n"
    "s_add_u32 m0, s48, 123904\n"
    "s_nop 0\n"
    "buffer_load_dwordx4 v9, s[24:27], s87 offen offset:0 lds\n"
    "s_add_u32 s87, s87, 65536\n"
    "s_add_u32 m0, s48, 131072\n"
    "s_nop 0\n"
    "buffer_load_dwordx4 v8, s[24:27], s87 offen offset:0 lds\n"
    "s_add_u32 m0, s48, 132096\n"
    "s_nop 0\n"
    "buffer_load_dwordx4 v9, s[24:27], s87 offen offset:0 lds\n"
    "s_add_u32 s87, s87, 65536\n"
    "s_add_u32 m0, s48, 139264\n"
    "s_nop 0\n"
    "buffer_load_dwordx4 v8, s[24:27], s87 offen offset:0 lds\n"
    "s_add_u32 m0, s48, 140288\n"
    "s_nop 0\n"
    "buffer_load_dwordx4 v9, s[24:27], s87 offen offset:0 lds\n"
    "s_add_u32 s87, s87, 65536\n"
    "s_add_u32 m0, s48, 147456\n"
    "s_nop 0\n"
    "buffer_load_dwordx4 v8, s[24:27], s87 offen offset:0 lds\n"
    "s_add_u32 m0, s48, 148480\n"
    "s_nop 0\n"
    "buffer_load_dwordx4 v9, s[24:27], s87 offen offset:0 lds\n"
    "s_add_u32 s87, s87, 65536\n"
    "s_add_u32 m0, s48, 155648\n"
    "s_nop 0\n"
    "buffer_load_dwordx4 v8, s[24:27], s87 offen offset:0 lds\n"
    "s_add_u32 m0, s48, 156672\n"
    "s_nop 0\n"
    "buffer_load_dwordx4 v9, s[24:27], s87 offen offset:0 lds\n"
    "s_add_u32 s87, s87, 65536\n"
    "s_sub_u32 s87, s87, 524288\n"
    "s_waitcnt vmcnt(16)\n"
    ST(7)
    "v_add_u32 v16, 0x1000, v1\n"
    "s_mov_b32 s66, 0x10000\n"
    "Lspin_30_%=:\n"
    "v_cmp_ne_u32 vcc, s46, v28\n"
    "s_cbranch_vccz Lok_29_%=\n"
    "buffer_load_dword v28, v246, s[8:11], 0 offen sc1\n"
    "s_waitcnt vmcnt(0)\n"
    "s_sub_u32 s66, s66, 1\n"
    "s_cbranch_scc0 Lspin_30_%=\n"
    "Lok_29_%=:\n"
    ST(8)
    "buffer_load_dwordx4 v[60:63], v1, s[8:11], s85 offen offset:0 sc1\n"
    "buffer_load_dwordx4 v[64:67], v1, s[8:11], s85 offen offset:1024 sc1\n"
    "buffer_load_dwordx4 v[68:71], v1, s[8:11], s85 offen offset:2048 sc1\n"
    "s_waitcnt lgkmcnt(0)\n"
    "buffer_load_dwordx4 v[72:75], v1, s[8:11], s85 offen offset:3072 sc1\n"
    "buffer_load_dwordx4 v[76:79], v16, s[8:11], s85 offen offset:0 sc1\n"
    "buffer_load_dwordx4 v[80:83], v16, s[8:11], s85 offen offset:1024 sc1\n"
    "buffer_load_dwordx4 v[84:87], v16, s[8:11], s85 offen offset:2048 sc1\n"
    "s_add_u32 s85, s85, 7168\n"
    "buffer_load_dwordx4 v[88:91], v1, s[8:11], s85 offen offset:0 sc1\n"
    "buffer_load_dwordx4 v[92:95], v1, s[8:11], s85 offen offset:1024 sc1\n"
    "buffer_load_dwordx4 v[96:99], v1, s[8:11], s85 offen offset:2048 sc1\n"
    "buffer_load_dwordx4 v[100:103], v1, s[8:11], s85 offen offset:3072 sc1\n"
    "buffer_load_dwordx4 v[104:107], v16, s[8:11], s85 offen offset:0 sc1\n"
    "buffer_load_dwordx4 v[108:111], v16, s[8:11], s85 offen offset:1024 sc1\n"
    "buffer_load_dwordx4 v[112:115], v16, s[8:11], s85 offen offset:2048 sc1\n"
    "s_add_u32 s85, s85, 7168\n"
    "buffer_load_dwordx4 v[116:119], v1, s[8:11], s85 offen offset:0 sc1\n"
    "buffer_load_dwordx4 v[120:123], v1, s[8:11], s85 offen offset:1024 sc1\n"
    "buffer_load_dwordx4 v[124:127], v1, s[8:11], s85 offen offset:2048 sc1\n"
    "buffer_load_dwordx4 v[128:131], v1, s[8:11], s85 offen offset:3072 sc1\n"
    "buffer_load_dwordx4 v[132:135], v16, s[8:11], s85 offen offset:0 sc1\n"
    "buffer_load_dwordx4 v[136:139], v16, s[8:11], s85 offen offset:1024 sc1\n"
    "buffer_load_dwordx4 v[140:143], v16, s[8:11], s85 offen offset:2048 sc1\n"
    "s_add_u32 s85, s85, 7168\n"
    "LG_4_%=:\n"
    "s_barrier\n"
    "s_waitcnt vmcnt(20)\n"
    "v_mfma_f32_16x16x32_bf16 v[144:147], v[60:63], a[0:3], 0\n"
    "v_mfma_f32_16x16x32_bf16 v[172:175], v[60:63], a[4:7], 0\n"
    "s_lshr_b32 s75, s2, 3\n"
    "v_mfma_f32_16x16x32_bf16 v[200:203], v[60:63], a[8:11], 0\n"
    "s_lshl2_add_u32 s75, s75, s45\n"
    "s_and_b32 s76, s2, 1\n"
    "v_mfma_f32_16x16x32_bf16 v[228:231], v[60:63], a[12:15], 0\n"
    "s_lshl_b32 s76, s76, 9\n"
    "s_waitcnt vmcnt(19)\n"
    "v_mfma_f32_16x16x32_bf16 v[148:151], v[64:67], a[0:3], 0\n"
    "buffer_load_dwordx4 v[60:63], v1, s[8:11], s85 offen offset:0 sc1\n"
    "v_mfma_f32_16x16x32_bf16 v[176:179], v[64:67], a[4:7], 0\n"
    "v_lshrrev_b32 v40, 2, v0\n"
    "v_mfma_f32_16x16x32_bf16 v[204:207], v[64:67], a[8:11], 0\n"
    "v_and_b32 v41, 3, v0\n"
    "v_lshlrev_b32 v41, 7, v41\n"
    "v_mfma_f32_16x16x32_bf16 v[232:235], v[64:67], a[12:15], 0\n"
    "v_lshl_add_u32 v59, v40, 10, v41\n"
    "s_waitcnt vmcnt(19)\n"
    "v_mfma_f32_16x16x32_bf16 v[152:155], v[68:71], a[0:3], 0\n"
    "buffer_load_dwordx4 v[64:67], v1, s[8:11], s85 offen offset:1024 sc1\n"
    "v_mfma_f32_16x16x32_bf16 v[180:183], v[68:71], a[4:7], 0\n"
    "s_lshl_b32 s80, s75, 12\n"
    "v_mfma_f32_16x16x32_bf16 v[208:211], v[68:71], a[8:11], 0\n"
    "s_add_u32 s80, s80, s76\n"
    "s_mul_i32 s81, s75, 7168\n"
    "v_mfma_f32_16x16x32_bf16 v[236:239], v[68:71], a[12:15], 0\n"
    "s_add_u32 s81, s81, s76\n"
    "s_waitcnt vmcnt(19)\n"
    "v_mfma_f32_16x16x32_bf16 v[156:159], v[72:75], a[0:3], 0\n"
    "buffer_load_dwordx4 v[68:71], v1, s[8:11], s85 offen offset:2048 sc1\n"
    "v_mfma_f32_16x16x32_bf16 v[184:187], v[72:75], a[4:7], 0\n"
    "s_lshl_b32 s82, s45, 8\n"
    "v_mfma_f32_16x16x32_bf16 v[212:215], v[72:75], a[8:11], 0\n"
    "s_add_u32 s82, s82, 90112\n"
    "s_mov_b32 s30, 917504\n"
    "v_mfma_f32_16x16x32_bf16 v[240:243], v[72:75], a[12:15], 0\n"
    "s_mov_b32 s31, 0x20000\n"
    "s_waitcnt vmcnt(19)\n"
    "v_mfma_f32_16x16x32_bf16 v[160:163], v[76:79], a[0:3], 0\n"
    "buffer_load_dwordx4 v[72:75], v1, s[8:11], s85 offen offset:3072 sc1\n"
    "v_mfma_f32_16x16x32_bf16 v[188:191], v[76:79], a[4:7], 0\n"
    "s_lshl2_add_u32 s67, s61, s45\n"
    "v_mfma_f32_16x16x32_bf16 v[216:219], v[76:79], a[8:11], 0\n"
    "s_and_b32 s68, s62, 1\n"
    "s_mul_i32 s69, s62, 7\n"
    "v_mfma_f32_16x16x32_bf16 v[244:247], v[76:79], a[12:15], 0\n"
    "s_lshl3_add_u32 s70, s61, s62\n"
    "s_waitcnt vmcnt(19)\n"
    "v_mfma_f32_16x16x32_bf16 v[164:167], v[80:83], a[0:3], 0\n"
    "buffer_load_dwordx4 v[76:79], v16, s[8:11], s85 offen offset:0 sc1\n"
    "v_mfma_f32_16x16x32_bf16 v[192:195], v[80:83], a[4:7], 0\n"
    "s_lshl2_add_u32 s71, s70, s45\n"
    "v_mfma_f32_16x16x32_bf16 v[220:223], v[80:83], a[8:11], 0\n"
    "s_lshl_b32 s71, s71, 7\n"
    "s_add_u32 s71, s71, 200704\n"
    "v_mfma_f32_16x16x32_bf16 v[248:251], v[80:83], a[12:15], 0\n"
    "v_mov_b32 v38, s71\n"
    "s_waitcnt vmcnt(19)\n"
    "v_mfma_f32_16x16x32_bf16 v[168:171], v[84:87], a[0:3], 0\n"
    "buffer_load_dwordx4 v[80:83], v16, s[8:11], s85 offen offset:1024 sc1\n"
    "v_mfma_f32_16x16x32_bf16 v[196:199], v[84:87], a[4:7], 0\n"
    "s_lshl_b32 s70, s70, 7\n"
    "v_mfma_f32_16x16x32_bf16 v[224:227], v[84:87], a[8:11], 0\n"
    "s_lshl2_add_u32 s71, s45, s70\n"
    "s_add_u32 s71, s71, 69632\n"
    "v_mfma_f32_16x16x32_bf16 v[252:255], v[84:87], a[12:15], 0\n"
    "v_mov_b32 v39, s71\n"
    ST(32)
    "s_waitcnt vmcnt(19)\n"
    "v_mfma_f32_16x16x32_bf16 v[144:147], v[88:91], a[16:19], v[144:147]\n"
    "buffer_load_dwordx4 v[84:87], v16, s[8:11], s85 offen offset:2048 sc1\n"
    "v_mfma_f32_16x16x32_bf16 v[172:175], v[88:91], a[20:23], v[172:175]\n"
    "s_add_u32 s85, s85, 7168\n"
    "s_and_b32 s70, s70, 0xfffff800\n"
    "v_mfma_f32_16x16x32_bf16 v[200:203], v[88:91], a[24:27], v[200:203]\n"
    "s_add_u32 s70, s70, 69632\n"
    "v_lshrrev_b32_e64 v40, 2, v0\n"
    "v_mfma_f32_16x16x32_bf16 v[228:231], v[88:91], a[28:31], v[228:231]\n"
    "v_and_b32 v41, 3, v0\n"
    "s_waitcnt vmcnt(19)\n"
    "v_mfma_f32_16x16x32_bf16 v[148:151], v[92:95], a[16:19], v[148:151]\n"
    "buffer_load_dwordx4 v[88:91], v1, s[8:11], s85 offen offset:0 sc1\n"
    "v_mfma_f32_16x16x32_bf16 v[176:179], v[92:95], a[20:23], v[176:179]\n"
    "v_lshlrev_b32_e64 v41, 2, v41\n"
    "v_mfma_f32_16x16x32_bf16 v[204:207], v[92:95], a[24:27], v[204:207]\n"
    "v_lshl_add_u32 v40, v40, 7, v41\n"
    "v_add_u32_e64 v17, s70, v40\n"
    "v_mfma_f32_16x16x32_bf16 v[232:235], v[92:95], a[28:31], v[232:235]\n"
    "s_and_b32 s72, s45, 1\n"
    "s_waitcnt vmcnt(19)\n"
    "v_mfma_f32_16x16x32_bf16 v[152:155], v[96:99], a[16:19], v[152:155]\n"
    "buffer_load_dwordx4 v[92:95], v1, s[8:11], s85 offen offset:1024 sc1\n"
    "v_mfma_f32_16x16x32_bf16 v[180:183], v[96:99], a[20:23], v[180:183]\n"
    "s_lshl1_add_u32 s70, s67, s72\n"
    "v_mfma_f32_16x16x32_bf16 v[208:211], v[96:99], a[24:27], v[208:211]\n"
    "s_lshl_b32 s70, s70, 13\n"
    "s_add_u32 s86, s70, 11636736\n"
    "v_mfma_f32_16x16x32_bf16 v[236:239], v[96:99], a[28:31], v[236:239]\n"
    "s_xor_b32 s70, s70, 16384\n"
    "s_waitcnt vmcnt(19)\n"
    "v_mfma_f32_16x16x32_bf16 v[156:159], v[100:103], a[16:19], v[156:159]\n"
    "buffer_load_dwordx4 v[96:99], v1, s[8:11], s85 offen offset:2048 sc1\n"
    "v_mfma_f32_16x16x32_bf16 v[184:187], v[100:103], a[20:23], v[184:187]\n"
    "s_add_u32 s53, s70, 11636736\n"
    "v_mfma_f32_16x16x32_bf16 v[212:215], v[100:103], a[24:27], v[212:215]\n"
    "s_lshl_b32 s71, s62, 7\n"
    "s_mul_i32 s70, s67, 56\n"
    "v_mfma_f32_16x16x32_bf16 v[240:243], v[100:103], a[28:31], v[240:243]\n"
    "s_add_u32 s70, s70, s69\n"
    "s_waitcnt vmcnt(19)\n"
    "v_mfma_f32_16x16x32_bf16 v[160:163], v[104:107], a[16:19], v[160:163]\n"
    "buffer_load_dwordx4 v[100:103], v1, s[8:11], s85 offen offset:3072 sc1\n"
    "v_mfma_f32_16x16x32_bf16 v[188:191], v[104:107], a[20:23], v[188:191]\n"
    "s_add_u32 s72, s70, s68\n"
    "v_mfma_f32_16x16x32_bf16 v[216:219], v[104:107], a[24:27], v[216:219]\n"
    "s_lshl_b32 s72, s72, 9\n"
    "s_add_u32 s88, s72, 4296704\n"
    "v_mfma_f32_16x16x32_bf16 v[244:247], v[104:107], a[28:31], v[244:247]\n"
    "s_cmp_eq_u32 s68, 0\n"
    "s_cselect_b32 s72, 6, 0\n"
    "s_cselect_b32 s73, 3, 0\n"
    "s_waitcnt vmcnt(19)\n"
    "v_mfma_f32_16x16x32_bf16 v[164:167], v[108:111], a[16:19], v[164:167]\n"
    "buffer_load_dwordx4 v[104:107], v16, s[8:11], s85 offen offset:0 sc1\n"
    "v_mfma_f32_16x16x32_bf16 v[192:195], v[108:111], a[20:23], v[192:195]\n"
    "s_add_u32 s72, s72, s70\n"
    "v_mfma_f32_16x16x32_bf16 v[220:223], v[108:111], a[24:27], v[220:223]\n"
    "s_lshl_b32 s72, s72, 9\n"
    "s_add_u32 s89, s72, 4296704\n"
    "v_mfma_f32_16x16x32_bf16 v[248:251], v[108:111], a[28:31], v[248:251]\n"
    "s_mul_i32 s70, s67, 28\n"
    "s_waitcnt vmcnt(19)\n"
    "v_mfma_f32_16x16x32_bf16 v[168:171], v[112:115], a[16:19], v[168:171]\n"
    "buffer_load_dwordx4 v[108:111], v16, s[8:11], s85 offen offset:1024 sc1\n"
    "v_mfma_f32_16x16x32_bf16 v[196:199], v[112:115], a[20:23], v[196:199]\n"
    "s_lshl_b32 s72, s70, 10\n"
    "v_mfma_f32_16x16x32_bf16 v[224:227], v[112:115], a[24:27], v[224:227]\n"
    "s_add_u32 s92, s72, 7966720\n"
    "s_lshr_b32 s72, s69, 1\n"
    "v_mfma_f32_16x16x32_bf16 v[252:255], v[112:115], a[28:31], v[252:255]\n"
    "s_add_u32 s74, s72, s68\n"
    ST(33)
    "s_waitcnt vmcnt(19)\n"
    "v_mfma_f32_16x16x32_bf16 v[144:147], v[116:119], a[32:35], v[144:147]\n"
    "buffer_load_dwordx4 v[112:115], v16, s[8:11], s85 offen offset:2048 sc1\n"
    "v_mfma_f32_16x16x32_bf16 v[172:175], v[116:119], a[36:39], v[172:175]\n"
    "s_add_u32 s85, s85, 7168\n"
    "s_lshl_b32 s74, s74, 10\n"
    "v_mfma_f32_16x16x32_bf16 v[200:203], v[116:119], a[40:43], v[200:203]\n"
    "s_add_u32 s90, s74, s92\n"
    "s_add_u32 s72, s72, s73\n"
    "v_mfma_f32_16x16x32_bf16 v[228:231], v[116:119], a[44:47], v[228:231]\n"
    "s_lshl_b32 s72, s72, 10\n"
    "s_waitcnt vmcnt(19)\n"
    "v_mfma_f32_16x16x32_bf16 v[148:151], v[120:123], a[32:35], v[148:151]\n"
    "buffer_load_dwordx4 v[116:119], v1, s[8:11], s85 offen offset:0 sc1\n"
    "v_mfma_f32_16x16x32_bf16 v[176:179], v[120:123], a[36:39], v[176:179]\n"
    "s_lshl3_add_u32 s72, s68, s72\n"
    "v_mfma_f32_16x16x32_bf16 v[204:207], v[120:123], a[40:43], v[204:207]\n"
    "s_add_u32 s91, s72, s92\n"
    "s_and_b32 s72, s45, 1\n"
    "v_mfma_f32_16x16x32_bf16 v[232:235], v[120:123], a[44:47], v[232:235]\n"
    "s_mul_i32 s72, s72, 14336\n"
    "s_waitcnt vmcnt(19)\n"
    "v_mfma_f32_16x16x32_bf16 v[152:155], v[124:127], a[32:35], v[152:155]\n"
    "buffer_load_dwordx4 v[120:123], v1, s[8:11], s85 offen offset:1024 sc1\n"
    "v_mfma_f32_16x16x32_bf16 v[180:183], v[124:127], a[36:39], v[180:183]\n"
    "s_add_u32 s92, s92, s72\n"
    "v_mfma_f32_16x16x32_bf16 v[208:211], v[124:127], a[40:43], v[208:211]\n"
    "s_xor_b32 s70, s67, 1\n"
    "s_mul_i32 s70, s70, 28672\n"
    "v_mfma_f32_16x16x32_bf16 v[236:239], v[124:127], a[44:47], v[236:239]\n"
    "s_add_u32 s70, s70, s72\n"
    "s_waitcnt vmcnt(19)\n"
    "v_mfma_f32_16x16x32_bf16 v[156:159], v[128:131], a[32:35], v[156:159]\n"
    "buffer_load_dwordx4 v[124:127], v1, s[8:11], s85 offen offset:2048 sc1\n"
    "v_mfma_f32_16x16x32_bf16 v[184:187], v[128:131], a[36:39], v[184:187]\n"
    "s_add_u32 s54, s70, 7966720\n"
    "v_mfma_f32_16x16x32_bf16 v[212:215], v[128:131], a[40:43], v[212:215]\n"
    "s_lshr_b32 s70, s45, 1\n"
    "s_lshl1_add_u32 s70, s61, s70\n"
    "v_mfma_f32_16x16x32_bf16 v[240:243], v[128:131], a[44:47], v[240:243]\n"
    "s_lshl_b32 s70, s70, 5\n"
    "s_waitcnt vmcnt(19)\n"
    "v_mfma_f32_16x16x32_bf16 v[160:163], v[132:135], a[32:35], v[160:163]\n"
    "buffer_load_dwordx4 v[128:131], v1, s[8:11], s85 offen offset:3072 sc1\n"
    "v_mfma_f32_16x16x32_bf16 v[188:191], v[132:135], a[36:39], v[188:191]\n"
    "s_and_b32 s72, s45, 1\n"
    "v_mfma_f32_16x16x32_bf16 v[216:219], v[132:135], a[40:43], v[216:219]\n"
    "s_add_u32 s70, s70, s72\n"
    "v_lshrrev_b32_e64 v40, 1, v14\n"
    "v_mfma_f32_16x16x32_bf16 v[244:247], v[132:135], a[44:47], v[244:247]\n"
    "v_add_u32 v40, s70, v40\n"
    "s_waitcnt vmcnt(19)\n"
    "v_mfma_f32_16x16x32_bf16 v[164:167], v[136:139], a[32:35], v[164:167]\n"
    "buffer_load_dwordx4 v[132:135], v16, s[8:11], s85 offen offset:0 sc1\n"
    "v_mfma_f32_16x16x32_bf16 v[192:195], v[136:139], a[36:39], v[192:195]\n"
    "v_lshlrev_b32_e64 v18, 1, v40\n"
    "v_mfma_f32_16x16x32_bf16 v[220:223], v[136:139], a[40:43], v[220:223]\n"
    "v_lshl_add_u32 v40, v40, 10, s71\n"
    "v_bfe_u32 v41, v0, 4, 1\n"
    "v_mfma_f32_16x16x32_bf16 v[248:251], v[136:139], a[44:47], v[248:251]\n"
    "v_lshl_add_u32 v40, v41, 5, v40\n"
    "s_waitcnt vmcnt(19)\n"
    "v_mfma_f32_16x16x32_bf16 v[168:171], v[140:143], a[32:35], v[168:171]\n"
    "buffer_load_dwordx4 v[136:139], v16, s[8:11], s85 offen offset:1024 sc1\n"
    "v_mfma_f32_16x16x32_bf16 v[196:199], v[140:143], a[36:39], v[196:199]\n"
    "v_lshrrev_b32 v41, 5, v0\n"
    "v_mfma_f32_16x16x32_bf16 v[224:227], v[140:143], a[40:43], v[224:227]\n"
    "v_lshl_add_u32 v24, v41, 4, v40\n"
    "s_add_u32 s70, s45, 0\n"
    "v_mfma_f32_16x16x32_bf16 v[252:255], v[140:143], a[44:47], v[252:255]\n"
    "s_and_b32 s70, s70, 3\n"
    ST(34)
    "s_waitcnt vmcnt(19)\n"
    "v_mfma_f32_16x16x32_bf16 v[144:147], v[60:63], a[48:51], v[144:147]\n"
    "buffer_load_dwordx4 v[140:143], v16, s[8:11], s85 offen offset:2048 sc1\n"
    "v_mfma_f32_16x16x32_bf16 v[172:175], v[60:63], a[52:55], v[172:175]\n"
    "s_add_u32 s85, s85, 7168\n"
    "s_mul_i32 s70, s70, 21504\n"
    "v_mfma_f32_16x16x32_bf16 v[200:203], v[60:63], a[56:59], v[200:203]\n"
    "s_add_u32 s70, s70, 0\n"
    "v_add_u32_e64 v22, s70, v1\n"
    "v_mfma_f32_16x16x32_bf16 v[228:231], v[60:63], a[60:63], v[228:231]\n"
    "s_add_u32 s70, s45, 1\n"
    "s_waitcnt vmcnt(19)\n"
    "v_mfma_f32_16x16x32_bf16 v[148:151], v[64:67], a[48:51], v[148:151]\n"
    "buffer_load_dwordx4 v[60:63], v1, s[8:11], s85 offen offset:0 sc1\n"
    "v_mfma_f32_16x16x32_bf16 v[176:179], v[64:67], a[52:55], v[176:179]\n"
    "buffer_store_dword v13, v39, s[8:11], 0 offen sc1\n"
    "s_and_b32 s70, s70, 3\n"
    "v_mfma_f32_16x16x32_bf16 v[204:207], v[64:67], a[56:59], v[204:207]\n"
    "s_mul_i32 s70, s70, 21504\n"
    "s_add_u32 s70, s70, 0\n"
    "v_mfma_f32_16x16x32_bf16 v[232:235], v[64:67], a[60:63], v[232:235]\n"
    "v_add_u32 v19, s70, v1\n"
    "s_waitcnt vmcnt(20)\n"
    "v_mfma_f32_16x16x32_bf16 v[152:155], v[68:71], a[48:51], v[152:155]\n"
    "buffer_load_dwordx4 v[64:67], v1, s[8:11], s85 offen offset:1024 sc1\n"
    "v_mfma_f32_16x16x32_bf16 v[180:183], v[68:71], a[52:55], v[180:183]\n"
    "s_add_u32 s70, s45, 2\n"
    "v_mfma_f32_16x16x32_bf16 v[208:211], v[68:71], a[56:59], v[208:211]\n"
    "s_and_b32 s70, s70, 3\n"
    "s_mul_i32 s70, s70, 21504\n"
    "v_mfma_f32_16x16x32_bf16 v[236:239], v[68:71], a[60:63], v[236:239]\n"
    "s_add_u32 s70, s70, 7168\n"
    "s_waitcnt vmcnt(20)\n"
    "v_mfma_f32_16x16x32_bf16 v[156:159], v[72:75], a[48:51], v[156:159]\n"
    "buffer_load_dwordx4 v[68:71], v1, s[8:11], s85 offen offset:2048 sc1\n"
    "v_mfma_f32_16x16x32_bf16 v[184:187], v[72:75], a[52:55], v[184:187]\n"
    "v_add_u32 v20, s70, v1\n"
    "v_mfma_f32_16x16x32_bf16 v[212:215], v[72:75], a[56:59], v[212:215]\n"
    "s_add_u32 s70, s45, 3\n"
    "s_and_b32 s70, s70, 3\n"
    "v_mfma_f32_16x16x32_bf16 v[240:243], v[72:75], a[60:63], v[240:243]\n"
    "s_mul_i32 s70, s70, 21504\n"
    "s_waitcnt vmcnt(20)\n"
    "v_mfma_f32_16x16x32_bf16 v[160:163], v[76:79], a[48:51], v[160:163]\n"
    "buffer_load_dwordx4 v[72:75], v1, s[8:11], s85 offen offset:3072 sc1\n"
    "v_mfma_f32_16x16x32_bf16 v[188:191], v[76:79], a[52:55], v[188:191]\n"
    "s_add_u32 s70, s70, 14336\n"
    "v_mfma_f32_16x16x32_bf16 v[216:219], v[76:79], a[56:59], v[216:219]\n"
    "v_add_u32 v21, s70, v1\n"
    "s_and_b32 s72, s45, 1\n"
    "v_mfma_f32_16x16x32_bf16 v[244:247], v[76:79], a[60:63], v[244:247]\n"
    "s_lshl_b32 s70, s72, 15\n"
    "s_waitcnt vmcnt(20)\n"
    "v_mfma_f32_16x16x32_bf16 v[164:167], v[80:83], a[48:51], v[164:167]\n"
    "buffer_load_dwordx4 v[76:79], v16, s[8:11], s85 offen offset:0 sc1\n"
    "v_mfma_f32_16x16x32_bf16 v[192:195], v[80:83], a[52:55], v[192:195]\n"
    "s_add_u32 s70, s70, 98304\n"
    "v_mfma_f32_16x16x32_bf16 v[220:223], v[80:83], a[56:59], v[220:223]\n"
    "v_add_u32 v26, s70, v10\n"
    "v_xor_b32_e64 v27, 32, v26\n"
    "v_mfma_f32_16x16x32_bf16 v[248:251], v[80:83], a[60:63], v[248:251]\n"
    "v_xor_b32 v28, 64, v26\n"
    "s_waitcnt vmcnt(20)\n"
    "v_mfma_f32_16x16x32_bf16 v[168:171], v[84:87], a[48:51], v[168:171]\n"
    "buffer_load_dwordx4 v[80:83], v16, s[8:11], s85 offen offset:1024 sc1\n"
    "v_mfma_f32_16x16x32_bf16 v[196:199], v[84:87], a[52:55], v[196:199]\n"
    "v_xor_b32 v29, 0x60, v26\n"
    "v_mfma_f32_16x16x32_bf16 v[224:227], v[84:87], a[56:59], v[224:227]\n"
    "v_lshrrev_b32_e64 v40, 4, v0\n"
    "v_bfe_u32 v41, v0, 2, 2\n"
    "v_mfma_f32_16x16x32_bf16 v[252:255], v[84:87], a[60:63], v[252:255]\n"
    "v_lshl_add_u32 v42, v40, 2, v41\n"
    ST(35)
    "s_waitcnt vmcnt(20)\n"
    "v_mfma_f32_16x16x32_bf16 v[144:147], v[88:91], a[64:67], v[144:147]\n"
    "buffer_load_dwordx4 v[84:87], v16, s[8:11], s85 offen offset:2048 sc1\n"
    "v_mfma_f32_16x16x32_bf16 v[172:175], v[88:91], a[68:71], v[172:175]\n"
    "s_add_u32 s85, s85, 7168\n"
    "v_and_b32 v43, 3, v0\n"
    "v_mfma_f32_16x16x32_bf16 v[200:203], v[88:91], a[72:75], v[200:203]\n"
    "v_lshlrev_b32_e64 v43, 3, v43\n"
    "v_lshl_add_u32 v42, v42, 7, v43\n"
    "v_mfma_f32_16x16x32_bf16 v[228:231], v[88:91], a[76:79], v[228:231]\n"
    "v_lshrrev_b32 v43, 1, v40\n"
    "s_waitcnt vmcnt(20)\n"
    "v_mfma_f32_16x16x32_bf16 v[148:151], v[92:95], a[64:67], v[148:151]\n"
    "buffer_load_dwordx4 v[88:91], v1, s[8:11], s85 offen offset:0 sc1\n"
    "v_mfma_f32_16x16x32_bf16 v[176:179], v[92:95], a[68:71], v[176:179]\n"
    "v_xor_b32_e64 v43, v43, v40\n"
    "v_mfma_f32_16x16x32_bf16 v[204:207], v[92:95], a[72:75], v[204:207]\n"
    "v_and_b32 v43, 1, v43\n"
    "v_lshrrev_b32 v41, 1, v41\n"
    "v_mfma_f32_16x16x32_bf16 v[232:235], v[92:95], a[76:79], v[232:235]\n"
    "v_lshl_or_b32 v41, v43, 1, v41\n"
    "s_waitcnt vmcnt(20)\n"
    "v_mfma_f32_16x16x32_bf16 v[152:155], v[96:99], a[64:67], v[152:155]\n"
    "buffer_load_dwordx4 v[92:95], v1, s[8:11], s85 offen offset:1024 sc1\n"
    "v_mfma_f32_16x16x32_bf16 v[180:183], v[96:99], a[68:71], v[180:183]\n"
    "v_lshl_add_u32 v30, v41, 5, v42\n"
    "v_mfma_f32_16x16x32_bf16 v[208:211], v[96:99], a[72:75], v[208:211]\n"
    "s_mul_i32 s70, s72, 57344\n"
    "v_add_u32 v30, s70, v30\n"
    "v_mfma_f32_16x16x32_bf16 v[236:239], v[96:99], a[76:79], v[236:239]\n"
    "v_xor_b32 v31, 32, v30\n"
    "s_waitcnt vmcnt(20)\n"
    "v_mfma_f32_16x16x32_bf16 v[156:159], v[100:103], a[64:67], v[156:159]\n"
    "buffer_load_dwordx4 v[96:99], v1, s[8:11], s85 offen offset:2048 sc1\n"
    "v_mfma_f32_16x16x32_bf16 v[184:187], v[100:103], a[68:71], v[184:187]\n"
    "v_xor_b32_e64 v32, 64, v30\n"
    "v_mfma_f32_16x16x32_bf16 v[212:215], v[100:103], a[72:75], v[212:215]\n"
    "v_xor_b32 v33, 0x60, v30\n"
    "s_lshl_b32 s70, s45, 12\n"
    "v_mfma_f32_16x16x32_bf16 v[240:243], v[100:103], a[76:79], v[240:243]\n"
    "s_add_u32 s70, s70, 131072\n"
    "s_waitcnt vmcnt(20)\n"
    "v_mfma_f32_16x16x32_bf16 v[160:163], v[104:107], a[64:67], v[160:163]\n"
    "buffer_load_dwordx4 v[100:103], v1, s[8:11], s85 offen offset:3072 sc1\n"
    "v_mfma_f32_16x16x32_bf16 v[188:191], v[104:107], a[68:71], v[188:191]\n"
    "v_add_u32_e64 v34, s70, v1\n"
    "v_mfma_f32_16x16x32_bf16 v[216:219], v[104:107], a[72:75], v[216:219]\n"
    "v_xor_b32 v35, 0x1000, v34\n"
    "v_bfe_u32 v40, v0, 3, 1\n"
    "v_mfma_f32_16x16x32_bf16 v[244:247], v[104:107], a[76:79], v[244:247]\n"
    "v_mul_u32_u24 v40, 0x3fc0, v40\n"
    "s_waitcnt vmcnt(20)\n"
    "v_mfma_f32_16x16x32_bf16 v[164:167], v[108:111], a[64:67], v[164:167]\n"
    "buffer_load_dwordx4 v[104:107], v16, s[8:11], s85 offen offset:0 sc1\n"
    "v_mfma_f32_16x16x32_bf16 v[192:195], v[108:111], a[68:71], v[192:195]\n"
    "v_sub_u32 v36, v24, v40\n"
    "v_mfma_f32_16x16x32_bf16 v[220:223], v[108:111], a[72:75], v[220:223]\n"
    "s_lshl_b32 s70, s61, 5\n"
    "s_add_u32 s70, s70, s45\n"
    "v_mfma_f32_16x16x32_bf16 v[248:251], v[108:111], a[76:79], v[248:251]\n"
    "s_lshl_b32 s70, s70, 7\n"
    "s_waitcnt vmcnt(20)\n"
    "v_mfma_f32_16x16x32_bf16 v[168:171], v[112:115], a[64:67], v[168:171]\n"
    "buffer_load_dwordx4 v[108:111], v16, s[8:11], s85 offen offset:1024 sc1\n"
    "v_mfma_f32_16x16x32_bf16 v[196:199], v[112:115], a[68:71], v[196:199]\n"
    "s_add_u32 s70, s70, 200704\n"
    "v_mfma_f32_16x16x32_bf16 v[224:227], v[112:115], a[72:75], v[224:227]\n"
    "v_and_b32_e64 v40, 3, v0\n"
    "v_lshl_add_u32 v40, v40, 9, s70\n"
    "v_mfma_f32_16x16x32_bf16 v[252:255], v[112:115], a[76:79], v[252:255]\n"
    "v_bfe_u32 v41, v0, 2, 1\n"
    ST(36)
    "s_waitcnt vmcnt(20)\n"
    "v_mfma_f32_16x16x32_bf16 v[144:147], v[116:119], a[80:83], v[144:147]\n"
    "buffer_load_dwordx4 v[112:115], v16, s[8:11], s85 offen offset:2048 sc1\n"
    "v_mfma_f32_16x16x32_bf16 v[172:175], v[116:119], a[84:87], v[172:175]\n"
    "s_add_u32 s85, s85, 7168\n"
    "v_lshlrev_b32 v41, 7, v41\n"
    "v_mfma_f32_16x16x32_bf16 v[200:203], v[116:119], a[88:91], v[200:203]\n"
    "v_xor_b32 v40, v40, v41\n"
    "s_lshl_b32 s73, s72, 11\n"
    "v_mfma_f32_16x16x32_bf16 v[228:231], v[116:119], a[92:95], v[228:231]\n"
    "v_add_u32 v25, s73, v40\n"
    "s_waitcnt vmcnt(20)\n"
    "v_mfma_f32_16x16x32_bf16 v[148:151], v[120:123], a[80:83], v[148:151]\n"
    "buffer_load_dwordx4 v[116:119], v1, s[8:11], s85 offen offset:0 sc1\n"
    "v_mfma_f32_16x16x32_bf16 v[176:179], v[120:123], a[84:87], v[176:179]\n"
    "s_mov_b64 s[40:41], s[34:35]\n"
    "v_mfma_f32_16x16x32_bf16 v[204:207], v[120:123], a[88:91], v[204:207]\n"
    "s_mov_b32 s42, 4096\n"
    "s_mov_b32 s43, 0x20000\n"
    "v_mfma_f32_16x16x32_bf16 v[232:235], v[120:123], a[92:95], v[232:235]\n"
    "s_mov_b32 s38, 2097152\n"
    "s_waitcnt vmcnt(20)\n"
    "v_mfma_f32_16x16x32_bf16 v[152:155], v[124:127], a[80:83], v[152:155]\n"
    "buffer_load_dwordx4 v[120:123], v1, s[8:11], s85 offen offset:1024 sc1\n"
    "v_mfma_f32_16x16x32_bf16 v[180:183], v[124:127], a[84:87], v[180:183]\n"
    "s_mov_b32 s39, 0x20000\n"
    "v_mfma_f32_16x16x32_bf16 v[208:211], v[124:127], a[88:91], v[208:211]\n"
    "v_mfma_f32_16x16x32_bf16 v[236:239], v[124:127], a[92:95], v[236:239]\n"
    "s_waitcnt vmcnt(20)\n"
    "v_mfma_f32_16x16x32_bf16 v[156:159], v[128:131], a[80:83], v[156:159]\n"
    "buffer_load_dwordx4 v[124:127], v1, s[8:11], s85 offen offset:2048 sc1\n"
    "v_mfma_f32_16x16x32_bf16 v[184:187], v[128:131], a[84:87], v[184:187]\n"
    "v_mfma_f32_16x16x32_bf16 v[212:215], v[128:131], a[88:91], v[212:215]\n"
    "v_mfma_f32_16x16x32_bf16 v[240:243], v[128:131], a[92:95], v[240:243]\n"
    "s_waitcnt vmcnt(20)\n"
    "v_mfma_f32_16x16x32_bf16 v[160:163], v[132:135], a[80:83], v[160:163]\n"
    "buffer_load_dwordx4 v[128:131], v1, s[8:11], s85 offen offset:3072 sc1\n"
    "v_mfma_f32_16x16x32_bf16 v[188:191], v[132:135], a[84:87], v[188:191]\n"
    "v_mfma_f32_16x16x32_bf16 v[216:219], v[132:135], a[88:91], v[216:219]\n"
    "v_mfma_f32_16x16x32_bf16 v[244:247], v[132:135], a[92:95], v[244:247]\n"
    "s_waitcnt vmcnt(20)\n"
    "v_mfma_f32_16x16x32_bf16 v[164:167], v[136:139], a[80:83], v[164:167]\n"
    "buffer_load_dwordx4 v[132:135], v16, s[8:11], s85 offen offset:0 sc1\n"
    "v_mfma_f32_16x16x32_bf16 v[192:195], v[136:139], a[84:87], v[192:195]\n"
    "v_mfma_f32_16x16x32_bf16 v[220:223], v[136:139], a[88:91], v[220:223]\n"
    "v_mfma_f32_16x16x32_bf16 v[248:251], v[136:139], a[92:95], v[248:251]\n"
    "s_waitcnt vmcnt(20)\n"
    "v_mfma_f32_16x16x32_bf16 v[168:171], v[140:143], a[80:83], v[168:171]\n"
    "buffer_load_dwordx4 v[136:139], v16, s[8:11], s85 offen offset:1024 sc1\n"
    "v_mfma_f32_16x16x32_bf16 v[196:199], v[140:143], a[84:87], v[196:199]\n"
    "v_mfma_f32_16x16x32_bf16 v[224:227], v[140:143], a[88:91], v[224:227]\n"
    "v_mfma_f32_16x16x32_bf16 v[252:255], v[140:143], a[92:95], v[252:255]\n"
    ST(37)
    "s_waitcnt vmcnt(20)\n"
    "v_mfma_f32_16x16x32_bf16 v[144:147], v[60:63], a[96:99], v[144:147]\n"
    "buffer_load_dwordx4 v[140:143], v16, s[8:11], s85 offen offset:2048 sc1\n"
    "v_mfma_f32_16x16x32_bf16 v[172:175], v[60:63], a[100:103], v[172:175]\n"
    "s_add_u32 s85, s85, 7168\n"
    "v_mfma_f32_16x16x32_bf16 v[200:203], v[60:63], a[104:107], v[200:203]\n"
    "v_mfma_f32_16x16x32_bf16 v[228:231], v[60:63], a[108:111], v[228:231]\n"
    "s_waitcnt vmcnt(19)\n"
    "v_mfma_f32_16x16x32_bf16 v[148:151], v[64:67], a[96:99], v[148:151]\n"
    "buffer_load_dwordx4 v[60:63], v1, s[8:11], s85 offen offset:0 sc1\n"
    "v_mfma_f32_16x16x32_bf16 v[176:179], v[64:67], a[100:103], v[176:179]\n"
    "v_mfma_f32_16x16x32_bf16 v[204:207], v[64:67], a[104:107], v[204:207]\n"
    "v_mfma_f32_16x16x32_bf16 v[232:235], v[64:67], a[108:111], v[232:235]\n"
    "s_waitcnt vmcnt(19)\n"
    "v_mfma_f32_16x16x32_bf16 v[152:155], v[68:71], a[96:99], v[152:155]\n"
    "buffer_load_dwordx4 v[64:67], v1, s[8:11], s85 offen offset:1024 sc1\n"
    "v_mfma_f32_16x16x32_bf16 v[180:183], v[68:71], a[100:103], v[180:183]\n"
    "s_mov_b32 m0, s82\n"
    "s_mov_b64 exec, 0xfffffff\n"
    "buffer_load_dword v59, s[28:31], s81 offen lds\n"
    "s_mov_b64 exec, -1\n"
    "s_nop 3\n"
    "v_mfma_f32_16x16x32_bf16 v[208:211], v[68:71], a[104:107], v[208:211]\n"
    "v_mfma_f32_16x16x32_bf16 v[236:239], v[68:71], a[108:111], v[236:239]\n"
    "s_waitcnt vmcnt(20)\n"
    "v_mfma_f32_16x16x32_bf16 v[156:159], v[72:75], a[96:99], v[156:159]\n"
    "buffer_load_dwordx4 v[68:71], v1, s[8:11], s85 offen offset:2048 sc1\n"
    "v_mfma_f32_16x16x32_bf16 v[184:187], v[72:75], a[100:103], v[184:187]\n"
    "v_mfma_f32_16x16x32_bf16 v[212:215], v[72:75], a[104:107], v[212:215]\n"
    "v_mfma_f32_16x16x32_bf16 v[240:243], v[72:75], a[108:111], v[240:243]\n"
    "s_waitcnt vmcnt(20)\n"
    "v_mfma_f32_16x16x32_bf16 v[160:163], v[76:79], a[96:99], v[160:163]\n"
    "buffer_load_dwordx4 v[72:75], v1, s[8:11], s85 offen offset:3072 sc1\n"
    "v_mfma_f32_16x16x32_bf16 v[188:191], v[76:79], a[100:103], v[188:191]\n"
    "v_mfma_f32_16x16x32_bf16 v[216:219], v[76:79], a[104:107], v[216:219]\n"
    "v_mfma_f32_16x16x32_bf16 v[244:247], v[76:79], a[108:111], v[244:247]\n"
    "s_waitcnt vmcnt(20)\n"
    "v_mfma_f32_16x16x32_bf16 v[164:167], v[80:83], a[96:99], v[164:167]\n"
    "buffer_load_dwordx4 v[76:79], v16, s[8:11], s85 offen offset:0 sc1\n"
    "v_mfma_f32_16x16x32_bf16 v[192:195], v[80:83], a[100:103], v[192:195]\n"
    "v_mfma_f32_16x16x32_bf16 v[220:223], v[80:83], a[104:107], v[220:223]\n"
    "v_mfma_f32_16x16x32_bf16 v[248:251], v[80:83], a[108:111], v[248:251]\n"
    "s_waitcnt vmcnt(20)\n"
    "v_mfma_f32_16x16x32_bf16 v[168:171], v[84:87], a[96:99], v[168:171]\n"
    "buffer_load_dwordx4 v[80:83], v16, s[8:11], s85 offen offset:1024 sc1\n"
    "v_mfma_f32_16x16x32_bf16 v[196:199], v[84:87], a[100:103], v[196:199]\n"
    "v_mfma_f32_16x16x32_bf16 v[224:227], v[84:87], a[104:107], v[224:227]\n"
    "v_mfma_f32_16x16x32_bf16 v[252:255], v[84:87], a[108:111], v[252:255]\n"
    ST(38)
    "s_waitcnt vmcnt(20)\n"
    "v_mfma_f32_16x16x32_bf16 v[144:147], v[88:91], a[112:115], v[144:147]\n"
    "buffer_load_dwordx4 v[84:87], v16, s[8:11], s85 offen offset:2048 sc1\n"
    "v_mfma_f32_16x16x32_bf16 v[172:175], v[88:91], a[116:119], v[172:175]\n"
    "s_add_u32 s85, s85, 7168\n"
    "v_mfma_f32_16x16x32_bf16 v[200:203], v[88:91], a[120:123], v[200:203]\n"
    "v_mfma_f32_16x16x32_bf16 v[228:231], v[88:91], a[124:127], v[228:231]\n"
    "s_waitcnt vmcnt(20)\n"
    "v_mfma_f32_16x16x32_bf16 v[148:151], v[92:95], a[112:115], v[148:151]\n"
    "buffer_load_dwordx4 v[88:91], v1, s[8:11], s85 offen offset:0 sc1\n"
    "v_mfma_f32_16x16x32_bf16 v[176:179], v[92:95], a[116:119], v[176:179]\n"
    "v_mfma_f32_16x16x32_bf16 v[204:207], v[92:95], a[120:123], v[204:207]\n"
    "v_mfma_f32_16x16x32_bf16 v[232:235], v[92:95], a[124:127], v[232:235]\n"
    "s_waitcnt vmcnt(20)\n"
    "v_mfma_f32_16x16x32_bf16 v[152:155], v[96:99], a[112:115], v[152:155]\n"
    "buffer_load_dwordx4 v[92:95], v1, s[8:11], s85 offen offset:1024 sc1\n"
    "v_mfma_f32_16x16x32_bf16 v[180:183], v[96:99], a[116:119], v[180:183]\n"
    "v_mfma_f32_16x16x32_bf16 v[208:211], v[96:99], a[120:123], v[208:211]\n"
    "v_mfma_f32_16x16x32_bf16 v[236:239], v[96:99], a[124:127], v[236:239]\n"
    "s_waitcnt vmcnt(20)\n"
    "v_mfma_f32_16x16x32_bf16 v[156:159], v[100:103], a[112:115], v[156:159]\n"
    "buffer_load_dwordx4 v[96:99], v1, s[8:11], s85 offen offset:2048 sc1\n"
    "v_mfma_f32_16x16x32_bf16 v[184:187], v[100:103], a[116:119], v[184:187]\n"
    "v_mfma_f32_16x16x32_bf16 v[212:215], v[100:103], a[120:123], v[212:215]\n"
    "v_mfma_f32_16x16x32_bf16 v[240:243], v[100:103], a[124:127], v[240:243]\n"
    "s_waitcnt vmcnt(20)\n"
    "v_mfma_f32_16x16x32_bf16 v[160:163], v[104:107], a[112:115], v[160:163]\n"
    "buffer_load_dwordx4 v[100:103], v1, s[8:11], s85 offen offset:3072 sc1\n"
    "v_mfma_f32_16x16x32_bf16 v[188:191], v[104:107], a[116:119], v[188:191]\n"
    "v_mfma_f32_16x16x32_bf16 v[216:219], v[104:107], a[120:123], v[216:219]\n"
    "v_mfma_f32_16x16x32_bf16 v[244:247], v[104:107], a[124:127], v[244:247]\n"
    "s_waitcnt vmcnt(20)\n"
    "v_mfma_f32_16x16x32_bf16 v[164:167], v[108:111], a[112:115], v[164:167]\n"
    "buffer_load_dwordx4 v[104:107], v16, s[8:11], s85 offen offset:0 sc1\n"
    "v_mfma_f32_16x16x32_bf16 v[192:195], v[108:111], a[116:119], v[192:195]\n"
    "v_mfma_f32_16x16x32_bf16 v[220:223], v[108:111], a[120:123], v[220:223]\n"
    "v_mfma_f32_16x16x32_bf16 v[248:251], v[108:111], a[124:127], v[248:251]\n"
    "s_waitcnt vmcnt(20)\n"
    "v_mfma_f32_16x16x32_bf16 v[168:171], v[112:115], a[112:115], v[168:171]\n"
    "buffer_load_dwordx4 v[108:111], v16, s[8:11], s85 offen offset:1024 sc1\n"
    "v_mfma_f32_16x16x32_bf16 v[196:199], v[112:115], a[116:119], v[196:199]\n"
    "v_mfma_f32_16x16x32_bf16 v[224:227], v[112:115], a[120:123], v[224:227]\n"
    "v_mfma_f32_16x16x32_bf16 v[252:255], v[112:115], a[124:127], v[252:255]\n"
    ST(39)
    "s_waitcnt vmcnt(20)\n"
    "v_mfma_f32_16x16x32_bf16 v[144:147], v[116:119], a[128:131], v[144:147]\n"
    "buffer_load_dwordx4 v[112:115], v16, s[8:11], s85 offen offset:2048 sc1\n"
    "v_mfma_f32_16x16x32_bf16 v[172:175], v[116:119], a[132:135], v[172:175]\n"
    "s_add_u32 s85, s85, 7168\n"
    "v_mfma_f32_16x16x32_bf16 v[200:203], v[116:119], a[136:139], v[200:203]\n"
    "v_mfma_f32_16x16x32_bf16 v[228:231], v[116:119], a[140:143], v[228:231]\n"
    "s_waitcnt vmcnt(20)\n"
    "v_mfma_f32_16x16x32_bf16 v[148:151], v[120:123], a[128:131], v[148:151]\n"
    "buffer_load_dwordx4 v[116:119], v1, s[8:11], s85 offen offset:0 sc1\n"
    "v_mfma_f32_16x16x32_bf16 v[176:179], v[120:123], a[132:135], v[176:179]\n"
    "v_mfma_f32_16x16x32_bf16 v[204:207], v[120:123], a[136:139], v[204:207]\n"
    "v_mfma_f32_16x16x32_bf16 v[232:235], v[120:123], a[140:143], v[232:235]\n"
    "s_waitcnt vmcnt(20)\n"
    "v_mfma_f32_16x16x32_bf16 v[152:155], v[124:127], a[128:131], v[152:155]\n"
    "buffer_load_dwordx4 v[120:123], v1, s[8:11], s85 offen offset:1024 sc1\n"
    "v_mfma_f32_16x16x32_bf16 v[180:183], v[124:127], a[132:135], v[180:183]\n"
    "v_mfma_f32_16x16x32_bf16 v[208:211], v[124:127], a[136:139], v[208:211]\n"
    "v_mfma_f32_16x16x32_bf16 v[236:239], v[124:127], a[140:143], v[236:239]\n"
    "s_waitcnt vmcnt(20)\n"
    "v_mfma_f32_16x16x32_bf16 v[156:159], v[128:131], a[128:131], v[156:159]\n"
    "buffer_load_dwordx4 v[124:127], v1, s[8:11], s85 offen offset:2048 sc1\n"
    "v_mfma_f32_16x16x32_bf16 v[184:187], v[128:131], a[132:135], v[184:187]\n"
    "buffer_load_dword v56, v17, s[8:11], 0 offen sc1\n"
    "v_mfma_f32_16x16x32_bf16 v[212:215], v[128:131], a[136:139], v[212:215]\n"
    "v_mfma_f32_16x16x32_bf16 v[240:243], v[128:131], a[140:143], v[240:243]\n"
    "s_waitcnt vmcnt(21)\n"
    "v_mfma_f32_16x16x32_bf16 v[160:163], v[132:135], a[128:131], v[160:163]\n"
    "buffer_load_dwordx4 v[128:131], v1, s[8:11], s85 offen offset:3072 sc1\n"
    "v_mfma_f32_16x16x32_bf16 v[188:191], v[132:135], a[132:135], v[188:191]\n"
    "v_mfma_f32_16x16x32_bf16 v[216:219], v[132:135], a[136:139], v[216:219]\n"
    "v_mfma_f32_16x16x32_bf16 v[244:247], v[132:135], a[140:143], v[244:247]\n"
    "s_waitcnt vmcnt(21)\n"
    "v_mfma_f32_16x16x32_bf16 v[164:167], v[136:139], a[128:131], v[164:167]\n"
    "buffer_load_dwordx4 v[132:135], v16, s[8:11], s85 offen offset:0 sc1\n"
    "v_mfma_f32_16x16x32_bf16 v[192:195], v[136:139], a[132:135], v[192:195]\n"
    "v_mfma_f32_16x16x32_bf16 v[220:223], v[136:139], a[136:139], v[220:223]\n"
    "v_mfma_f32_16x16x32_bf16 v[248:251], v[136:139], a[140:143], v[248:251]\n"
    "s_waitcnt vmcnt(21)\n"
    "v_mfma_f32_16x16x32_bf16 v[168:171], v[140:143], a[128:131], v[168:171]\n"
    "buffer_load_dwordx4 v[136:139], v16, s[8:11], s85 offen offset:1024 sc1\n"
    "v_mfma_f32_16x16x32_bf16 v[196:199], v[140:143], a[132:135], v[196:199]\n"
    "v_mfma_f32_16x16x32_bf16 v[224:227], v[140:143], a[136:139], v[224:227]\n"
    "v_mfma_f32_16x16x32_bf16 v[252:255], v[140:143], a[140:143], v[252:255]\n"
    ST(40)
    "s_waitcnt vmcnt(21)\n"
    "v_mfma_f32_16x16x32_bf16 v[144:147], v[60:63], a[144:147], v[144:147]\n"
    "buffer_load_dwordx4 v[140:143], v16, s[8:11], s85 offen offset:2048 sc1\n"
    "v_mfma_f32_16x16x32_bf16 v[172:175], v[60:63], a[148:151], v[172:175]\n"
    "s_add_u32 s85, s85, 7168\n"
    "v_mfma_f32_16x16x32_bf16 v[200:203], v[60:63], a[152:155], v[200:203]\n"
    "v_mfma_f32_16x16x32_bf16 v[228:231], v[60:63], a[156:159], v[228:231]\n"
    "s_waitcnt vmcnt(21)\n"
    "v_mfma_f32_16x16x32_bf16 v[148:151], v[64:67], a[144:147], v[148:151]\n"
    "buffer_load_dwordx4 v[60:63], v1, s[8:11], s85 offen offset:0 sc1\n"
    "v_mfma_f32_16x16x32_bf16 v[176:179], v[64:67], a[148:151], v[176:179]\n"
    "v_mfma_f32_16x16x32_bf16 v[204:207], v[64:67], a[152:155], v[204:207]\n"
    "v_mfma_f32_16x16x32_bf16 v[232:235], v[64:67], a[156:159], v[232:235]\n"
    "s_waitcnt vmcnt(20)\n"
    "v_mfma_f32_16x16x32_bf16 v[152:155], v[68:71], a[144:147], v[152:155]\n"
    "buffer_load_dwordx4 v[64:67], v1, s[8:11], s85 offen offset:1024 sc1\n"
    "v_mfma_f32_16x16x32_bf16 v[180:183], v[68:71], a[148:151], v[180:183]\n"
    "v_mfma_f32_16x16x32_bf16 v[208:211], v[68:71], a[152:155], v[208:211]\n"
    "v_mfma_f32_16x16x32_bf16 v[236:239], v[68:71], a[156:159], v[236:239]\n"
    "s_waitcnt vmcnt(20)\n"
    "v_mfma_f32_16x16x32_bf16 v[156:159], v[72:75], a[144:147], v[156:159]\n"
    "buffer_load_dwordx4 v[68:71], v1, s[8:11], s85 offen offset:2048 sc1\n"
    "v_mfma_f32_16x16x32_bf16 v[184:187], v[72:75], a[148:151], v[184:187]\n"
    "v_mfma_f32_16x16x32_bf16 v[212:215], v[72:75], a[152:155], v[212:215]\n"
    "v_mfma_f32_16x16x32_bf16 v[240:243], v[72:75], a[156:159], v[240:243]\n"
    "s_waitcnt vmcnt(20)\n"
    "v_mfma_f32_16x16x32_bf16 v[160:163], v[76:79], a[144:147], v[160:163]\n"
    "buffer_load_dwordx4 v[72:75], v1, s[8:11], s85 offen offset:3072 sc1\n"
    "v_mfma_f32_16x16x32_bf16 v[188:191], v[76:79], a[148:151], v[188:191]\n"
    "v_mfma_f32_16x16x32_bf16 v[216:219], v[76:79], a[152:155], v[216:219]\n"
    "v_mfma_f32_16x16x32_bf16 v[244:247], v[76:79], a[156:159], v[244:247]\n"
    "s_waitcnt vmcnt(20)\n"
    "v_mfma_f32_16x16x32_bf16 v[164:167], v[80:83], a[144:147], v[164:167]\n"
    "buffer_load_dwordx4 v[76:79], v16, s[8:11], s85 offen offset:0 sc1\n"
    "v_mfma_f32_16x16x32_bf16 v[192:195], v[80:83], a[148:151], v[192:195]\n"
    "v_mfma_f32_16x16x32_bf16 v[220:223], v[80:83], a[152:155], v[220:223]\n"
    "v_mfma_f32_16x16x32_bf16 v[248:251], v[80:83], a[156:159], v[248:251]\n"
    "s_waitcnt vmcnt(20)\n"
    "v_mfma_f32_16x16x32_bf16 v[168:171], v[84:87], a[144:147], v[168:171]\n"
    "buffer_load_dwordx4 v[80:83], v16, s[8:11], s85 offen offset:1024 sc1\n"
    "v_mfma_f32_16x16x32_bf16 v[196:199], v[84:87], a[148:151], v[196:199]\n"
    "v_mfma_f32_16x16x32_bf16 v[224:227], v[84:87], a[152:155], v[224:227]\n"
    "v_mfma_f32_16x16x32_bf16 v[252:255], v[84:87], a[156:159], v[252:255]\n"
    ST(41)
    "s_waitcnt vmcnt(20)\n"
    "v_mfma_f32_16x16x32_bf16 v[144:147], v[88:91], a[160:163], v[144:147]\n"
    "buffer_load_dwordx4 v[84:87], v16, s[8:11], s85 offen offset:2048 sc1\n"
    "v_mfma_f32_16x16x32_bf16 v[172:175], v[88:91], a[164:167], v[172:175]\n"
    "s_add_u32 s85, s85, 7168\n"
    "v_mfma_f32_16x16x32_bf16 v[200:203], v[88:91], a[168:171], v[200:203]\n"
    "v_mfma_f32_16x16x32_bf16 v[228:231], v[88:91], a[172:175], v[228:231]\n"
    "s_waitcnt vmcnt(20)\n"
    "v_mfma_f32_16x16x32_bf16 v[148:151], v[92:95], a[160:163], v[148:151]\n"
    "buffer_load_dwordx4 v[88:91], v1, s[8:11], s85 offen offset:0 sc1\n"
    "v_mfma_f32_16x16x32_bf16 v[176:179], v[92:95], a[164:167], v[176:179]\n"
    "v_mfma_f32_16x16x32_bf16 v[204:207], v[92:95], a[168:171], v[204:207]\n"
    "v_mfma_f32_16x16x32_bf16 v[232:235], v[92:95], a[172:175], v[232:235]\n"
    "s_waitcnt vmcnt(20)\n"
    "v_mfma_f32_16x16x32_bf16 v[152:155], v[96:99], a[160:163], v[152:155]\n"
    "buffer_load_dwordx4 v[92:95], v1, s[8:11], s85 offen offset:1024 sc1\n"
    "v_mfma_f32_16x16x32_bf16 v[180:183], v[96:99], a[164:167], v[180:183]\n"
    "v_mfma_f32_16x16x32_bf16 v[208:211], v[96:99], a[168:171], v[208:211]\n"
    "v_mfma_f32_16x16x32_bf16 v[236:239], v[96:99], a[172:175], v[236:239]\n"
    "s_waitcnt vmcnt(20)\n"
    "v_mfma_f32_16x16x32_bf16 v[156:159], v[100:103], a[160:163], v[156:159]\n"
    "buffer_load_dwordx4 v[96:99], v1, s[8:11], s85 offen offset:2048 sc1\n"
    "v_mfma_f32_16x16x32_bf16 v[184:187], v[100:103], a[164:167], v[184:187]\n"
    "v_mfma_f32_16x16x32_bf16 v[212:215], v[100:103], a[168:171], v[212:215]\n"
    "v_mfma_f32_16x16x32_bf16 v[240:243], v[100:103], a[172:175], v[240:243]\n"
    "s_waitcnt vmcnt(20)\n"
    "v_mfma_f32_16x16x32_bf16 v[160:163], v[104:107], a[160:163], v[160:163]\n"
    "buffer_load_dwordx4 v[100:103], v1, s[8:11], s85 offen offset:3072 sc1\n"
    "v_mfma_f32_16x16x32_bf16 v[188:191], v[104:107], a[164:167], v[188:191]\n"
    "v_mfma_f32_16x16x32_bf16 v[216:219], v[104:107], a[168:171], v[216:219]\n"
    "v_mfma_f32_16x16x32_bf16 v[244:247], v[104:107], a[172:175], v[244:247]\n"
    "s_waitcnt vmcnt(20)\n"
    "v_mfma_f32_16x16x32_bf16 v[164:167], v[108:111], a[160:163], v[164:167]\n"
    "buffer_load_dwordx4 v[104:107], v16, s[8:11], s85 offen offset:0 sc1\n"
    "v_mfma_f32_16x16x32_bf16 v[192:195], v[108:111], a[164:167], v[192:195]\n"
    "v_mfma_f32_16x16x32_bf16 v[220:223], v[108:111], a[168:171], v[220:223]\n"
    "v_mfma_f32_16x16x32_bf16 v[248:251], v[108:111], a[172:175], v[248:251]\n"
    "s_waitcnt vmcnt(20)\n"
    "v_mfma_f32_16x16x32_bf16 v[168:171], v[112:115], a[160:163], v[168:171]\n"
    "buffer_load_dwordx4 v[108:111], v16, s[8:11], s85 offen offset:1024 sc1\n"
    "v_mfma_f32_16x16x32_bf16 v[196:199], v[112:115], a[164:167], v[196:199]\n"
    "v_mfma_f32_16x16x32_bf16 v[224:227], v[112:115], a[168:171], v[224:227]\n"
    "v_mfma_f32_16x16x32_bf16 v[252:255], v[112:115], a[172:175], v[252:255]\n"
    ST(42)
    "s_waitcnt vmcnt(20)\n"
    "v_mfma_f32_16x16x32_bf16 v[144:147], v[116:119], a[176:179], v[144:147]\n"
    "buffer_load_dwordx4 v[112:115], v16, s[8:11], s85 offen offset:2048 sc1\n"
    "v_mfma_f32_16x16x32_bf16 v[172:175], v[116:119], a[180:183], v[172:175]\n"
    "s_add_u32 s85, s85, 7168\n"
    "v_mfma_f32_16x16x32_bf16 v[200:203], v[116:119], a[184:187], v[200:203]\n"
    "v_mfma_f32_16x16x32_bf16 v[228:231], v[116:119], a[188:191], v[228:231]\n"
    "s_waitcnt vmcnt(20)\n"
    "v_mfma_f32_16x16x32_bf16 v[148:151], v[120:123], a[176:179], v[148:151]\n"
    "buffer_load_dwordx4 v[116:119], v1, s[8:11], s85 offen offset:0 sc1\n"
    "v_mfma_f32_16x16x32_bf16 v[176:179], v[120:123], a[180:183], v[176:179]\n"
    "v_mfma_f32_16x16x32_bf16 v[204:207], v[120:123], a[184:187], v[204:207]\n"
    "v_mfma_f32_16x16x32_bf16 v[232:235], v[120:123], a[188:191], v[232:235]\n"
    "s_waitcnt vmcnt(20)\n"
    "v_mfma_f32_16x16x32_bf16 v[152:155], v[124:127], a[176:179], v[152:155]\n"
    "buffer_load_dwordx4 v[120:123], v1, s[8:11], s85 offen offset:1024 sc1\n"
    "v_mfma_f32_16x16x32_bf16 v[180:183], v[124:127], a[180:183], v[180:183]\n"
    "v_mfma_f32_16x16x32_bf16 v[208:211], v[124:127], a[184:187], v[208:211]\n"
    "v_mfma_f32_16x16x32_bf16 v[236:239], v[124:127], a[188:191], v[236:239]\n"
    "s_waitcnt vmcnt(19)\n"
    "v_mfma_f32_16x16x32_bf16 v[156:159], v[128:131], a[176:179], v[156:159]\n"
    "buffer_load_dwordx4 v[124:127], v1, s[8:11], s85 offen offset:2048 sc1\n"
    "v_mfma_f32_16x16x32_bf16 v[184:187], v[128:131], a[180:183], v[184:187]\n"
    "v_mfma_f32_16x16x32_bf16 v[212:215], v[128:131], a[184:187], v[212:215]\n"
    "v_mfma_f32_16x16x32_bf16 v[240:243], v[128:131], a[188:191], v[240:243]\n"
    "s_waitcnt vmcnt(19)\n"
    "v_mfma_f32_16x16x32_bf16 v[160:163], v[132:135], a[176:179], v[160:163]\n"
    "buffer_load_dwordx4 v[128:131], v1, s[8:11], s85 offen offset:3072 sc1\n"
    "v_mfma_f32_16x16x32_bf16 v[188:191], v[132:135], a[180:183], v[188:191]\n"
    "v_mfma_f32_16x16x32_bf16 v[216:219], v[132:135], a[184:187], v[216:219]\n"
    "v_mfma_f32_16x16x32_bf16 v[244:247], v[132:135], a[188:191], v[244:247]\n"
    "s_waitcnt vmcnt(19)\n"
    "v_mfma_f32_16x16x32_bf16 v[164:167], v[136:139], a[176:179], v[164:167]\n"
    "buffer_load_dwordx4 v[132:135], v16, s[8:11], s85 offen offset:0 sc1\n"
    "v_mfma_f32_16x16x32_bf16 v[192:195], v[136:139], a[180:183], v[192:195]\n"
    "v_mfma_f32_16x16x32_bf16 v[220:223], v[136:139], a[184:187], v[220:223]\n"
    "v_mfma_f32_16x16x32_bf16 v[248:251], v[136:139], a[188:191], v[248:251]\n"
    "s_waitcnt vmcnt(19)\n"
    "v_mfma_f32_16x16x32_bf16 v[168:171], v[140:143], a[176:179], v[168:171]\n"
    "buffer_load_dwordx4 v[136:139], v16, s[8:11], s85 offen offset:1024 sc1\n"
    "v_mfma_f32_16x16x32_bf16 v[196:199], v[140:143], a[180:183], v[196:199]\n"
    "v_mfma_f32_16x16x32_bf16 v[224:227], v[140:143], a[184:187], v[224:227]\n"
    "v_mfma_f32_16x16x32_bf16 v[252:255], v[140:143], a[188:191], v[252:255]\n"
    ST(43)
    "v_cmp_ne_u32 vcc, s46, v56\n"
    "s_cbranch_vccnz Lslow_31_%=\n"
    "Lback_32_%=:\n"
    "s_waitcnt vmcnt(19)\n"
    "v_mfma_f32_16x16x32_bf16 v[144:147], v[60:63], a[192:195], v[144:147]\n"
    "buffer_load_dwordx4 v[140:143], v16, s[8:11], s85 offen offset:2048 sc1\n"
    "v_mfma_f32_16x16x32_bf16 v[172:175], v[60:63], a[196:199], v[172:175]\n"
    "s_add_u32 s85, s85, 7168\n"
    "v_mfma_f32_16x16x32_bf16 v[200:203], v[60:63], a[200:203], v[200:203]\n"
    "v_mfma_f32_16x16x32_bf16 v[228:231], v[60:63], a[204:207], v[228:231]\n"
    "s_waitcnt vmcnt(19)\n"
    "v_mfma_f32_16x16x32_bf16 v[148:151], v[64:67], a[192:195], v[148:151]\n"
    "buffer_load_dwordx4 v[60:63], v1, s[8:11], s85 offen offset:0 sc1\n"
    "v_mfma_f32_16x16x32_bf16 v[176:179], v[64:67], a[196:199], v[176:179]\n"
    "v_mfma_f32_16x16x32_bf16 v[204:207], v[64:67], a[200:203], v[204:207]\n"
    "v_mfma_f32_16x16x32_bf16 v[232:235], v[64:67], a[204:207], v[232:235]\n"
    "s_waitcnt vmcnt(19)\n"
    "v_mfma_f32_16x16x32_bf16 v[152:155], v[68:71], a[192:195], v[152:155]\n"
    "buffer_load_dwordx4 v[64:67], v1, s[8:11], s85 offen offset:1024 sc1\n"
    "v_mfma_f32_16x16x32_bf16 v[180:183], v[68:71], a[196:199], v[180:183]\n"
    "v_mfma_f32_16x16x32_bf16 v[208:211], v[68:71], a[200:203], v[208:211]\n"
    "v_mfma_f32_16x16x32_bf16 v[236:239], v[68:71], a[204:207], v[236:239]\n"
    "s_waitcnt vmcnt(19)\n"
    "v_mfma_f32_16x16x32_bf16 v[156:159], v[72:75], a[192:195], v[156:159]\n"
    "buffer_load_dwordx4 v[68:71], v1, s[8:11], s85 offen offset:2048 sc1\n"
    "v_mfma_f32_16x16x32_bf16 v[184:187], v[72:75], a[196:199], v[184:187]\n"
    "v_mfma_f32_16x16x32_bf16 v[212:215], v[72:75], a[200:203], v[212:215]\n"
    "v_mfma_f32_16x16x32_bf16 v[240:243], v[72:75], a[204:207], v[240:243]\n"
    "s_waitcnt vmcnt(19)\n"
    "v_mfma_f32_16x16x32_bf16 v[160:163], v[76:79], a[192:195], v[160:163]\n"
    "buffer_load_dwordx4 v[72:75], v1, s[8:11], s85 offen offset:3072 sc1\n"
    "v_mfma_f32_16x16x32_bf16 v[188:191], v[76:79], a[196:199], v[188:191]\n"
    "v_mfma_f32_16x16x32_bf16 v[216:219], v[76:79], a[200:203], v[216:219]\n"
    "v_mfma_f32_16x16x32_bf16 v[244:247], v[76:79], a[204:207], v[244:247]\n"
    "s_waitcnt vmcnt(19)\n"
    "v_mfma_f32_16x16x32_bf16 v[164:167], v[80:83], a[192:195], v[164:167]\n"
    "buffer_load_dwordx4 v[76:79], v16, s[8:11], s85 offen offset:0 sc1\n"
    "v_mfma_f32_16x16x32_bf16 v[192:195], v[80:83], a[196:199], v[192:195]\n"
    "v_mfma_f32_16x16x32_bf16 v[220:223], v[80:83], a[200:203], v[220:223]\n"
    "v_mfma_f32_16x16x32_bf16 v[248:251], v[80:83], a[204:207], v[248:251]\n"
    "s_waitcnt vmcnt(19)\n"
    "v_mfma_f32_16x16x32_bf16 v[168:171], v[84:87], a[192:195], v[168:171]\n"
    "buffer_load_dwordx4 v[80:83], v16, s[8:11], s85 offen offset:1024 sc1\n"
    "v_mfma_f32_16x16x32_bf16 v[196:199], v[84:87], a[196:199], v[196:199]\n"
    "v_mfma_f32_16x16x32_bf16 v[224:227], v[84:87], a[200:203], v[224:227]\n"
    "v_mfma_f32_16x16x32_bf16 v[252:255], v[84:87], a[204:207], v[252:255]\n"
    ST(44)
    "s_waitcnt vmcnt(19)\n"
    "v_mfma_f32_16x16x32_bf16 v[144:147], v[88:91], a[208:211], v[144:147]\n"
    "buffer_load_dwordx4 v[84:87], v16, s[8:11], s85 offen offset:2048 sc1\n"
    "v_mfma_f32_16x16x32_bf16 v[172:175], v[88:91], a[212:215], v[172:175]\n"
    "s_add_u32 s85, s85, 7168\n"
    "v_mfma_f32_16x16x32_bf16 v[200:203], v[88:91], a[216:219], v[200:203]\n"
    "v_mfma_f32_16x16x32_bf16 v[228:231], v[88:91], a[220:223], v[228:231]\n"
    "s_waitcnt vmcnt(19)\n"
    "v_mfma_f32_16x16x32_bf16 v[148:151], v[92:95], a[208:211], v[148:151]\n"
    "buffer_load_dwordx2 v[40:41], v12, s[8:11], s88 offen offset:0 sc1\n"
    "v_mfma_f32_16x16x32_bf16 v[176:179], v[92:95], a[212:215], v[176:179]\n"
    "v_mfma_f32_16x16x32_bf16 v[204:207], v[92:95], a[216:219], v[204:207]\n"
    "v_mfma_f32_16x16x32_bf16 v[232:235], v[92:95], a[220:223], v[232:235]\n"
    "s_waitcnt vmcnt(19)\n"
    "v_mfma_f32_16x16x32_bf16 v[152:155], v[96:99], a[208:211], v[152:155]\n"
    "buffer_load_dwordx2 v[42:43], v12, s[8:11], s88 offen offset:512 sc1\n"
    "v_mfma_f32_16x16x32_bf16 v[180:183], v[96:99], a[212:215], v[180:183]\n"
    "v_mfma_f32_16x16x32_bf16 v[208:211], v[96:99], a[216:219], v[208:211]\n"
    "v_mfma_f32_16x16x32_bf16 v[236:239], v[96:99], a[220:223], v[236:239]\n"
    "s_waitcnt vmcnt(19)\n"
    "v_mfma_f32_16x16x32_bf16 v[156:159], v[100:103], a[208:211], v[156:159]\n"
    "buffer_load_dwordx2 v[44:45], v12, s[8:11], s88 offen offset:1024 sc1\n"
    "v_mfma_f32_16x16x32_bf16 v[184:187], v[100:103], a[212:215], v[184:187]\n"
    "v_mfma_f32_16x16x32_bf16 v[212:215], v[100:103], a[216:219], v[212:215]\n"
    "v_mfma_f32_16x16x32_bf16 v[240:243], v[100:103], a[220:223], v[240:243]\n"
    "s_waitcnt vmcnt(19)\n"
    "v_mfma_f32_16x16x32_bf16 v[160:163], v[104:107], a[208:211], v[160:163]\n"
    "buffer_load_dwordx2 v[46:47], v12, s[8:11], s88 offen offset:1536 sc1\n"
    "v_mfma_f32_16x16x32_bf16 v[188:191], v[104:107], a[212:215], v[188:191]\n"
    "v_mfma_f32_16x16x32_bf16 v[216:219], v[104:107], a[216:219], v[216:219]\n"
    "v_mfma_f32_16x16x32_bf16 v[244:247], v[104:107], a[220:223], v[244:247]\n"
    "s_waitcnt vmcnt(19)\n"
    "v_mfma_f32_16x16x32_bf16 v[164:167], v[108:111], a[208:211], v[164:167]\n"
    "buffer_load_dwordx2 v[48:49], v12, s[8:11], s88 offen offset:2048 sc1\n"
    "v_mfma_f32_16x16x32_bf16 v[192:195], v[108:111], a[212:215], v[192:195]\n"
    "v_mfma_f32_16x16x32_bf16 v[220:223], v[108:111], a[216:219], v[220:223]\n"
    "v_mfma_f32_16x16x32_bf16 v[248:251], v[108:111], a[220:223], v[248:251]\n"
    "s_waitcnt vmcnt(19)\n"
    "v_mfma_f32_16x16x32_bf16 v[168:171], v[112:115], a[208:211], v[168:171]\n"
    "buffer_load_dwordx2 v[50:51], v12, s[8:11], s88 offen offset:2560 sc1\n"
    "v_mfma_f32_16x16x32_bf16 v[196:199], v[112:115], a[212:215], v[196:199]\n"
    "v_mfma_f32_16x16x32_bf16 v[224:227], v[112:115], a[216:219], v[224:227]\n"
    "v_mfma_f32_16x16x32_bf16 v[252:255], v[112:115], a[220:223], v[252:255]\n"
    ST(45)
    "s_waitcnt vmcnt(19)\n"
    "v_mfma_f32_16x16x32_bf16 v[172:175], v[116:119], a[228:231], v[172:175]\n"
    "s_waitcnt vmcnt(18)\n"
    "v_mfma_f32_16x16x32_bf16 v[176:179], v[120:123], a[228:231], v[176:179]\n"
    "buffer_load_dwordx2 v[52:53], v12, s[8:11], s89 offen offset:0 sc1\n"
    "s_waitcnt vmcnt(18)\n"
    "v_mfma_f32_16x16x32_bf16 v[180:183], v[124:127], a[228:231], v[180:183]\n"
    "s_waitcnt vmcnt(17)\n"
    "v_mfma_f32_16x16x32_bf16 v[184:187], v[128:131], a[228:231], v[184:187]\n"
    "s_waitcnt vmcnt(16)\n"
    "v_mfma_f32_16x16x32_bf16 v[188:191], v[132:135], a[228:231], v[188:191]\n"
    "s_waitcnt vmcnt(15)\n"
    "v_mfma_f32_16x16x32_bf16 v[192:195], v[136:139], a[228:231], v[192:195]\n"
    "buffer_load_ushort v57, v18, s[40:43], 0 offen\n"
    "s_waitcnt vmcnt(15)\n"
    "v_mfma_f32_16x16x32_bf16 v[196:199], v[140:143], a[228:231], v[196:199]\n"
    "s_waitcnt vmcnt(14)\n"
    "v_mfma_f32_16x16x32_bf16 v[172:175], v[60:63], a[244:247], v[172:175]\n"
    "s_waitcnt vmcnt(13)\n"
    "v_mfma_f32_16x16x32_bf16 v[176:179], v[64:67], a[244:247], v[176:179]\n"
    "s_waitcnt vmcnt(12)\n"
    "v_mfma_f32_16x16x32_bf16 v[180:183], v[68:71], a[244:247], v[180:183]\n"
    "buffer_load_dwordx4 a[0:3], v1, s[8:11], s86 offen offset:0 sc1\n"
    "s_waitcnt vmcnt(12)\n"
    "v_mfma_f32_16x16x32_bf16 v[184:187], v[72:75], a[244:247], v[184:187]\n"
    "s_waitcnt vmcnt(11)\n"
    "v_mfma_f32_16x16x32_bf16 v[188:191], v[76:79], a[244:247], v[188:191]\n"
    "s_waitcnt vmcnt(10)\n"
    "v_mfma_f32_16x16x32_bf16 v[192:195], v[80:83], a[244:247], v[192:195]\n"
    "s_waitcnt vmcnt(9)\n"
    "v_mfma_f32_16x16x32_bf16 v[196:199], v[84:87], a[244:247], v[196:199]\n"
    "buffer_load_dwordx4 a[4:7], v1, s[8:11], s53 offen offset:0 sc1\n"
    "v_mfma_f32_16x16x32_bf16 v[200:203], v[116:119], a[232:235], v[200:203]\n"
    "v_mfma_f32_16x16x32_bf16 v[204:207], v[120:123], a[232:235], v[204:207]\n"
    "v_mfma_f32_16x16x32_bf16 v[208:211], v[124:127], a[232:235], v[208:211]\n"
    "ds_write_b128 v19, v[172:175] offset:0\n"
    "v_mfma_f32_16x16x32_bf16 v[212:215], v[128:131], a[232:235], v[212:215]\n"
    "buffer_load_dwordx4 a[8:11], v1, s[8:11], s86 offen offset:1024 sc1\n"
    "v_mfma_f32_16x16x32_bf16 v[216:219], v[132:135], a[232:235], v[216:219]\n"
    "v_mfma_f32_16x16x32_bf16 v[220:223], v[136:139], a[232:235], v[220:223]\n"
    "ds_write_b128 v19, v[176:179] offset:1024\n"
    "v_mfma_f32_16x16x32_bf16 v[224:227], v[140:143], a[232:235], v[224:227]\n"
    "v_mfma_f32_16x16x32_bf16 v[200:203], v[60:63], a[248:251], v[200:203]\n"
    "buffer_load_dwordx4 a[12:15], v1, s[8:11], s53 offen offset:1024 sc1\n"
    "v_mfma_f32_16x16x32_bf16 v[204:207], v[64:67], a[248:251], v[204:207]\n"
    "ds_write_b128 v19, v[180:183] offset:2048\n"
    "v_mfma_f32_16x16x32_bf16 v[208:211], v[68:71], a[248:251], v[208:211]\n"
    "v_mfma_f32_16x16x32_bf16 v[212:215], v[72:75], a[248:251], v[212:215]\n"
    "v_mfma_f32_16x16x32_bf16 v[216:219], v[76:79], a[248:251], v[216:219]\n"
    "ds_write_b128 v19, v[184:187] offset:3072\n"
    "v_mfma_f32_16x16x32_bf16 v[220:223], v[80:83], a[248:251], v[220:223]\n"
    "v_mfma_f32_16x16x32_bf16 v[224:227], v[84:87], a[248:251], v[224:227]\n"
    "v_mfma_f32_16x16x32_bf16 v[228:231], v[116:119], a[236:239], v[228:231]\n"
    "ds_write_b128 v19, v[188:191] offset:4096\n"
    "v_mfma_f32_16x16x32_bf16 v[232:235], v[120:123], a[236:239], v[232:235]\n"
    "buffer_load_dwordx4 a[16:19], v1, s[8:11], s86 offen offset:2048 sc1\n"
    "v_mfma_f32_16x16x32_bf16 v[236:239], v[124:127], a[236:239], v[236:239]\n"
    "v_mfma_f32_16x16x32_bf16 v[240:243], v[128:131], a[236:239], v[240:243]\n"
    "ds_write_b128 v19, v[192:195] offset:5120\n"
    "v_mfma_f32_16x16x32_bf16 v[244:247], v[132:135], a[236:239], v[244:247]\n"
    "v_mfma_f32_16x16x32_bf16 v[248:251], v[136:139], a[236:239], v[248:251]\n"
    "buffer_load_dwordx4 a[20:23], v1, s[8:11], s53 offen offset:2048 sc1\n"
    "v_mfma_f32_16x16x32_bf16 v[252:255], v[140:143], a[236:239], v[252:255]\n"
    "ds_write_b128 v19, v[196:199] offset:6144\n"
    "v_mfma_f32_16x16x32_bf16 v[228:231], v[60:63], a[252:255], v[228:231]\n"
    "v_mfma_f32_16x16x32_bf16 v[232:235], v[64:67], a[252:255], v[232:235]\n"
    "v_mfma_f32_16x16x32_bf16 v[236:239], v[68:71], a[252:255], v[236:239]\n"
    "ds_write_b128 v20, v[200:203] offset:0\n"
    "v_mfma_f32_16x16x32_bf16 v[240:243], v[72:75], a[252:255], v[240:243]\n"
    "v_mfma_f32_16x16x32_bf16 v[244:247], v[76:79], a[252:255], v[244:247]\n"
    "v_mfma_f32_16x16x32_bf16 v[248:251], v[80:83], a[252:255], v[248:251]\n"
    "ds_write_b128 v20, v[204:207] offset:1024\n"
    "v_mfma_f32_16x16x32_bf16 v[252:255], v[84:87], a[252:255], v[252:255]\n"
    "buffer_load_dwordx4 a[24:27], v1, s[8:11], s86 offen offset:3072 sc1\n"
    "v_mfma_f32_16x16x32_bf16 v[144:147], v[116:119], a[224:227], v[144:147]\n"
    "v_mfma_f32_16x16x32_bf16 v[148:151], v[120:123], a[224:227], v[148:151]\n"
    "ds_write_b128 v20, v[208:211] offset:2048\n"
    "v_mfma_f32_16x16x32_bf16 v[152:155], v[124:127], a[224:227], v[152:155]\n"
    "v_mfma_f32_16x16x32_bf16 v[156:159], v[128:131], a[224:227], v[156:159]\n"
    "buffer_load_dwordx4 a[28:31], v1, s[8:11], s53 offen offset:3072 sc1\n"
    "v_mfma_f32_16x16x32_bf16 v[160:163], v[132:135], a[224:227], v[160:163]\n"
    "ds_write_b128 v20, v[212:215] offset:3072\n"
    "v_mfma_f32_16x16x32_bf16 v[164:167], v[136:139], a[224:227], v[164:167]\n"
    "v_mfma_f32_16x16x32_bf16 v[168:171], v[140:143], a[224:227], v[168:171]\n"
    "v_mfma_f32_16x16x32_bf16 v[144:147], v[60:63], a[240:243], v[144:147]\n"
    "ds_write_b128 v20, v[216:219] offset:4096\n"
    "v_mfma_f32_16x16x32_bf16 v[148:151], v[64:67], a[240:243], v[148:151]\n"
    "v_mfma_f32_16x16x32_bf16 v[152:155], v[68:71], a[240:243], v[152:155]\n"
    "v_mfma_f32_16x16x32_bf16 v[156:159], v[72:75], a[240:243], v[156:159]\n"
    "ds_write_b128 v20, v[220:223] offset:5120\n"
    "v_mfma_f32_16x16x32_bf16 v[160:163], v[76:79], a[240:243], v[160:163]\n"
    "buffer_load_dwordx4 a[32:35], v16, s[8:11], s86 offen offset:0 sc1\n"
    "v_mfma_f32_16x16x32_bf16 v[164:167], v[80:83], a[240:243], v[164:167]\n"
    "v_mfma_f32_16x16x32_bf16 v[168:171], v[84:87], a[240:243], v[168:171]\n"
    "ds_write_b128 v20, v[224:227] offset:6144\n"
    ST(47)
    "s_waitcnt vmcnt(9)\n"
    "ds_write_b128 v21, v[228:231] offset:0\n"
    "v_lshlrev_b32_e64 v58, 16, v57\n"
    "v_pk_add_f32 v[144:145], v[144:145], v[58:59] op_sel_hi:[1,0]\n"
    "v_pk_add_f32 v[146:147], v[146:147], v[58:59] op_sel_hi:[1,0]\n"
    "v_lshlrev_b32_e64 v172, 16, v40\n"
    "v_and_b32 v173, 0xffff0000, v40\n"
    "v_lshlrev_b32_e64 v174, 16, v41\n"
    "ds_write_b128 v21, v[232:235] offset:1024\n"
    "v_and_b32 v175, 0xffff0000, v41\n"
    "v_pk_add_f32 v[148:149], v[148:149], v[58:59] op_sel_hi:[1,0]\n"
    "v_pk_add_f32 v[150:151], v[150:151], v[58:59] op_sel_hi:[1,0]\n"
    "v_lshlrev_b32_e64 v176, 16, v42\n"
    "v_and_b32 v177, 0xffff0000, v42\n"
    "v_lshlrev_b32_e64 v178, 16, v43\n"
    "ds_write_b128 v21, v[236:239] offset:2048\n"
    "v_and_b32 v179, 0xffff0000, v43\n"
    "v_pk_add_f32 v[152:153], v[152:153], v[58:59] op_sel_hi:[1,0]\n"
    "v_pk_add_f32 v[154:155], v[154:155], v[58:59] op_sel_hi:[1,0]\n"
    "v_lshlrev_b32_e64 v180, 16, v44\n"
    "v_and_b32 v181, 0xffff0000, v44\n"
    "v_lshlrev_b32_e64 v182, 16, v45\n"
    "ds_write_b128 v21, v[240:243] offset:3072\n"
    "v_and_b32 v183, 0xffff0000, v45\n"
    "v_pk_add_f32 v[156:157], v[156:157], v[58:59] op_sel_hi:[1,0]\n"
    "v_pk_add_f32 v[158:159], v[158:159], v[58:59] op_sel_hi:[1,0]\n"
    "v_lshlrev_b32_e64 v184, 16, v46\n"
    "v_and_b32 v185, 0xffff0000, v46\n"
    "v_lshlrev_b32_e64 v186, 16, v47\n"
    "ds_write_b128 v21, v[244:247] offset:4096\n"
    "v_and_b32 v187, 0xffff0000, v47\n"
    "v_pk_add_f32 v[160:161], v[160:161], v[58:59] op_sel_hi:[1,0]\n"
    "v_pk_add_f32 v[162:163], v[162:163], v[58:59] op_sel_hi:[1,0]\n"
    "v_lshlrev_b32_e64 v188, 16, v48\n"
    "v_and_b32 v189, 0xffff0000, v48\n"
    "v_lshlrev_b32_e64 v190, 16, v49\n"
    "ds_write_b128 v21, v[248:251] offset:5120\n"
    "v_and_b32 v191, 0xffff0000, v49\n"
    "v_pk_add_f32 v[164:165], v[164:165], v[58:59] op_sel_hi:[1,0]\n"
    "v_pk_add_f32 v[166:167], v[166:167], v[58:59] op_sel_hi:[1,0]\n"
    "v_lshlrev_b32_e64 v192, 16, v50\n"
    "v_and_b32 v193, 0xffff0000, v50\n"
    "v_lshlrev_b32_e64 v194, 16, v51\n"
    "ds_write_b128 v21, v[252:255] offset:6144\n"
    "v_and_b32 v195, 0xffff0000, v51\n"
    "v_pk_add_f32 v[168:169], v[168:169], v[58:59] op_sel_hi:[1,0]\n"
    "v_pk_add_f32 v[170:171], v[170:171], v[58:59] op_sel_hi:[1,0]\n"
    "v_lshlrev_b32_e64 v196, 16, v52\n"
    "v_and_b32 v197, 0xffff0000, v52\n"
    "v_lshlrev_b32_e64 v198, 16, v53\n"
    "v_and_b32 v199, 0xffff0000, v53\n"
    "s_waitcnt lgkmcnt(0)\n"
    ST(9)
    "s_barrier\n"
    "ds_read_b128 v[60:63], v22 offset:0\n"
    "ds_read_b128 v[64:67], v22 offset:7168\n"
    "ds_read_b128 v[68:71], v22 offset:14336\n"
    "ds_read_b128 v[72:75], v22 offset:1024\n"
    "ds_read_b128 v[76:79], v22 offset:8192\n"
    "ds_read_b128 v[80:83], v22 offset:15360\n"
    "ds_read_b128 v[84:87], v22 offset:2048\n"
    "ds_read_b128 v[88:91], v22 offset:9216\n"
    "ds_read_b128 v[92:95], v22 offset:16384\n"
    "ds_read_b128 v[96:99], v22 offset:3072\n"
    "ds_read_b128 v[100:103], v22 offset:10240\n"
    "ds_read_b128 v[104:107], v22 offset:17408\n"
    "ds_read_b128 v[108:111], v22 offset:4096\n"
    "ds_read_b128 v[112:115], v22 offset:11264\n"
    "ds_read_b128 v[116:119], v22 offset:18432\n"
    "s_waitcnt lgkmcnt(12)\n"
    "s_nop 0\n"
    "ds_read_b128 v[120:123], v22 offset:5120\n"
    "ds_read_b128 v[124:127], v22 offset:12288\n"
    "ds_read_b128 v[128:131], v22 offset:19456\n"
    "v_pk_add_f32 v[60:61], v[60:61], v[64:65]\n"
    "v_pk_add_f32 v[62:63], v[62:63], v[66:67]\n"
    "v_pk_add_f32 v[144:145], v[144:145], v[68:69]\n"
    "v_pk_add_f32 v[146:147], v[146:147], v[70:71]\n"
    "v_pk_add_f32 v[144:145], v[144:145], v[60:61]\n"
    "v_pk_add_f32 v[146:147], v[146:147], v[62:63]\n"
    "v_pk_mul_f32 v[144:145], v[144:145], v[172:173]\n"
    "v_pk_mul_f32 v[146:147], v[146:147], v[174:175]\n"
    "v_cvt_pk_bf16_f32 v200, v144, v145\n"
    "v_cvt_pk_bf16_f32 v201, v146, v147\n"
    "s_waitcnt lgkmcnt(12)\n"
    "s_nop 0\n"
    "ds_read_b128 v[132:135], v22 offset:6144\n"
    "ds_read_b128 v[136:139], v22 offset:13312\n"
    "ds_read_b128 v[140:143], v22 offset:20480\n"
    "v_pk_add_f32 v[72:73], v[72:73], v[76:77]\n"
    "v_pk_add_f32 v[74:75], v[74:75], v[78:79]\n"
    "v_pk_add_f32 v[148:149], v[148:149], v[80:81]\n"
    "v_pk_add_f32 v[150:151], v[150:151], v[82:83]\n"
    "v_pk_add_f32 v[148:149], v[148:149], v[72:73]\n"
    "v_pk_add_f32 v[150:151], v[150:151], v[74:75]\n"
    "v_pk_mul_f32 v[148:149], v[148:149], v[176:177]\n"
    "v_pk_mul_f32 v[150:151], v[150:151], v[178:179]\n"
    "v_cvt_pk_bf16_f32 v202, v148, v149\n"
    "v_cvt_pk_bf16_f32 v203, v150, v151\n"
    "buffer_store_dwordx4 v[200:203], v1, s[8:11], s90 offen offset:0 sc1\n"
    "s_waitcnt lgkmcnt(12)\n"
    "s_nop 0\n"
    "v_pk_add_f32 v[84:85], v[84:85], v[88:89]\n"
    "v_pk_add_f32 v[86:87], v[86:87], v[90:91]\n"
    "v_pk_add_f32 v[152:153], v[152:153], v[92:93]\n"
    "v_pk_add_f32 v[154:155], v[154:155], v[94:95]\n"
    "v_pk_add_f32 v[152:153], v[152:153], v[84:85]\n"
    "v_pk_add_f32 v[154:155], v[154:155], v[86:87]\n"
    "v_pk_mul_f32 v[152:153], v[152:153], v[180:181]\n"
    "v_pk_mul_f32 v[154:155], v[154:155], v[182:183]\n"
    "v_cvt_pk_bf16_f32 v204, v152, v153\n"
    "v_cvt_pk_bf16_f32 v205, v154, v155\n"
    "s_waitcnt lgkmcnt(9)\n"
    "s_nop 0\n"
    "v_pk_add_f32 v[96:97], v[96:97], v[100:101]\n"
    "v_pk_add_f32 v[98:99], v[98:99], v[102:103]\n"
    "v_pk_add_f32 v[156:157], v[156:157], v[104:105]\n"
    "v_pk_add_f32 v[158:159], v[158:159], v[106:107]\n"
    "v_pk_add_f32 v[156:157], v[156:157], v[96:97]\n"
    "v_pk_add_f32 v[158:159], v[158:159], v[98:99]\n"
    "v_pk_mul_f32 v[156:157], v[156:157], v[184:185]\n"
    "v_pk_mul_f32 v[158:159], v[158:159], v[186:187]\n"
    "v_cvt_pk_bf16_f32 v206, v156, v157\n"
    "v_cvt_pk_bf16_f32 v207, v158, v159\n"
    "buffer_store_dwordx4 v[204:207], v1, s[8:11], s90 offen offset:1024 sc1\n"
    "s_waitcnt lgkmcnt(6)\n"
    "s_nop 0\n"
    "v_pk_add_f32 v[108:109], v[108:109], v[112:113]\n"
    "v_pk_add_f32 v[110:111], v[110:111], v[114:115]\n"
    "v_pk_add_f32 v[160:161], v[160:161], v[116:117]\n"
    "v_pk_add_f32 v[162:163], v[162:163], v[118:119]\n"
    "v_pk_add_f32 v[160:161], v[160:161], v[108:109]\n"
    "v_pk_add_f32 v[162:163], v[162:163], v[110:111]\n"
    "v_pk_mul_f32 v[160:161], v[160:161], v[188:189]\n"
    "v_pk_mul_f32 v[162:163], v[162:163], v[190:191]\n"
    "v_cvt_pk_bf16_f32 v208, v160, v161\n"
    "v_cvt_pk_bf16_f32 v209, v162, v163\n"
    "s_waitcnt lgkmcnt(3)\n"
    "s_nop 0\n"
    "v_pk_add_f32 v[120:121], v[120:121], v[124:125]\n"
    "v_pk_add_f32 v[122:123], v[122:123], v[126:127]\n"
    "v_pk_add_f32 v[164:165], v[164:165], v[128:129]\n"
    "v_pk_add_f32 v[166:167], v[166:167], v[130:131]\n"
    "v_pk_add_f32 v[164:165], v[164:165], v[120:121]\n"
    "v_pk_add_f32 v[166:167], v[166:167], v[122:123]\n"
    "v_pk_mul_f32 v[164:165], v[164:165], v[192:193]\n"
    "v_pk_mul_f32 v[166:167], v[166:167], v[194:195]\n"
    "v_cvt_pk_bf16_f32 v210, v164, v165\n"
    "v_cvt_pk_bf16_f32 v211, v166, v167\n"
    "buffer_store_dwordx4 v[208:211], v1, s[8:11], s90 offen offset:2048 sc1\n"
    "s_waitcnt lgkmcnt(0)\n"
    "s_nop 0\n"
    "v_pk_add_f32 v[132:133], v[132:133], v[136:137]\n"
    "v_pk_add_f32 v[134:135], v[134:135], v[138:139]\n"
    "v_pk_add_f32 v[168:169], v[168:169], v[140:141]\n"
    "v_pk_add_f32 v[170:171], v[170:171], v[142:143]\n"
    "v_pk_add_f32 v[168:169], v[168:169], v[132:133]\n"
    "v_pk_add_f32 v[170:171], v[170:171], v[134:135]\n"
    "v_pk_mul_f32 v[168:169], v[168:169], v[196:197]\n"
    "v_pk_mul_f32 v[170:171], v[170:171], v[198:199]\n"
    "v_cvt_pk_bf16_f32 v212, v168, v169\n"
    "v_cvt_pk_bf16_f32 v213, v170, v171\n"
    "buffer_store_dwordx2 v[212:213], v1, s[8:11], s91 offen sc1\n"
    ST(10)
    "s_barrier\n"
    "s_nop 0\n"
    "buffer_load_dwordx4 a[36:39], v16, s[8:11], s53 offen offset:0 sc1\n"
    "ds_read_b64_tr_b16 v[48:49], v26 offset:0\n"
    "ds_read_b64_tr_b16 v[50:51], v28 offset:512\n"
    "ds_read_b64_tr_b16 v[52:53], v27 offset:0\n"
    "ds_read_b64_tr_b16 v[54:55], v29 offset:512\n"
    "ds_read_b64_tr_b16 v[56:57], v28 offset:0\n"
    "ds_read_b64_tr_b16 v[58:59], v26 offset:512\n"
    "buffer_load_dwordx4 a[40:43], v16, s[8:11], s86 offen offset:1024 sc1\n"
    "ds_read_b64_tr_b16 v[60:61], v29 offset:0\n"
    "ds_read_b64_tr_b16 v[62:63], v27 offset:512\n"
    "ds_read_b64_tr_b16 v[64:65], v26 offset:4096\n"
    "ds_read_b64_tr_b16 v[66:67], v28 offset:4608\n"
    "ds_read_b64_tr_b16 v[68:69], v27 offset:4096\n"
    "ds_read_b64_tr_b16 v[70:71], v29 offset:4608\n"
    "buffer_load_dwordx4 a[44:47], v16, s[8:11], s53 offen offset:1024 sc1\n"
    "ds_read_b64_tr_b16 v[72:73], v28 offset:4096\n"
    "ds_read_b64_tr_b16 v[74:75], v26 offset:4608\n"
    "ds_read_b64_tr_b16 v[76:77], v29 offset:4096\n"
    "ds_read_b64_tr_b16 v[78:79], v27 offset:4608\n"
    "ds_read_b64_tr_b16 v[80:81], v26 offset:8192\n"
    "ds_read_b64_tr_b16 v[82:83], v28 offset:8704\n"
    "ds_read_b64_tr_b16 v[84:85], v27 offset:8192\n"
    "buffer_load_dwordx4 a[48:51], v16, s[8:11], s86 offen offset:2048 sc1\n"
    "ds_read_b64_tr_b16 v[86:87], v29 offset:8704\n"
    "ds_read_b64_tr_b16 v[88:89], v28 offset:8192\n"
    "ds_read_b64_tr_b16 v[90:91], v26 offset:8704\n"
    "ds_read_b64_tr_b16 v[92:93], v29 offset:8192\n"
    "ds_read_b64_tr_b16 v[94:95], v27 offset:8704\n"
    "ds_read_b64_tr_b16 v[96:97], v26 offset:12288\n"
    "buffer_load_dwordx4 a[52:55], v16, s[8:11], s53 offen offset:2048 sc1\n"
    "ds_read_b64_tr_b16 v[98:99], v28 offset:12800\n"
    "ds_read_b64_tr_b16 v[100:101], v27 offset:12288\n"
    "ds_read_b64_tr_b16 v[102:103], v29 offset:12800\n"
    "ds_read_b64_tr_b16 v[104:105], v28 offset:12288\n"
    "ds_read_b64_tr_b16 v[106:107], v26 offset:12800\n"
    "ds_read_b64_tr_b16 v[108:109], v29 offset:12288\n"
    "ds_read_b64_tr_b16 v[110:111], v27 offset:12800\n"
    ST(48)
    "s_waitcnt vmcnt(5)\n"
    ST(49)
    "s_nop 0\n"
    "buffer_store_dword v13, v38, s[8:11], 0 offen sc1\n"
    "buffer_load_dwordx4 a[56:59], v16, s[8:11], s86 offen offset:3072 sc1\n"
    "ds_read_b64_tr_b16 a[64:65], v26 offset:16384\n"
    "buffer_load_dwordx4 a[60:63], v16, s[8:11], s53 offen offset:3072 sc1\n"
    "ds_read_b64_tr_b16 a[66:67], v28 offset:16896\n"
    "ds_read_b64_tr_b16 a[68:69], v27 offset:16384\n"
    "s_add_u32 s70, s87, 0\n"
    "s_add_u32 m0, s48, 0\n"
    "s_nop 0\n"
    "buffer_load_dwordx4 v8, s[28:31], s70 offen offset:0 lds\n"
    "ds_read_b64_tr_b16 a[70:71], v29 offset:16896\n"
    "ds_read_b64_tr_b16 a[72:73], v28 offset:16384\n"
    "s_add_u32 m0, s48, 1024\n"
    "s_nop 0\n"
    "buffer_load_dwordx4 v9, s[28:31], s70 offen offset:0 lds\n"
    "ds_read_b64_tr_b16 a[74:75], v26 offset:16896\n"
    "ds_read_b64_tr_b16 a[76:77], v29 offset:16384\n"
    "s_add_u32 s70, s87, 458752\n"
    "s_add_u32 m0, s48, 57344\n"
    "s_nop 0\n"
    "buffer_load_dwordx4 v8, s[28:31], s70 offen offset:0 lds\n"
    "ds_read_b64_tr_b16 a[78:79], v27 offset:16896\n"
    "s_add_u32 m0, s48, 58368\n"
    "s_nop 0\n"
    "buffer_load_dwordx4 v9, s[28:31], s70 offen offset:0 lds\n"
    "ds_read_b64_tr_b16 a[80:81], v26 offset:20480\n"
    "ds_read_b64_tr_b16 a[82:83], v28 offset:20992\n"
    "s_add_u32 s70, s87, 65536\n"
    "s_add_u32 m0, s48, 8192\n"
    "s_nop 0\n"
    "buffer_load_dwordx4 v8, s[28:31], s70 offen offset:0 lds\n"
    "ds_read_b64_tr_b16 a[84:85], v27 offset:20480\n"
    "ds_read_b64_tr_b16 a[86:87], v29 offset:20992\n"
    "s_add_u32 m0, s48, 9216\n"
    "s_nop 0\n"
    "buffer_load_dwordx4 v9, s[28:31], s70 offen offset:0 lds\n"
    "ds_read_b64_tr_b16 a[88:89], v28 offset:20480\n"
    "ds_read_b64_tr_b16 a[90:91], v26 offset:20992\n"
    "s_add_u32 s70, s87, 524288\n"
    "s_add_u32 m0, s48, 65536\n"
    "s_nop 0\n"
    "buffer_load_dwordx4 v8, s[28:31], s70 offen offset:0 lds\n"
    "ds_read_b64_tr_b16 a[92:93], v29 offset:20480\n"
    "ds_read_b64_tr_b16 a[94:95], v27 offset:20992\n"
    "s_add_u32 m0, s48, 66560\n"
    "s_nop 0\n"
    "buffer_load_dwordx4 v9, s[28:31], s70 offen offset:0 lds\n"
    "ds_read_b64_tr_b16 a[96:97], v26 offset:24576\n"
    "s_add_u32 s70, s87, 131072\n"
    "s_add_u32 m0, s48, 16384\n"
    "s_nop 0\n"
    "buffer_load_dwordx4 v8, s[28:31], s70 offen offset:0 lds\n"
    "ds_read_b64_tr_b16 a[98:99], v28 offset:25088\n"
    "ds_read_b64_tr_b16 a[100:101], v27 offset:24576\n"
    "s_add_u32 m0, s48, 17408\n"
    "s_nop 0\n"
    "buffer_load_dwordx4 v9, s[28:31], s70 offen offset:0 lds\n"
    "ds_read_b64_tr_b16 a[102:103], v29 offset:25088\n"
    "ds_read_b64_tr_b16 a[104:105], v28 offset:24576\n"
    "s_add_u32 s70, s87, 589824\n"
    "s_add_u32 m0, s48, 73728\n"
    "s_nop 0\n"
    "buffer_load_dwordx4 v8, s[28:31], s70 offen offset:0 lds\n"
    "ds_read_b64_tr_b16 a[106:107], v26 offset:25088\n"
    "ds_read_b64_tr_b16 a[108:109], v29 offset:24576\n"
    "s_add_u32 m0, s48, 74752\n"
    "s_nop 0\n"
    "buffer_load_dwordx4 v9, s[28:31], s70 offen offset:0 lds\n"
    "ds_read_b64_tr_b16 a[110:111], v27 offset:25088\n"
    "s_add_u32 s70, s87, 196608\n"
    "s_add_u32 m0, s48, 24576\n"
    "s_nop 0\n"
    "buffer_load_dwordx4 v8, s[28:31], s70 offen offset:0 lds\n"
    "ds_read_b64_tr_b16 a[112:113], v26 offset:28672\n"
    "ds_read_b64_tr_b16 a[114:115], v28 offset:29184\n"
    "s_add_u32 m0, s48, 25600\n"
    "s_nop 0\n"
    "buffer_load_dwordx4 v9, s[28:31], s70 offen offset:0 lds\n"
    "ds_read_b64_tr_b16 a[116:117], v27 offset:28672\n"
    "ds_read_b64_tr_b16 a[118:119], v29 offset:29184\n"
    "s_add_u32 s70, s87, 655360\n"
    "s_add_u32 m0, s48, 81920\n"
    "s_nop 0\n"
    "buffer_load_dwordx4 v8, s[28:31], s70 offen offset:0 lds\n"
    "ds_read_b64_tr_b16 a[120:121], v28 offset:28672\n"
    "ds_read_b64_tr_b16 a[122:123], v26 offset:29184\n"
    "s_add_u32 m0, s48, 82944\n"
    "s_nop 0\n"
    "buffer_load_dwordx4 v9, s[28:31], s70 offen offset:0 lds\n"
    "ds_read_b64_tr_b16 a[124:125], v29 offset:28672\n"
    "ds_read_b64_tr_b16 a[126:127], v27 offset:29184\n"
    "s_waitcnt vmcnt(16) lgkmcnt(0)\n"
    ST(50)
    "s_barrier\n"
    "v_mfma_f32_16x16x32_bf16 v[224:227], v[48:51], a[0:3], 0\n"
    "v_mfma_f32_16x16x32_bf16 v[228:231], v[52:55], a[0:3], 0\n"
    "s_add_u32 s70, s87, 262144\n"
    "s_add_u32 m0, s48, 32768\n"
    "s_nop 0\n"
    "buffer_load_dwordx4 v8, s[28:31], s70 offen offset:0 lds\n"
    "v_mfma_f32_16x16x32_bf16 v[232:235], v[56:59], a[0:3], 0\n"
    "v_mfma_f32_16x16x32_bf16 v[236:239], v[60:63], a[0:3], 0\n"
    "s_add_u32 m0, s48, 33792\n"
    "s_nop 0\n"
    "buffer_load_dwordx4 v9, s[28:31], s70 offen offset:0 lds\n"
    "v_mfma_f32_16x16x32_bf16 v[240:243], v[48:51], a[4:7], 0\n"
    "v_mfma_f32_16x16x32_bf16 v[244:247], v[52:55], a[4:7], 0\n"
    "s_add_u32 s70, s87, 720896\n"
    "s_add_u32 m0, s48, 90112\n"
    "s_nop 0\n"
    "buffer_load_dwordx4 v8, s[28:31], s70 offen offset:0 lds\n"
    "v_mfma_f32_16x16x32_bf16 v[248:251], v[56:59], a[4:7], 0\n"
    "v_mfma_f32_16x16x32_bf16 v[252:255], v[60:63], a[4:7], 0\n"
    "s_add_u32 m0, s48, 91136\n"
    "s_nop 0\n"
    "buffer_load_dwordx4 v9, s[28:31], s70 offen offset:0 lds\n"
    "v_mfma_f32_16x16x32_bf16 v[224:227], v[64:67], a[8:11], v[224:227]\n"
    "v_mfma_f32_16x16x32_bf16 v[228:231], v[68:71], a[8:11], v[228:231]\n"
    "s_add_u32 s70, s87, 327680\n"
    "s_add_u32 m0, s48, 40960\n"
    "s_nop 0\n"
    "buffer_load_dwordx4 v8, s[28:31], s70 offen offset:0 lds\n"
    "v_mfma_f32_16x16x32_bf16 v[232:235], v[72:75], a[8:11], v[232:235]\n"
    "v_mfma_f32_16x16x32_bf16 v[236:239], v[76:79], a[8:11], v[236:239]\n"
    "s_add_u32 m0, s48, 41984\n"
    "s_nop 0\n"
    "buffer_load_dwordx4 v9, s[28:31], s70 offen offset:0 lds\n"
    "v_mfma_f32_16x16x32_bf16 v[240:243], v[64:67], a[12:15], v[240:243]\n"
    "v_mfma_f32_16x16x32_bf16 v[244:247], v[68:71], a[12:15], v[244:247]\n"
    "s_add_u32 s70, s87, 786432\n"
    "s_add_u32 m0, s48, 98304\n"
    "s_nop 0\n"
    "buffer_load_dwordx4 v8, s[28:31], s70 offen offset:0 lds\n"
    "v_mfma_f32_16x16x32_bf16 v[248:251], v[72:75], a[12:15], v[248:251]\n"
    "v_mfma_f32_16x16x32_bf16 v[252:255], v[76:79], a[12:15], v[252:255]\n"
    "s_add_u32 m0, s48, 99328\n"
    "s_nop 0\n"
    "buffer_load_dwordx4 v9, s[28:31], s70 offen offset:0 lds\n"
    "v_mfma_f32_16x16x32_bf16 v[224:227], v[80:83], a[16:19], v[224:227]\n"
    "v_mfma_f32_16x16x32_bf16 v[228:231], v[84:87], a[16:19], v[228:231]\n"
    "buffer_load_dword v39, v25, s[8:11], 0 offen sc1\n"
    "v_mfma_f32_16x16x32_bf16 v[232:235], v[88:91], a[16:19], v[232:235]\n"
    "v_mfma_f32_16x16x32_bf16 v[236:239], v[92:95], a[16:19], v[236:239]\n"
    "s_add_u32 s70, s87, 393216\n"
    "s_add_u32 m0, s48, 49152\n"
    "s_nop 0\n"
    "buffer_load_dwordx4 v8, s[28:31], s70 offen offset:0 lds\n"
    "v_mfma_f32_16x16x32_bf16 v[240:243], v[80:83], a[20:23], v[240:243]\n"
    "v_mfma_f32_16x16x32_bf16 v[244:247], v[84:87], a[20:23], v[244:247]\n"
    "s_add_u32 m0, s48, 50176\n"
    "s_nop 0\n"
    "buffer_load_dwordx4 v9, s[28:31], s70 offen offset:0 lds\n"
    "v_mfma_f32_16x16x32_bf16 v[248:251], v[88:91], a[20:23], v[248:251]\n"
    "v_mfma_f32_16x16x32_bf16 v[252:255], v[92:95], a[20:23], v[252:255]\n"
    "s_add_u32 s70, s87, 851968\n"
    "s_add_u32 m0, s48, 106496\n"
    "s_nop 0\n"
    "buffer_load_dwordx4 v8, s[28:31], s70 offen offset:0 lds\n"
    "v_mfma_f32_16x16x32_bf16 v[224:227], v[96:99], a[24:27], v[224:227]\n"
    "v_mfma_f32_16x16x32_bf16 v[228:231], v[100:103], a[24:27], v[228:231]\n"
    "s_add_u32 m0, s48, 107520\n"
    "s_nop 0\n"
    "buffer_load_dwordx4 v9, s[28:31], s70 offen offset:0 lds\n"
    "v_mfma_f32_16x16x32_bf16 v[232:235], v[104:107], a[24:27], v[232:235]\n"
    "v_mfma_f32_16x16x32_bf16 v[236:239], v[108:111], a[24:27], v[236:239]\n"
    "v_mfma_f32_16x16x32_bf16 v[240:243], v[96:99], a[28:31], v[240:243]\n"
    "buffer_load_dwordx4 v[40:43], v24, s[4:7], 0 offen offset:0\n"
    "v_mfma_f32_16x16x32_bf16 v[244:247], v[100:103], a[28:31], v[244:247]\n"
    "v_mfma_f32_16x16x32_bf16 v[248:251], v[104:107], a[28:31], v[248:251]\n"
    "buffer_load_dwordx4 v[44:47], v24, s[4:7], 0 offen offset:64\n"
    "v_mfma_f32_16x16x32_bf16 v[252:255], v[108:111], a[28:31], v[252:255]\n"
    ST(52)
    "s_waitcnt vmcnt(15)\n"
    "s_barrier\n"
    "ds_read_b64_tr_b16 a[128:129], v30 offset:0\n"
    "ds_read_b64_tr_b16 a[130:131], v30 offset:2048\n"
    "ds_read_b64_tr_b16 a[132:133], v31 offset:0\n"
    "ds_read_b64_tr_b16 a[134:135], v31 offset:2048\n"
    "ds_read_b64_tr_b16 a[136:137], v32 offset:0\n"
    "ds_read_b64_tr_b16 a[138:139], v32 offset:2048\n"
    "ds_read_b64_tr_b16 a[140:141], v33 offset:0\n"
    "ds_read_b64_tr_b16 a[142:143], v33 offset:2048\n"
    "ds_read_b64_tr_b16 a[144:145], v30 offset:4096\n"
    "ds_read_b64_tr_b16 a[146:147], v30 offset:6144\n"
    "ds_read_b64_tr_b16 a[148:149], v31 offset:4096\n"
    "ds_read_b64_tr_b16 a[150:151], v31 offset:6144\n"
    "ds_read_b64_tr_b16 a[152:153], v32 offset:4096\n"
    "ds_read_b64_tr_b16 a[154:155], v32 offset:6144\n"
    "ds_read_b64_tr_b16 a[156:157], v33 offset:4096\n"
    "ds_read_b64_tr_b16 a[158:159], v33 offset:6144\n"
    "ds_read_b64_tr_b16 a[160:161], v30 offset:8192\n"
    "ds_read_b64_tr_b16 a[162:163], v30 offset:10240\n"
    "ds_read_b64_tr_b16 a[164:165], v31 offset:8192\n"
    "ds_read_b64_tr_b16 a[166:167], v31 offset:10240\n"
    "ds_read_b64_tr_b16 a[168:169], v32 offset:8192\n"
    "ds_read_b64_tr_b16 a[170:171], v32 offset:10240\n"
    "ds_read_b64_tr_b16 a[172:173], v33 offset:8192\n"
    "ds_read_b64_tr_b16 a[174:175], v33 offset:10240\n"
    ST(51)
    "s_waitcnt vmcnt(6)\n"
    ST(11)
    "s_mov_b32 s66, 0x10000\n"
    "Lspin_35_%=:\n"
    "v_cmp_ne_u32 vcc, s46, v39\n"
    "s_cbranch_vccz Lok_34_%=\n"
    "buffer_load_dword v39, v25, s[8:11], 0 offen sc1\n"
    "s_waitcnt vmcnt(0)\n"
    "s_sub_u32 s66, s66, 1\n"
    "s_cbranch_scc0 Lspin_35_%=\n"
    "Lok_34_%=:\n"
    ST(6)
    "v_mfma_f32_16x16x32_bf16 v[224:227], a[64:67], a[32:35], v[224:227]\n"
    "ds_read_b64_tr_b16 a[176:177], v30 offset:12288\n"
    "v_mfma_f32_16x16x32_bf16 v[228:231], a[68:71], a[32:35], v[228:231]\n"
    "ds_read_b64_tr_b16 a[178:179], v30 offset:14336\n"
    "v_mfma_f32_16x16x32_bf16 v[232:235], a[72:75], a[32:35], v[232:235]\n"
    "ds_read_b64_tr_b16 a[180:181], v31 offset:12288\n"
    "buffer_load_dwordx4 v[112:115], v1, s[8:11], s92 offen offset:0 sc1\n"
    "v_mfma_f32_16x16x32_bf16 v[236:239], a[76:79], a[32:35], v[236:239]\n"
    "ds_read_b64_tr_b16 a[182:183], v31 offset:14336\n"
    "ds_read_b64_tr_b16 a[184:185], v32 offset:12288\n"
    "v_mfma_f32_16x16x32_bf16 v[240:243], a[64:67], a[36:39], v[240:243]\n"
    "ds_read_b64_tr_b16 a[186:187], v32 offset:14336\n"
    "v_mfma_f32_16x16x32_bf16 v[244:247], a[68:71], a[36:39], v[244:247]\n"
    "ds_read_b64_tr_b16 a[188:189], v33 offset:12288\n"
    "v_mfma_f32_16x16x32_bf16 v[248:251], a[72:75], a[36:39], v[248:251]\n"
    "ds_read_b64_tr_b16 a[190:191], v33 offset:14336\n"
    "buffer_load_dwordx4 v[116:119], v1, s[8:11], s54 offen offset:0 sc1\n"
    "v_mfma_f32_16x16x32_bf16 v[252:255], a[76:79], a[36:39], v[252:255]\n"
    "ds_read_b64_tr_b16 a[192:193], v30 offset:16384\n"
    "ds_read_b64_tr_b16 a[194:195], v30 offset:18432\n"
    "v_mfma_f32_16x16x32_bf16 v[224:227], a[80:83], a[40:43], v[224:227]\n"
    "ds_read_b64_tr_b16 a[196:197], v31 offset:16384\n"
    "v_mfma_f32_16x16x32_bf16 v[228:231], a[84:87], a[40:43], v[228:231]\n"
    "ds_read_b64_tr_b16 a[198:199], v31 offset:18432\n"
    "v_mfma_f32_16x16x32_bf16 v[232:235], a[88:91], a[40:43], v[232:235]\n"
    "ds_read_b64_tr_b16 a[200:201], v32 offset:16384\n"
    "buffer_load_dwordx4 v[120:123], v1, s[8:11], s92 offen offset:1024 sc1\n"
    "v_mfma_f32_16x16x32_bf16 v[236:239], a[92:95], a[40:43], v[236:239]\n"
    "ds_read_b64_tr_b16 a[202:203], v32 offset:18432\n"
    "ds_read_b64_tr_b16 a[204:205], v33 offset:16384\n"
    "v_mfma_f32_16x16x32_bf16 v[240:243], a[80:83], a[44:47], v[240:243]\n"
    "ds_read_b64_tr_b16 a[206:207], v33 offset:18432\n"
    "v_mfma_f32_16x16x32_bf16 v[244:247], a[84:87], a[44:47], v[244:247]\n"
    "ds_read_b64_tr_b16 a[208:209], v30 offset:20480\n"
    "v_mfma_f32_16x16x32_bf16 v[248:251], a[88:91], a[44:47], v[248:251]\n"
    "ds_read_b64_tr_b16 a[210:211], v30 offset:22528\n"
    "buffer_load_dwordx4 v[124:127], v1, s[8:11], s54 offen offset:1024 sc1\n"
    "v_mfma_f32_16x16x32_bf16 v[252:255], a[92:95], a[44:47], v[252:255]\n"
    "ds_read_b64_tr_b16 a[212:213], v31 offset:20480\n"
    "ds_read_b64_tr_b16 a[214:215], v31 offset:22528\n"
    "v_mfma_f32_16x16x32_bf16 v[224:227], a[96:99], a[48:51], v[224:227]\n"
    "ds_read_b64_tr_b16 a[216:217], v32 offset:20480\n"
    "v_mfma_f32_16x16x32_bf16 v[228:231], a[100:103], a[48:51], v[228:231]\n"
    "ds_read_b64_tr_b16 a[218:219], v32 offset:22528\n"
    "v_mfma_f32_16x16x32_bf16 v[232:235], a[104:107], a[48:51], v[232:235]\n"
    "ds_read_b64_tr_b16 a[220:221], v33 offset:20480\n"
    "buffer_load_dwordx4 v[128:131], v1, s[8:11], s92 offen offset:2048 sc1\n"
    "v_mfma_f32_16x16x32_bf16 v[236:239], a[108:111], a[48:51], v[236:239]\n"
    "ds_read_b64_tr_b16 a[222:223], v33 offset:22528\n"
    "ds_read_b64_tr_b16 a[224:225], v30 offset:24576\n"
    "v_mfma_f32_16x16x32_bf16 v[240:243], a[96:99], a[52:55], v[240:243]\n"
    "ds_read_b64_tr_b16 a[226:227], v30 offset:26624\n"
    "v_mfma_f32_16x16x32_bf16 v[244:247], a[100:103], a[52:55], v[244:247]\n"
    "ds_read_b64_tr_b16 a[228:229], v31 offset:24576\n"
    "v_mfma_f32_16x16x32_bf16 v[248:251], a[104:107], a[52:55], v[248:251]\n"
    "ds_read_b64_tr_b16 a[230:231], v31 offset:26624\n"
    "buffer_load_dwordx4 v[132:135], v1, s[8:11], s54 offen offset:2048 sc1\n"
    "v_mfma_f32_16x16x32_bf16 v[252:255], a[108:111], a[52:55], v[252:255]\n"
    "ds_read_b64_tr_b16 a[232:233], v32 offset:24576\n"
    "ds_read_b64_tr_b16 a[234:235], v32 offset:26624\n"
    "v_mfma_f32_16x16x32_bf16 v[224:227], a[112:115], a[56:59], v[224:227]\n"
    "ds_read_b64_tr_b16 a[236:237], v33 offset:24576\n"
    "v_mfma_f32_16x16x32_bf16 v[228:231], a[116:119], a[56:59], v[228:231]\n"
    "ds_read_b64_tr_b16 a[238:239], v33 offset:26624\n"
    "v_mfma_f32_16x16x32_bf16 v[232:235], a[120:123], a[56:59], v[232:235]\n"
    "ds_read_b64_tr_b16 a[240:241], v30 offset:28672\n"
    "buffer_load_dwordx4 v[136:139], v1, s[8:11], s92 offen offset:3072 sc1\n"
    "s_add_u32 s92, s92, 4096\n"
    "v_mfma_f32_16x16x32_bf16 v[236:239], a[124:127], a[56:59], v[236:239]\n"
    "ds_read_b64_tr_b16 a[242:243], v30 offset:30720\n"
    "ds_read_b64_tr_b16 a[244:245], v31 offset:28672\n"
    "v_mfma_f32_16x16x32_bf16 v[240:243], a[112:115], a[60:63], v[240:243]\n"
    "ds_read_b64_tr_b16 a[246:247], v31 offset:30720\n"
    "v_mfma_f32_16x16x32_bf16 v[244:247], a[116:119], a[60:63], v[244:247]\n"
    "ds_read_b64_tr_b16 a[248:249], v32 offset:28672\n"
    "v_mfma_f32_16x16x32_bf16 v[248:251], a[120:123], a[60:63], v[248:251]\n"
    "ds_read_b64_tr_b16 a[250:251], v32 offset:30720\n"
    "buffer_load_dwordx4 v[140:143], v1, s[8:11], s54 offen offset:3072 sc1\n"
    "s_add_u32 s54, s54, 4096\n"
    "v_mfma_f32_16x16x32_bf16 v[252:255], a[124:127], a[60:63], v[252:255]\n"
    "ds_read_b64_tr_b16 a[252:253], v33 offset:28672\n"
    "ds_read_b64_tr_b16 a[254:255], v33 offset:30720\n"
    "s_waitcnt vmcnt(8)\n"
    "s_barrier\n"
    "buffer_load_dwordx4 v[144:147], v1, s[8:11], s92 offen offset:0 sc1\n"
    "ds_read_b64_tr_b16 v[48:49], v30 offset:32768\n"
    "ds_read_b64_tr_b16 v[50:51], v30 offset:34816\n"
    "ds_read_b64_tr_b16 v[52:53], v31 offset:32768\n"
    "ds_read_b64_tr_b16 v[54:55], v31 offset:34816\n"
    "ds_read_b64_tr_b16 v[56:57], v32 offset:32768\n"
    "ds_read_b64_tr_b16 v[58:59], v32 offset:34816\n"
    "buffer_load_dwordx4 v[148:151], v1, s[8:11], s54 offen offset:0 sc1\n"
    "ds_read_b64_tr_b16 v[60:61], v33 offset:32768\n"
    "ds_read_b64_tr_b16 v[62:63], v33 offset:34816\n"
    "ds_read_b64_tr_b16 v[64:65], v30 offset:36864\n"
    "ds_read_b64_tr_b16 v[66:67], v30 offset:38912\n"
    "ds_read_b64_tr_b16 v[68:69], v31 offset:36864\n"
    "ds_read_b64_tr_b16 v[70:71], v31 offset:38912\n"
    "buffer_load_dwordx4 v[152:155], v1, s[8:11], s92 offen offset:1024 sc1\n"
    "ds_read_b64_tr_b16 v[72:73], v32 offset:36864\n"
    "ds_read_b64_tr_b16 v[74:75], v32 offset:38912\n"
    "ds_read_b64_tr_b16 v[76:77], v33 offset:36864\n"
    "ds_read_b64_tr_b16 v[78:79], v33 offset:38912\n"
    "ds_read_b64_tr_b16 v[80:81], v30 offset:40960\n"
    "ds_read_b64_tr_b16 v[82:83], v30 offset:43008\n"
    "buffer_load_dwordx4 v[156:159], v1, s[8:11], s54 offen offset:1024 sc1\n"
    "ds_read_b64_tr_b16 v[84:85], v31 offset:40960\n"
    "ds_read_b64_tr_b16 v[86:87], v31 offset:43008\n"
    "ds_read_b64_tr_b16 v[88:89], v32 offset:40960\n"
    "ds_read_b64_tr_b16 v[90:91], v32 offset:43008\n"
    "ds_read_b64_tr_b16 v[92:93], v33 offset:40960\n"
    "ds_read_b64_tr_b16 v[94:95], v33 offset:43008\n"
    "buffer_load_dwordx4 v[160:163], v1, s[8:11], s92 offen offset:2048 sc1\n"
    "ds_read_b64_tr_b16 v[96:97], v30 offset:45056\n"
    "ds_read_b64_tr_b16 v[98:99], v30 offset:47104\n"
    "ds_read_b64_tr_b16 v[100:101], v31 offset:45056\n"
    "ds_read_b64_tr_b16 v[102:103], v31 offset:47104\n"
    "ds_read_b64_tr_b16 v[104:105], v32 offset:45056\n"
    "ds_read_b64_tr_b16 v[106:107], v32 offset:47104\n"
    "buffer_load_dwordx4 v[164:167], v1, s[8:11], s54 offen offset:2048 sc1\n"
    "ds_read_b64_tr_b16 v[108:109], v33 offset:45056\n"
    "ds_read_b64_tr_b16 v[110:111], v33 offset:47104\n"
    "ds_read_b64_tr_b16 a[64:65], v30 offset:49152\n"
    "ds_read_b64_tr_b16 a[66:67], v30 offset:51200\n"
    "ds_read_b64_tr_b16 a[68:69], v31 offset:49152\n"
    "ds_read_b64_tr_b16 a[70:71], v31 offset:51200\n"
    "buffer_load_dwordx4 v[168:171], v1, s[8:11], s92 offen offset:3072 sc1\n"
    "s_add_u32 s92, s92, 4096\n"
    "ds_read_b64_tr_b16 a[72:73], v32 offset:49152\n"
    "ds_read_b64_tr_b16 a[74:75], v32 offset:51200\n"
    "ds_read_b64_tr_b16 a[76:77], v33 offset:49152\n"
    "ds_read_b64_tr_b16 a[78:79], v33 offset:51200\n"
    "ds_read_b64_tr_b16 a[80:81], v30 offset:53248\n"
    "ds_read_b64_tr_b16 a[82:83], v30 offset:55296\n"
    "buffer_load_dwordx4 v[172:175], v1, s[8:11], s54 offen offset:3072 sc1\n"
    "s_add_u32 s54, s54, 4096\n"
    "ds_read_b64_tr_b16 a[84:85], v31 offset:53248\n"
    "ds_read_b64_tr_b16 a[86:87], v31 offset:55296\n"
    "ds_read_b64_tr_b16 a[88:89], v32 offset:53248\n"
    "ds_read_b64_tr_b16 a[90:91], v32 offset:55296\n"
    "ds_read_b64_tr_b16 a[92:93], v33 offset:53248\n"
    "ds_read_b64_tr_b16 a[94:95], v33 offset:55296\n"
    "s_waitcnt vmcnt(15) lgkmcnt(0)\n"
    ST(53)
    "v_mfma_f32_16x16x32_bf16 v[224:227], a[128:131], v[112:115], v[224:227]\n"
    "v_mfma_f32_16x16x32_bf16 v[228:231], a[132:135], v[112:115], v[228:231]\n"
    "buffer_load_dwordx4 v[176:179], v1, s[8:11], s92 offen offset:0 sc1\n"
    "v_mfma_f32_16x16x32_bf16 v[232:235], a[136:139], v[112:115], v[232:235]\n"
    "v_mfma_f32_16x16x32_bf16 v[236:239], a[140:143], v[112:115], v[236:239]\n"
    "s_waitcnt vmcnt(15)\n"
    "v_mfma_f32_16x16x32_bf16 v[240:243], a[128:131], v[116:119], v[240:243]\n"
    "v_mfma_f32_16x16x32_bf16 v[244:247], a[132:135], v[116:119], v[244:247]\n"
    "buffer_load_dwordx4 v[180:183], v1, s[8:11], s54 offen offset:0 sc1\n"
    "v_mfma_f32_16x16x32_bf16 v[248:251], a[136:139], v[116:119], v[248:251]\n"
    "v_mfma_f32_16x16x32_bf16 v[252:255], a[140:143], v[116:119], v[252:255]\n"
    "s_waitcnt vmcnt(15)\n"
    "v_mfma_f32_16x16x32_bf16 v[224:227], a[144:147], v[120:123], v[224:227]\n"
    "v_mfma_f32_16x16x32_bf16 v[228:231], a[148:151], v[120:123], v[228:231]\n"
    "buffer_load_dwordx4 v[184:187], v1, s[8:11], s92 offen offset:1024 sc1\n"
    "v_mfma_f32_16x16x32_bf16 v[232:235], a[152:155], v[120:123], v[232:235]\n"
    "v_mfma_f32_16x16x32_bf16 v[236:239], a[156:159], v[120:123], v[236:239]\n"
    "s_waitcnt vmcnt(15)\n"
    "v_mfma_f32_16x16x32_bf16 v[240:243], a[144:147], v[124:127], v[240:243]\n"
    "v_mfma_f32_16x16x32_bf16 v[244:247], a[148:151], v[124:127], v[244:247]\n"
    "buffer_load_dwordx4 v[188:191], v1, s[8:11], s54 offen offset:1024 sc1\n"
    "v_mfma_f32_16x16x32_bf16 v[248:251], a[152:155], v[124:127], v[248:251]\n"
    "v_mfma_f32_16x16x32_bf16 v[252:255], a[156:159], v[124:127], v[252:255]\n"
    "s_waitcnt vmcnt(15)\n"
    "v_mfma_f32_16x16x32_bf16 v[224:227], a[160:163], v[128:131], v[224:227]\n"
    "v_mfma_f32_16x16x32_bf16 v[228:231], a[164:167], v[128:131], v[228:231]\n"
    "buffer_load_dwordx4 v[192:195], v1, s[8:11], s92 offen offset:2048 sc1\n"
    "v_mfma_f32_16x16x32_bf16 v[232:235], a[168:171], v[128:131], v[232:235]\n"
    "v_mfma_f32_16x16x32_bf16 v[236:239], a[172:175], v[128:131], v[236:239]\n"
    "s_waitcnt vmcnt(15)\n"
    "v_mfma_f32_16x16x32_bf16 v[240:243], a[160:163], v[132:135], v[240:243]\n"
    "v_mfma_f32_16x16x32_bf16 v[244:247], a[164:167], v[132:135], v[244:247]\n"
    "buffer_load_dwordx4 v[196:199], v1, s[8:11], s54 offen offset:2048 sc1\n"
    "v_mfma_f32_16x16x32_bf16 v[248:251], a[168:171], v[132:135], v[248:251]\n"
    "v_mfma_f32_16x16x32_bf16 v[252:255], a[172:175], v[132:135], v[252:255]\n"
    "s_waitcnt vmcnt(15)\n"
    "v_mfma_f32_16x16x32_bf16 v[224:227], a[176:179], v[136:139], v[224:227]\n"
    "v_mfma_f32_16x16x32_bf16 v[228:231], a[180:183], v[136:139], v[228:231]\n"
    "buffer_load_dwordx4 v[200:203], v1, s[8:11], s92 offen offset:3072 sc1\n"
    "s_add_u32 s92, s92, 4096\n"
    "v_mfma_f32_16x16x32_bf16 v[232:235], a[184:187], v[136:139], v[232:235]\n"
    "v_mfma_f32_16x16x32_bf16 v[236:239], a[188:191], v[136:139], v[236:239]\n"
    "s_waitcnt vmcnt(15)\n"
    "v_mfma_f32_16x16x32_bf16 v[240:243], a[176:179], v[140:143], v[240:243]\n"
    "v_mfma_f32_16x16x32_bf16 v[244:247], a[180:183], v[140:143], v[244:247]\n"
    "buffer_load_dwordx4 v[204:207], v1, s[8:11], s54 offen offset:3072 sc1\n"
    "s_add_u32 s54, s54, 4096\n"
    "v_mfma_f32_16x16x32_bf16 v[248:251], a[184:187], v[140:143], v[248:251]\n"
    "v_mfma_f32_16x16x32_bf16 v[252:255], a[188:191], v[140:143], v[252:255]\n"
    "s_waitcnt vmcnt(15)\n"
    "v_mfma_f32_16x16x32_bf16 v[224:227], a[192:195], v[144:147], v[224:227]\n"
    "v_mfma_f32_16x16x32_bf16 v[228:231], a[196:199], v[144:147], v[228:231]\n"
    "buffer_load_dwordx4 v[208:211], v1, s[8:11], s92 offen offset:0 sc1\n"
    "v_mfma_f32_16x16x32_bf16 v[232:235], a[200:203], v[144:147], v[232:235]\n"
    "v_mfma_f32_16x16x32_bf16 v[236:239], a[204:207], v[144:147], v[236:239]\n"
    "s_waitcnt vmcnt(15)\n"
    "v_mfma_f32_16x16x32_bf16 v[240:243], a[192:195], v[148:151], v[240:243]\n"
    "v_mfma_f32_16x16x32_bf16 v[244:247], a[196:199], v[148:151], v[244:247]\n"
    "buffer_load_dwordx4 v[212:215], v1, s[8:11], s54 offen offset:0 sc1\n"
    "v_mfma_f32_16x16x32_bf16 v[248:251], a[200:203], v[148:151], v[248:251]\n"
    "v_mfma_f32_16x16x32_bf16 v[252:255], a[204:207], v[148:151], v[252:255]\n"
    "s_waitcnt vmcnt(15)\n"
    "v_mfma_f32_16x16x32_bf16 v[224:227], a[208:211], v[152:155], v[224:227]\n"
    "v_mfma_f32_16x16x32_bf16 v[228:231], a[212:215], v[152:155], v[228:231]\n"
    "buffer_load_dwordx4 v[216:219], v1, s[8:11], s92 offen offset:1024 sc1\n"
    "v_mfma_f32_16x16x32_bf16 v[232:235], a[216:219], v[152:155], v[232:235]\n"
    "v_mfma_f32_16x16x32_bf16 v[236:239], a[220:223], v[152:155], v[236:239]\n"
    "s_waitcnt vmcnt(15)\n"
    "v_mfma_f32_16x16x32_bf16 v[240:243], a[208:211], v[156:159], v[240:243]\n"
    "v_mfma_f32_16x16x32_bf16 v[244:247], a[212:215], v[156:159], v[244:247]\n"
    "buffer_load_dwordx4 v[220:223], v1, s[8:11], s54 offen offset:1024 sc1\n"
    "v_mfma_f32_16x16x32_bf16 v[248:251], a[216:219], v[156:159], v[248:251]\n"
    "v_mfma_f32_16x16x32_bf16 v[252:255], a[220:223], v[156:159], v[252:255]\n"
    "s_waitcnt vmcnt(15)\n"
    "v_mfma_f32_16x16x32_bf16 v[224:227], a[224:227], v[160:163], v[224:227]\n"
    "v_mfma_f32_16x16x32_bf16 v[228:231], a[228:231], v[160:163], v[228:231]\n"
    "v_mfma_f32_16x16x32_bf16 v[232:235], a[232:235], v[160:163], v[232:235]\n"
    "v_mfma_f32_16x16x32_bf16 v[236:239], a[236:239], v[160:163], v[236:239]\n"
    "s_waitcnt vmcnt(14)\n"
    "v_mfma_f32_16x16x32_bf16 v[240:243], a[224:227], v[164:167], v[240:243]\n"
    "v_mfma_f32_16x16x32_bf16 v[244:247], a[228:231], v[164:167], v[244:247]\n"
    "v_mfma_f32_16x16x32_bf16 v[248:251], a[232:235], v[164:167], v[248:251]\n"
    "v_mfma_f32_16x16x32_bf16 v[252:255], a[236:239], v[164:167], v[252:255]\n"
    "s_waitcnt vmcnt(13)\n"
    "v_mfma_f32_16x16x32_bf16 v[224:227], a[240:243], v[168:171], v[224:227]\n"
    "v_mfma_f32_16x16x32_bf16 v[228:231], a[244:247], v[168:171], v[228:231]\n"
    "v_mfma_f32_16x16x32_bf16 v[232:235], a[248:251], v[168:171], v[232:235]\n"
    "v_mfma_f32_16x16x32_bf16 v[236:239], a[252:255], v[168:171], v[236:239]\n"
    "s_waitcnt vmcnt(12)\n"
    "v_mfma_f32_16x16x32_bf16 v[240:243], a[240:243], v[172:175], v[240:243]\n"
    "v_mfma_f32_16x16x32_bf16 v[244:247], a[244:247], v[172:175], v[244:247]\n"
    "v_mfma_f32_16x16x32_bf16 v[248:251], a[248:251], v[172:175], v[248:251]\n"
    "v_mfma_f32_16x16x32_bf16 v[252:255], a[252:255], v[172:175], v[252:255]\n"
    "s_waitcnt vmcnt(11)\n"
    "v_mfma_f32_16x16x32_bf16 v[224:227], v[48:51], v[176:179], v[224:227]\n"
    "v_mfma_f32_16x16x32_bf16 v[228:231], v[52:55], v[176:179], v[228:231]\n"
    "v_mfma_f32_16x16x32_bf16 v[232:235], v[56:59], v[176:179], v[232:235]\n"
    "v_mfma_f32_16x16x32_bf16 v[236:239], v[60:63], v[176:179], v[236:239]\n"
    "s_waitcnt vmcnt(10)\n"
    "v_mfma_f32_16x16x32_bf16 v[240:243], v[48:51], v[180:183], v[240:243]\n"
    "v_mfma_f32_16x16x32_bf16 v[244:247], v[52:55], v[180:183], v[244:247]\n"
    "v_mfma_f32_16x16x32_bf16 v[248:251], v[56:59], v[180:183], v[248:251]\n"
    "v_mfma_f32_16x16x32_bf16 v[252:255], v[60:63], v[180:183], v[252:255]\n"
    "s_waitcnt vmcnt(9)\n"
    "v_mfma_f32_16x16x32_bf16 v[224:227], v[64:67], v[184:187], v[224:227]\n"
    "v_mfma_f32_16x16x32_bf16 v[228:231], v[68:71], v[184:187], v[228:231]\n"
    "v_mfma_f32_16x16x32_bf16 v[232:235], v[72:75], v[184:187], v[232:235]\n"
    "v_mfma_f32_16x16x32_bf16 v[236:239], v[76:79], v[184:187], v[236:239]\n"
    "s_waitcnt vmcnt(8)\n"
    "v_mfma_f32_16x16x32_bf16 v[240:243], v[64:67], v[188:191], v[240:243]\n"
    "v_mfma_f32_16x16x32_bf16 v[244:247], v[68:71], v[188:191], v[244:247]\n"
    "v_mfma_f32_16x16x32_bf16 v[248:251], v[72:75], v[188:191], v[248:251]\n"
    "v_mfma_f32_16x16x32_bf16 v[252:255], v[76:79], v[188:191], v[252:255]\n"
    "s_waitcnt vmcnt(7)\n"
    "v_mfma_f32_16x16x32_bf16 v[224:227], v[80:83], v[192:195], v[224:227]\n"
    "v_mfma_f32_16x16x32_bf16 v[228:231], v[84:87], v[192:195], v[228:231]\n"
    "v_mfma_f32_16x16x32_bf16 v[232:235], v[88:91], v[192:195], v[232:235]\n"
    "v_mfma_f32_16x16x32_bf16 v[236:239], v[92:95], v[192:195], v[236:239]\n"
    "s_waitcnt vmcnt(6)\n"
    "v_mfma_f32_16x16x32_bf16 v[240:243], v[80:83], v[196:199], v[240:243]\n"
    "v_mfma_f32_16x16x32_bf16 v[244:247], v[84:87], v[196:199], v[244:247]\n"
    "v_mfma_f32_16x16x32_bf16 v[248:251], v[88:91], v[196:199], v[248:251]\n"
    "v_mfma_f32_16x16x32_bf16 v[252:255], v[92:95], v[196:199], v[252:255]\n"
    ST(54)
    "s_movk_i32 s53, 0x4000\n"
    "s_waitcnt vmcnt(4)\n"
    "v_mfma_f32_16x16x32_bf16 v[240:243], v[96:99], v[204:207], v[240:243]\n"
    "v_mfma_f32_16x16x32_bf16 v[244:247], v[100:103], v[204:207], v[244:247]\n"
    "v_mfma_f32_16x16x32_bf16 v[248:251], v[104:107], v[204:207], v[248:251]\n"
    "v_mfma_f32_16x16x32_bf16 v[252:255], v[108:111], v[204:207], v[252:255]\n"
    "s_waitcnt vmcnt(2)\n"
    "v_mfma_f32_16x16x32_bf16 v[240:243], a[64:67], v[212:215], v[240:243]\n"
    "v_mfma_f32_16x16x32_bf16 v[244:247], a[68:71], v[212:215], v[244:247]\n"
    "v_mfma_f32_16x16x32_bf16 v[248:251], a[72:75], v[212:215], v[248:251]\n"
    "v_mfma_f32_16x16x32_bf16 v[252:255], a[76:79], v[212:215], v[252:255]\n"
    "s_waitcnt vmcnt(0)\n"
    "v_mfma_f32_16x16x32_bf16 v[240:243], a[80:83], v[220:223], v[240:243]\n"
    "v_mfma_f32_16x16x32_bf16 v[244:247], a[84:87], v[220:223], v[244:247]\n"
    "v_mfma_f32_16x16x32_bf16 v[248:251], a[88:91], v[220:223], v[248:251]\n"
    "v_mfma_f32_16x16x32_bf16 v[252:255], a[92:95], v[220:223], v[252:255]\n"
    "v_mfma_f32_16x16x32_bf16 v[224:227], v[96:99], v[200:203], v[224:227]\n"
    "v_permlane16_swap_b32 v40, v42\n"
    "v_permlane16_swap_b32 v41, v43\n"
    "v_mfma_f32_16x16x32_bf16 v[228:231], v[100:103], v[200:203], v[228:231]\n"
    "v_permlane16_swap_b32 v44, v46\n"
    "v_permlane16_swap_b32 v45, v47\n"
    "v_mfma_f32_16x16x32_bf16 v[232:235], v[104:107], v[200:203], v[232:235]\n"
    "v_lshlrev_b32_e64 v128, 16, v40\n"
    "v_and_b32 v129, 0xffff0000, v40\n"
    "v_mfma_f32_16x16x32_bf16 v[236:239], v[108:111], v[200:203], v[236:239]\n"
    "v_lshlrev_b32_e64 v130, 16, v41\n"
    "v_and_b32 v131, 0xffff0000, v41\n"
    "v_mfma_f32_16x16x32_bf16 v[224:227], a[64:67], v[208:211], v[224:227]\n"
    "v_lshlrev_b32_e64 v132, 16, v42\n"
    "v_and_b32 v133, 0xffff0000, v42\n"
    "ds_write_b128 v35, v[240:243] offset:0\n"
    "v_mfma_f32_16x16x32_bf16 v[228:231], a[68:71], v[208:211], v[228:231]\n"
    "v_lshlrev_b32_e64 v134, 16, v43\n"
    "v_and_b32 v135, 0xffff0000, v43\n"
    "v_mfma_f32_16x16x32_bf16 v[232:235], a[72:75], v[208:211], v[232:235]\n"
    "v_lshlrev_b32_e64 v136, 16, v44\n"
    "v_and_b32 v137, 0xffff0000, v44\n"
    "ds_write_b128 v35, v[244:247] offset:1024\n"
    "v_mfma_f32_16x16x32_bf16 v[236:239], a[76:79], v[208:211], v[236:239]\n"
    "v_lshlrev_b32_e64 v138, 16, v45\n"
    "v_and_b32 v139, 0xffff0000, v45\n"
    "v_mfma_f32_16x16x32_bf16 v[224:227], a[80:83], v[216:219], v[224:227]\n"
    "v_lshlrev_b32_e64 v140, 16, v46\n"
    "v_and_b32 v141, 0xffff0000, v46\n"
    "ds_write_b128 v35, v[248:251] offset:2048\n"
    "v_mfma_f32_16x16x32_bf16 v[228:231], a[84:87], v[216:219], v[228:231]\n"
    "v_lshlrev_b32_e64 v142, 16, v47\n"
    "v_and_b32 v143, 0xffff0000, v47\n"
    "v_mfma_f32_16x16x32_bf16 v[232:235], a[88:91], v[216:219], v[232:235]\n"
    "ds_write_b128 v35, v[252:255] offset:3072\n"
    "v_mfma_f32_16x16x32_bf16 v[236:239], a[92:95], v[216:219], v[236:239]\n"
    "s_waitcnt lgkmcnt(0)\n"
    "s_barrier\n"
    "ds_read_b128 v[160:163], v34 offset:0\n"
    "ds_read_b128 v[164:167], v34 offset:1024\n"
    "ds_read_b128 v[168:171], v34 offset:2048\n"
    "ds_read_b128 v[172:175], v34 offset:3072\n"
    ST(59)
    "v_pk_add_f32 v[128:129], v[128:129], v[224:225]\n"
    "v_pk_add_f32 v[130:131], v[130:131], v[226:227]\n"
    "v_pk_add_f32 v[132:133], v[132:133], v[228:229]\n"
    "v_pk_add_f32 v[134:135], v[134:135], v[230:231]\n"
    "v_pk_add_f32 v[136:137], v[136:137], v[232:233]\n"
    "v_pk_add_f32 v[138:139], v[138:139], v[234:235]\n"
    "v_pk_add_f32 v[140:141], v[140:141], v[236:237]\n"
    "v_pk_add_f32 v[142:143], v[142:143], v[238:239]\n"
    "s_waitcnt lgkmcnt(2)\n"
    "s_nop 0\n"
    "v_pk_add_f32 v[128:129], v[128:129], v[160:161]\n"
    "v_pk_add_f32 v[130:131], v[130:131], v[162:163]\n"
    "v_pk_add_f32 v[132:133], v[132:133], v[164:165]\n"
    "v_pk_add_f32 v[134:135], v[134:135], v[166:167]\n"
    "v_cvt_pk_bf16_f32 v144, v128, v129\n"
    "v_cvt_pk_bf16_f32 v145, v130, v131\n"
    "v_cvt_pk_bf16_f32 v146, v132, v133\n"
    "v_cvt_pk_bf16_f32 v147, v134, v135\n"
    "s_nop 0\n"
    "v_permlane16_swap_b32 v144, v146\n"
    "v_permlane16_swap_b32 v145, v147\n"
    "s_waitcnt lgkmcnt(0)\n"
    "v_pk_add_f32 v[136:137], v[136:137], v[168:169]\n"
    "v_pk_add_f32 v[138:139], v[138:139], v[170:171]\n"
    "v_pk_add_f32 v[140:141], v[140:141], v[172:173]\n"
    "v_pk_add_f32 v[142:143], v[142:143], v[174:175]\n"
    "v_cvt_pk_bf16_f32 v148, v136, v137\n"
    "v_cvt_pk_bf16_f32 v149, v138, v139\n"
    "v_cvt_pk_bf16_f32 v150, v140, v141\n"
    "v_cvt_pk_bf16_f32 v151, v142, v143\n"
    "s_nop 0\n"
    "v_permlane16_swap_b32 v148, v150\n"
    "v_permlane16_swap_b32 v149, v151\n"
    "v_mov_b32 v152, v144\n"
    "v_mov_b32 v153, v145\n"
    "v_mov_b32 v154, v146\n"
    "v_mov_b32_e64 v155, v147\n"
    "v_mov_b32_dpp v144, v148 row_ror:8 row_mask:0xf bank_mask:0xc\n"
    "v_mov_b32_dpp v145, v149 row_ror:8 row_mask:0xf bank_mask:0xc\n"
    "v_mov_b32_dpp v146, v150 row_ror:8 row_mask:0xf bank_mask:0xc\n"
    "v_mov_b32_dpp v147, v151 row_ror:8 row_mask:0xf bank_mask:0xc\n"
    "v_mov_b32_dpp v148, v152 row_ror:8 row_mask:0xf bank_mask:0x3\n"
    "v_mov_b32_dpp v149, v153 row_ror:8 row_mask:0xf bank_mask:0x3\n"
    "v_mov_b32_dpp v150, v154 row_ror:8 row_mask:0xf bank_mask:0x3\n"
    "v_mov_b32_dpp v151, v155 row_ror:8 row_mask:0xf bank_mask:0x3\n"
    "buffer_store_dwordx4 v[144:147], v36, s[36:39], 0 offen sc0 sc1\n"
    "buffer_store_dwordx4 v[148:151], v36, s[36:39], s53 offen sc0 sc1\n"
    "s_or_b32 s67, s2, s45\n"
    "s_cmp_lg_u32 s67, 0\n"
    "s_cbranch_scc1 Lend_36_%=\n"
    "buffer_store_dword v13, off, s[8:11], 0 sc1\n"
    "s_branch Lend_36_%=\n"
    "Lslow_31_%=:\n"
    "s_mov_b32 s66, 0x10000\n"
    "Lloop_33_%=:\n"
    "buffer_load_dword v56, v17, s[8:11], 0 offen sc1\n"
    "s_waitcnt vmcnt(0)\n"
    "v_cmp_ne_u32 vcc, s46, v56\n"
    "s_cbranch_vccz Lback_32_%=\n"
    "s_sub_u32 s66, s66, 1\n"
    "s_cbranch_scc0 Lloop_33_%=\n"
    "s_branch Lback_32_%=\n"
    "Lend_36_%=:\n"
    ST_END
    : "+{v0}"(tid), "+{s[0:1]}"(ka) : "{s2}"(blockIdx.x) : "memory", "scc", "vcc", "v1","v2","v3","v4","v5","v6","v7","v8","v9","v10","v11","v12","v13","v14","v15","v16","v17","v18","v19","v20","v21","v22","v23","v24","v25","v26","v27","v28","v29","v30","v31","v32","v33","v34","v35","v36","v37","v38","v39","v40","v41","v42","v43","v44","v45","v46","v47","v48","v49","v50","v51","v52","v53","v54","v55","v56","v57","v58","v59","v60","v61","v62","v63","v64","v65","v66","v67","v68","v69","v70","v71","v72","v73","v74","v75","v76","v77","v78","v79","v80","v81","v82","v83","v84","v85","v86","v87","v88","v89","v90","v91","v92","v93","v94","v95","v96","v97","v98","v99","v100","v101","v102","v103","v104","v105","v106","v107","v108","v109","v110","v111","v112","v113","v114","v115","v116","v117","v118","v119","v120","v121","v122","v123","v124","v125","v126","v127","v128","v129","v130","v131","v132","v133","v134","v135","v136","v137","v138","v139","v140","v141","v142","v143","v144","v145","v146","v147","v148","v149","v150","v151","v152","v153","v154","v155","v156","v157","v158","v159","v160","v161","v162","v163","v164","v165","v166","v167","v168","v169","v170","v171","v172","v173","v174","v175","v176","v177","v178","v179","v180","v181","v182","v183","v184","v185","v186","v187","v188","v189","v190","v191","v192","v193","v194","v195","v196","v197","v198","v199","v200","v201","v202","v203","v204","v205","v206","v207","v208","v209","v210","v211","v212","v213","v214","v215","v216","v217","v218","v219","v220","v221","v222","v223","v224","v225","v226","v227","v228","v229","v230","v231","v232","v233","v234","v235","v236","v237","v238","v239","v240","v241","v242","v243","v244","v245","v246","v247","v248","v249","v250","v251","v252","v253","v254","v255","a0","a1","a2","a3","a4","a5","a6","a7","a8","a9","a10","a11","a12","a13","a14","a15","a16","a17","a18","a19","a20","a21","a22","a23","a24","a25","a26","a27","a28","a29","a30","a31","a32","a33","a34","a35","a36","a37","a38","a39","a40","a41","a42","a43","a44","a45","a46","a47","a48","a49","a50","a51","a52","a53","a54","a55","a56","a57","a58","a59","a60","a61","a62","a63","a64","a65","a66","a67","a68","a69","a70","a71","a72","a73","a74","a75","a76","a77","a78","a79","a80","a81","a82","a83","a84","a85","a86","a87","a88","a89","a90","a91","a92","a93","a94","a95","a96","a97","a98","a99","a100","a101","a102","a103","a104","a105","a106","a107","a108","a109","a110","a111","a112","a113","a114","a115","a116","a117","a118","a119","a120","a121","a122","a123","a124","a125","a126","a127","a128","a129","a130","a131","a132","a133","a134","a135","a136","a137","a138","a139","a140","a141","a142","a143","a144","a145","a146","a147","a148","a149","a150","a151","a152","a153","a154","a155","a156","a157","a158","a159","a160","a161","a162","a163","a164","a165","a166","a167","a168","a169","a170","a171","a172","a173","a174","a175","a176","a177","a178","a179","a180","a181","a182","a183","a184","a185","a186","a187","a188","a189","a190","a191","a192","a193","a194","a195","a196","a197","a198","a199","a200","a201","a202","a203","a204","a205","a206","a207","a208","a209","a210","a211","a212","a213","a214","a215","a216","a217","a218","a219","a220","a221","a222","a223","a224","a225","a226","a227","a228","a229","a230","a231","a232","a233","a234","a235","a236","a237","a238","a239","a240","a241","a242","a243","a244","a245","a246","a247","a248","a249","a250","a251","a252","a253","a254","a255","s3","s4","s5","s6","s7","s8","s9","s10","s11","s12","s13","s14","s15","s16","s17","s18","s19","s20","s21","s22","s23","s24","s25","s26","s27","s28","s29","s30","s31","s33","s34","s35","s36","s37","s38","s39","s40","s41","s42","s43","s44","s45","s46","s47","s48","s49","s50","s51","s52","s53","s54","s55","s56","s57","s58","s59","s60","s61","s62","s63","s64","s65","s66","s67","s68","s69","s70","s71","s72","s73","s74","s75","s76","s77","s78","s79","s80","s81","s82","s83","s84","s85","s86","s87","s88","s89","s90","s91","s92","s93","s94","s95","s96","s97","s98","s99");
}
}

extern "C" void kernel_launch(void* const* d_in, const int* in_sizes, int n_in, void* d_out, int out_size, void* d_ws, size_t ws_size, hipStream_t stream) {
  (void)in_sizes; (void)n_in; (void)out_size; (void)ws_size;
  (void)hipFuncSetAttribute((const void*)mk::fused_kernel, hipFuncAttributeMaxDynamicSharedMemorySize, mk::LDS_BYTES);
  mk::fused_kernel<<<256, 256, mk::LDS_BYTES, stream>>>((const uint16_t*)d_in[0], (const uint16_t*)d_in[1], (const uint16_t*)d_in[2], (const uint16_t*)d_in[4],
      (const uint16_t*)d_in[5], (const uint16_t*)d_in[6], (const uint16_t*)d_in[7], (uint16_t*)d_out, (unsigned char*)d_ws);
}
```
